```python
import torch

def _softmax(x, axis=-1):
    return torch.softmax(x, dim=axis)

def _gelu(x):
    return torch.nn.functional.gelu(x, approximate='tanh')

def _silu(x):
    return torch.nn.functional.silu(x)

def _sigmoid(x):
    return torch.sigmoid(x)

def _layernorm(x, axis=-1, eps=1e-05):
    m = x.mean(dim=axis, keepdim=True)
    v = x.var(dim=axis, keepdim=True, unbiased=False)
    return (x - m) / torch.sqrt(v + eps)

def _scatter_add(tgt, idx, src, axis=0):
    return tgt.clone().index_add_(axis, idx, src)

def _segment_sum(src, seg, n):
    out = torch.zeros((n,) + src.shape[1:], dtype=src.dtype)
    return out.index_add_(0, seg, src)

def _pad(x, pw):
    flat = tuple((v for pair in reversed(pw) for v in pair))
    return torch.nn.functional.pad(x, flat)
kernel_name = 'parallel_sum__memory_aug_recurrent_mem_tr__mixer_channel_mlp__prompt'

def setup_inputs():

    _w = lambda a: (lambda t: t.to(torch.bfloat16) if t.is_floating_point() else t)(torch.as_tensor(a))

    def _A_setup():
        B, S, Dm = (1, 2048, 1024)
        H, Dh = (32, 128)
        M, seg = (8, 32)
        seg = min(seg, S)
        S = S // seg * seg
        return {'x': _w((lambda *s: torch.randn(*s, dtype=torch.bfloat16) * 0.05)(B, S, Dm)), 'mem0': _w((lambda *s: torch.randn(*s, dtype=torch.bfloat16) * 0.2)(B, M, Dm)), 'Wq': _w((lambda *s: torch.randn(*s, dtype=torch.bfloat16) * 1.6)(Dm, H, Dh) * Dm ** (-0.5)), 'Wk': _w((lambda *s: torch.randn(*s, dtype=torch.bfloat16) * 1.6)(Dm, H, Dh) * Dm ** (-0.5)), 'Wv': _w((lambda *s: torch.randn(*s, dtype=torch.bfloat16) * 0.2)(Dm, H, Dh) * Dm ** (-0.5)), 'Wo': _w((lambda *s: torch.randn(*s, dtype=torch.bfloat16) * 0.2)(H, Dh, Dm) * (H * Dh) ** (-0.5)), 'seg_len': seg}
    _A_in = _A_setup()

    def _B_setup():
        B, S, D, F = (1, 2048, 1024, 3584)
        return {'x': _w((lambda *s: torch.randn(*s, dtype=torch.bfloat16) * 0.05)(B, S, D)), 'w1': _w((lambda *s: torch.randn(*s, dtype=torch.bfloat16) * 1.6)(D, F) * D ** (-0.5)), 'b1': _w((lambda *s: torch.randn(*s, dtype=torch.bfloat16) * 0.4)(F)), 'w2': _w((lambda *s: torch.randn(*s, dtype=torch.bfloat16) * 0.05)(F, D) * F ** (-0.5)), 'b2': _w((lambda *s: torch.randn(*s, dtype=torch.bfloat16) * 0.05)(D))}
    _B_in = _B_setup()
    return {'A_x': _A_in['x'], 'A_mem0': _A_in['mem0'], 'A_Wq': _A_in['Wq'], 'A_Wk': _A_in['Wk'], 'A_Wv': _A_in['Wv'], 'A_Wo': _A_in['Wo'], 'A_seg_len': _A_in['seg_len'], 'B_x': _B_in['x'], 'B_w1': _B_in['w1'], 'B_b1': _B_in['b1'], 'B_w2': _B_in['w2'], 'B_b2': _B_in['b2']}

def reference(A_x, A_mem0, A_Wq, A_Wk, A_Wv, A_Wo, A_seg_len, B_x, B_w1, B_b1, B_w2, B_b2):

    def _A_ref(x, mem0, Wq, Wk, Wv, Wo, seg_len):
        B, S, Dm = x.shape
        H, Dh = (Wq.shape[1], Wq.shape[2])
        M = mem0.shape[1]
        scale = Dh ** (-0.5)
        n_seg = S // seg_len
        L = M + seg_len
        idx = torch.arange(L)
        mask = (idx[:, None] < M) | (idx[:, None] >= idx[None, :])
        mem = mem0
        outs = []
        for s in range(n_seg):
            xs = x[:, s * seg_len:(s + 1) * seg_len, :].clone()
            h = torch.concatenate([mem, xs], axis=1)
            q = torch.einsum('btd,dhe->bhte', h, Wq)
            k = torch.einsum('btd,dhe->bhte', h, Wk)
            v = torch.einsum('btd,dhe->bhte', h, Wv)
            scores = torch.einsum('bhqe,bhke->bhqk', q, k) * scale
            scores = torch.where(mask[None, None], scores, -1000000000.0)
            p = _softmax(scores, axis=-1)
            o = torch.einsum('bhqk,bhke->bhqe', p, v)
            o = torch.einsum('bhte,hed->btd', o, Wo)
            mem = o[:, :M, :].clone()
            outs.append(o[:, M:, :])
        return torch.concatenate(outs, axis=1)

    def _B_ref(x, w1, b1, w2, b2):
        y = _layernorm(x, axis=-1)
        h = _gelu(torch.einsum('bsd,df->bsf', y, w1) + b1)
        return torch.einsum('bsf,fd->bsd', h, w2) + b2
    x = A_x
    xn = _layernorm(x, axis=-1)
    return x + _A_ref(xn, A_mem0, A_Wq, A_Wk, A_Wv, A_Wo, A_seg_len) + _B_ref(xn, B_w1, B_b1, B_w2, B_b2)
```

```cpp
#include <hip/hip_runtime.h>
#include <hip/hip_bf16.h>
#include <stdint.h>

constexpr int S_LEN = 2048, DM = 1024, NH = 32, DH = 128, HD = NH * DH;
constexpr int MEMN = 8, SEG = 32, NSEG = S_LEN / SEG;
constexpr int FF = 3584;

typedef unsigned v4u __attribute__((ext_vector_type(4)));
typedef unsigned v2u __attribute__((ext_vector_type(2)));
typedef float f32x4 __attribute__((ext_vector_type(4)));
typedef float f32x2 __attribute__((ext_vector_type(2)));
typedef short bf16x8 __attribute__((ext_vector_type(8)));
typedef short s16x4 __attribute__((ext_vector_type(4)));
typedef __bf16 bfx2 __attribute__((ext_vector_type(2)));
typedef unsigned short u16;
#define LAS __attribute__((address_space(3)))
#define GAS __attribute__((address_space(1)))

constexpr size_t WS_XN = 0;
constexpr size_t WS_Y = WS_XN + (size_t)S_LEN * DM * 2;
constexpr size_t WS_Q = WS_Y + (size_t)S_LEN * DM * 2;
constexpr size_t WS_K = WS_Q + (size_t)S_LEN * HD * 2;
constexpr size_t WS_VT = WS_K + (size_t)S_LEN * HD * 2;
constexpr size_t WS_H = WS_VT + (size_t)S_LEN * HD * 2;
constexpr size_t WS_BO = WS_H + (size_t)S_LEN * FF * 2;
constexpr size_t WS_OT = WS_BO + (size_t)S_LEN * DM * 2;
constexpr size_t WS_VM = WS_OT + (size_t)S_LEN * HD * 2;
constexpr size_t WS_HOP = WS_VM + (size_t)NSEG * HD * MEMN * 2;
constexpr size_t HOP_MEM = 0;
constexpr size_t HOP_QK = HOP_MEM + (size_t)NSEG * 16384;
constexpr size_t HOP_OB = HOP_QK + (size_t)NSEG * NH * 4096;
constexpr size_t HOP_BYTES = HOP_OB + (size_t)NSEG * 65536;
constexpr size_t WS_ERR = WS_HOP + HOP_BYTES;
constexpr size_t WS_TOTAL = WS_ERR + 256;

constexpr unsigned SENT = 0xFFFFFFFFu;
constexpr unsigned SPINCAP = 1u << 20;

__device__ __forceinline__ float bf2f(unsigned b16) { return __builtin_bit_cast(float, b16 << 16); }
__device__ __forceinline__ float bflo(unsigned x) { return __builtin_bit_cast(float, x << 16); }
__device__ __forceinline__ float bfhi(unsigned x) { return __builtin_bit_cast(float, x & 0xFFFF0000u); }
__device__ __forceinline__ unsigned pack2(float a, float b) {
  f32x2 v = {a, b};
  bfx2 r = __builtin_convertvector(v, bfx2);
  return __builtin_bit_cast(unsigned, r);
}
__device__ __forceinline__ float rbf(float a) { return (float)(__bf16)a; }
__device__ __forceinline__ unsigned nosent(unsigned x) { return x == SENT ? 0xFFFFFFFEu : x; }
__device__ __forceinline__ f32x4 mfma16(v4u a, v4u b, f32x4 c) {
  return __builtin_amdgcn_mfma_f32_16x16x32_bf16(__builtin_bit_cast(bf16x8, a), __builtin_bit_cast(bf16x8, b), c, 0, 0, 0);
}
__device__ __forceinline__ v2u trread(LAS const char* p) {
  typedef short v4i16_t __attribute__((ext_vector_type(4)));
  return __builtin_bit_cast(v2u, __builtin_amdgcn_ds_read_tr16_b64_v4i16((LAS v4i16_t*)p));
}
__device__ __forceinline__ float ror(float x, int) = delete;
template <int N> __device__ __forceinline__ float row_ror(float x) {
  return __builtin_bit_cast(float, __builtin_amdgcn_update_dpp(0, __builtin_bit_cast(int, x), 0x120 + N, 0xf, 0xf, false));
}

__device__ __forceinline__ float wave_sum(float v) {
#pragma unroll
  for (int o = 32; o > 0; o >>= 1) v += __shfl_xor(v, o);
  return v;
}
__device__ __forceinline__ void ln16(float (&v)[16]) {
  float s = 0.f;
#pragma unroll
  for (int i = 0; i < 16; ++i) s += v[i];
  s = wave_sum(s);
  const float mean = s * (1.0f / DM);
  float q = 0.f;
#pragma unroll
  for (int i = 0; i < 16; ++i) { float d = v[i] - mean; q += d * d; }
  q = wave_sum(q);
  const float mb = rbf(mean);
  const float vb = rbf(q * (1.0f / DM));
  const float den = rbf(sqrtf(rbf(vb + 1e-5f)));
#pragma unroll
  for (int i = 0; i < 16; ++i) v[i] = rbf(rbf(v[i] - mb) / den);
}
__global__ __launch_bounds__(256) void k_ln(const u16* __restrict__ x, unsigned char* __restrict__ ws) {
  const int tid = threadIdx.x, lane = tid & 63, w = tid >> 6;
  const int row = blockIdx.x * 4 + w;
  {
    v4u s = {SENT, SENT, SENT, SENT};
    v4u* p = (v4u*)(ws + WS_HOP);
    const size_t n = HOP_BYTES / 16;
    for (size_t i = (size_t)blockIdx.x * 256 + tid; i < n; i += (size_t)gridDim.x * 256) p[i] = s;
    if (blockIdx.x == 0 && tid < 64) ((unsigned*)(ws + WS_ERR))[tid] = 0u;
  }
  const v4u* xr = (const v4u*)(x + (size_t)row * DM);
  v4u a = xr[lane], b = xr[64 + lane];
  float v[16];
  v[0] = bflo(a.x); v[1] = bfhi(a.x); v[2] = bflo(a.y); v[3] = bfhi(a.y);
  v[4] = bflo(a.z); v[5] = bfhi(a.z); v[6] = bflo(a.w); v[7] = bfhi(a.w);
  v[8] = bflo(b.x); v[9] = bfhi(b.x); v[10] = bflo(b.y); v[11] = bfhi(b.y);
  v[12] = bflo(b.z); v[13] = bfhi(b.z); v[14] = bflo(b.w); v[15] = bfhi(b.w);
  ln16(v);
  v4u* xn = (v4u*)(ws + WS_XN + (size_t)row * DM * 2);
  xn[lane] = v4u{pack2(v[0], v[1]), pack2(v[2], v[3]), pack2(v[4], v[5]), pack2(v[6], v[7])};
  xn[64 + lane] = v4u{pack2(v[8], v[9]), pack2(v[10], v[11]), pack2(v[12], v[13]), pack2(v[14], v[15])};
  ln16(v);
  v4u* y = (v4u*)(ws + WS_Y + (size_t)row * DM * 2);
  y[lane] = v4u{pack2(v[0], v[1]), pack2(v[2], v[3]), pack2(v[4], v[5]), pack2(v[6], v[7])};
  y[64 + lane] = v4u{pack2(v[8], v[9]), pack2(v[10], v[11]), pack2(v[12], v[13]), pack2(v[14], v[15])};
}

template <int BM> struct GemmCfg {
  static constexpr int BN = 128, BK = 64;
  static constexpr int A_BYTES = BM * BK * 2, B_BYTES = BK * BN * 2, ST_BYTES = A_BYTES + B_BYTES;
  static constexpr int LDS_BYTES = 3 * ST_BYTES;
};

template <int BM, int WM, int WN, bool TOUT, class Epi>
__device__ __forceinline__ void gemm_tile(const u16* __restrict__ A, int lda, const u16* __restrict__ B, int ldb, int K,
                                          int brow, int bcol, LAS char* lds, Epi epi) {
  using C = GemmCfg<BM>;
  constexpr int TM = BM / WM / 16, TN = C::BN / WN / 16;
  constexpr int AI = C::A_BYTES / 8192, BI = C::B_BYTES / 8192;
  const int tid = threadIdx.x, lane = tid & 63, w = __builtin_amdgcn_readfirstlane(tid >> 6);
  const int wr = w / WN, wc = w % WN, fr = lane & 15, fq = lane >> 4;
  const u16* asrc[AI];
  const u16* bsrc[BI];
#pragma unroll
  for (int i = 0; i < AI; ++i) {
    const int ii = w * AI + i, row = ii * 8 + (lane >> 3), ch = (lane & 7) ^ (row & 7);
    asrc[i] = A + (size_t)(brow + row) * lda + ch * 8;
  }
#pragma unroll
  for (int i = 0; i < BI; ++i) {
    const int ii = w * BI + i, row = ii * 4 + (lane >> 4), ch = (lane & 15) ^ (((row & 3) << 2) | ((row >> 2) & 3));
    bsrc[i] = B + (size_t)row * ldb + bcol + ch * 8;
  }
  auto stage = [&](int t, int buf) {
    LAS char* base = lds + buf * C::ST_BYTES;
#pragma unroll
    for (int i = 0; i < AI; ++i)
      __builtin_amdgcn_global_load_lds((const GAS void*)(asrc[i] + t * 64), (LAS void*)(base + (w * AI + i) * 1024), 16, 0, 0);
#pragma unroll
    for (int i = 0; i < BI; ++i)
      __builtin_amdgcn_global_load_lds((const GAS void*)(bsrc[i] + (size_t)t * 64 * ldb), (LAS void*)(base + C::A_BYTES + (w * BI + i) * 1024), 16, 0, 0);
  };
  int aoff[TM][2], boff[TN][2][2];
#pragma unroll
  for (int m = 0; m < TM; ++m)
#pragma unroll
    for (int k = 0; k < 2; ++k) {
      const int row = wr * (TM * 16) + m * 16 + fr, ch = k * 4 + fq;
      aoff[m][k] = row * 128 + ((ch ^ (row & 7)) * 16);
    }
  {
    const int q = fr >> 2, p = lane & 3;
#pragma unroll
    for (int n = 0; n < TN; ++n)
#pragma unroll
      for (int k = 0; k < 2; ++k)
#pragma unroll
        for (int t = 0; t < 2; ++t) {
          const int row = 32 * k + 8 * fq + 4 * t + q, c = wc * TN + n, ch = 2 * c + (p >> 1);
          boff[n][k][t] = C::A_BYTES + 256 * row + 16 * (ch ^ (((row & 3) << 2) | ((row >> 2) & 3))) + 8 * (p & 1);
        }
  }
  f32x4 acc[TM][TN];
#pragma unroll
  for (int m = 0; m < TM; ++m)
#pragma unroll
    for (int n = 0; n < TN; ++n) acc[m][n] = f32x4{0.f, 0.f, 0.f, 0.f};
  const int nt = K / 64;
  stage(0, 0);
  stage(1, 1);
  int rb = 0, sb = 2;
  for (int t = 0; t < nt; ++t) {
    if (t + 1 < nt) {
      if constexpr (AI + BI == 3) asm volatile("s_waitcnt vmcnt(3)" ::: "memory");
      else if constexpr (AI + BI == 4) asm volatile("s_waitcnt vmcnt(4)" ::: "memory");
      else asm volatile("s_waitcnt vmcnt(6)" ::: "memory");
    } else {
      asm volatile("s_waitcnt vmcnt(0)" ::: "memory");
    }
    asm volatile("s_waitcnt lgkmcnt(0)" ::: "memory");
    __builtin_amdgcn_s_barrier();
    asm volatile("" ::: "memory");
    if (t + 2 < nt) stage(t + 2, sb);
    LAS const char* base = lds + rb * C::ST_BYTES;
    v4u af[TM][2], bfr[TN][2];
#pragma unroll
    for (int k = 0; k < 2; ++k) {
#pragma unroll
      for (int n = 0; n < TN; ++n) {
        v2u lo = trread(base + boff[n][k][0]), hi = trread(base + boff[n][k][1]);
        bfr[n][k] = v4u{lo.x, lo.y, hi.x, hi.y};
      }
#pragma unroll
      for (int m = 0; m < TM; ++m) af[m][k] = *(LAS const v4u*)(base + aoff[m][k]);
    }
    __builtin_amdgcn_s_setprio(1);
#pragma unroll
    for (int k = 0; k < 2; ++k)
#pragma unroll
      for (int m = 0; m < TM; ++m)
#pragma unroll
        for (int n = 0; n < TN; ++n)
          acc[m][n] = TOUT ? mfma16(af[m][k], bfr[n][k], acc[m][n]) : mfma16(bfr[n][k], af[m][k], acc[m][n]);
    __builtin_amdgcn_s_setprio(0);
    rb = rb == 2 ? 0 : rb + 1;
    sb = sb == 2 ? 0 : sb + 1;
  }
#pragma unroll
  for (int m = 0; m < TM; ++m)
#pragma unroll
    for (int n = 0; n < TN; ++n) {
      const int r0 = brow + wr * (TM * 16) + m * 16, c0 = bcol + wc * (TN * 16) + n * 16;
      if (TOUT) epi(r0 + fq * 4, c0 + fr, acc[m][n]);
      else epi(r0 + fr, c0 + fq * 4, acc[m][n]);
    }
  asm volatile("s_waitcnt lgkmcnt(0)" ::: "memory");
  __builtin_amdgcn_s_barrier();
}

__device__ __forceinline__ float gelu_tanh(float x) {
  const float u = 0.7978845608028654f * (x + 0.044715f * x * x * x);
  const float e = __expf(2.0f * u);
  const float t = 1.0f - 2.0f / (e + 1.0f);
  return 0.5f * x * (1.0f + t);
}

template <int BM, int WM, int WN>
__device__ __forceinline__ void task_qkv(int rt, int ct, const u16* Wq, const u16* Wk, const u16* Wv, unsigned char* ws, LAS char* lds) {
  const u16* xn = (const u16*)(ws + WS_XN);
  const int which = ct >> 5, bcol = (ct & 31) * 128, brow = rt * BM;
  if (which < 2) {
    u16* out = (u16*)(ws + (which == 0 ? WS_Q : WS_K));
    gemm_tile<BM, WM, WN, false>(xn, DM, which == 0 ? Wq : Wk, HD, DM, brow, bcol, lds, [&](int r, int c, f32x4 v) {
      *(v2u*)(out + (size_t)r * HD + c) = v2u{pack2(v[0], v[1]), pack2(v[2], v[3])};
    });
  } else {
    u16* vt = (u16*)(ws + WS_VT);
    gemm_tile<BM, WM, WN, true>(xn, DM, Wv, HD, DM, brow, bcol, lds, [&](int r, int c, f32x4 v) {
      const int seg = r >> 5, t = r & 31, p = 8 * ((t & 15) >> 2) + 4 * (t >> 4);
      *(v2u*)(vt + ((size_t)seg * HD + c) * 32 + p) = v2u{pack2(v[0], v[1]), pack2(v[2], v[3])};
    });
  }
}
template <int BM, int WM, int WN>
__device__ __forceinline__ void task_mlp1(int rt, int ct, const u16* w1, const u16* b1, unsigned char* ws, LAS char* lds) {
  const u16* y = (const u16*)(ws + WS_Y);
  u16* h = (u16*)(ws + WS_H);
  gemm_tile<BM, WM, WN, false>(y, DM, w1, FF, DM, rt * BM, ct * 128, lds, [&](int r, int c, f32x4 v) {
    const v2u bb = *(const v2u*)(b1 + c);
    const float o0 = gelu_tanh(rbf(rbf(v[0]) + bflo(bb.x))), o1 = gelu_tanh(rbf(rbf(v[1]) + bfhi(bb.x)));
    const float o2 = gelu_tanh(rbf(rbf(v[2]) + bflo(bb.y))), o3 = gelu_tanh(rbf(rbf(v[3]) + bfhi(bb.y)));
    *(v2u*)(h + (size_t)r * FF + c) = v2u{pack2(o0, o1), pack2(o2, o3)};
  });
}
template <int BM, int WM, int WN>
__device__ __forceinline__ void task_mlp2(int rt, int ct, const u16* w2, const u16* b2, unsigned char* ws, LAS char* lds) {
  const u16* h = (const u16*)(ws + WS_H);
  u16* bo = (u16*)(ws + WS_BO);
  gemm_tile<BM, WM, WN, false>(h, FF, w2, DM, FF, rt * BM, ct * 128, lds, [&](int r, int c, f32x4 v) {
    const v2u bb = *(const v2u*)(b2 + c);
    *(v2u*)(bo + (size_t)r * DM + c) = v2u{pack2(rbf(v[0]) + bflo(bb.x), rbf(v[1]) + bfhi(bb.x)), pack2(rbf(v[2]) + bflo(bb.y), rbf(v[3]) + bfhi(bb.y))};
  });
}
template <int BM, int WM, int WN>
__device__ __forceinline__ void task_wo(int rt, int ct, const u16* Wo, const u16* x, u16* out, unsigned char* ws, LAS char* lds) {
  const u16* ot = (const u16*)(ws + WS_OT);
  const u16* bo = (const u16*)(ws + WS_BO);
  gemm_tile<BM, WM, WN, false>(ot, HD, Wo, DM, HD, rt * BM, ct * 128, lds, [&](int r, int c, f32x4 v) {
    const v2u xx = *(const v2u*)(x + (size_t)r * DM + c);
    const v2u bb = *(const v2u*)(bo + (size_t)r * DM + c);
    const float o0 = rbf(bflo(xx.x) + rbf(v[0])) + bflo(bb.x), o1 = rbf(bfhi(xx.x) + rbf(v[1])) + bfhi(bb.x);
    const float o2 = rbf(bflo(xx.y) + rbf(v[2])) + bflo(bb.y), o3 = rbf(bfhi(xx.y) + rbf(v[3])) + bfhi(bb.y);
    *(v2u*)(out + (size_t)r * DM + c) = v2u{pack2(o0, o1), pack2(o2, o3)};
  });
}

__global__ __launch_bounds__(512, 2) void k_qkv(const u16* Wq, const u16* Wk, const u16* Wv, unsigned char* ws) {
  extern __shared__ __attribute__((aligned(16))) char smem[];
  const int b = blockIdx.x;
  task_qkv<256, 4, 2>(b & 7, b >> 3, Wq, Wk, Wv, ws, (LAS char*)smem);
}
__global__ __launch_bounds__(512, 2) void k_mlp1(const u16* w1, const u16* b1, unsigned char* ws) {
  extern __shared__ __attribute__((aligned(16))) char smem[];
  const int b = blockIdx.x;
  task_mlp1<256, 4, 2>(b & 7, b >> 3, w1, b1, ws, (LAS char*)smem);
}
__global__ __launch_bounds__(512, 2) void k_mlp2(const u16* w2, const u16* b2, unsigned char* ws) {
  extern __shared__ __attribute__((aligned(16))) char smem[];
  const int b = blockIdx.x;
  task_mlp2<64, 2, 4>(b & 31, b >> 5, w2, b2, ws, (LAS char*)smem);
}
__global__ __launch_bounds__(512, 2) void k_wo(const u16* Wo, const u16* x, u16* out, unsigned char* ws) {
  extern __shared__ __attribute__((aligned(16))) char smem[];
  const int b = blockIdx.x;
  task_wo<64, 2, 4>(b & 31, b >> 5, Wo, x, out, ws, (LAS char*)smem);
}

__device__ __forceinline__ bool rdy(v4u v) { return max(max(v.x, v.y), max(v.z, v.w)) != SENT; }
__device__ __forceinline__ v4u ld_sc1(__amdgpu_buffer_rsrc_t rs, int off) { return __builtin_amdgcn_raw_buffer_load_b128(rs, off, 0, 16); }

template <int N>
__device__ __forceinline__ void poll(__amdgpu_buffer_rsrc_t rs, int off, v4u (&d)[N], unsigned* err) {
  unsigned spins = 0;
  for (;;) {
    bool ok = true;
#pragma unroll
    for (int i = 0; i < N; ++i) d[i] = ld_sc1(rs, off + i * 1024);
    asm volatile("" ::: "memory");
#pragma unroll
    for (int i = 0; i < N; ++i) ok = ok && rdy(d[i]);
    if (__all(ok)) break;
    if (++spins > SPINCAP) {
      if ((threadIdx.x & 63) == 0) __hip_atomic_store(err, 1u, __ATOMIC_RELAXED, __HIP_MEMORY_SCOPE_AGENT);
      break;
    }
    if ((spins & 1023u) == 0u && __hip_atomic_load(err, __ATOMIC_RELAXED, __HIP_MEMORY_SCOPE_AGENT) != 0u) break;
  }
}

constexpr int CH_RED = 0;
constexpr int CH_VM = 24576;
constexpr int CH_RED2 = 25088;
constexpr int CH_STG = 27136;
constexpr int CH_LDS = CH_STG + 8 * 16384;

__device__ __forceinline__ void chain_role(int c, const u16* __restrict__ mem0, const u16* __restrict__ Wq, const u16* __restrict__ Wk,
                                           const u16* __restrict__ Wv, const u16* __restrict__ Wo, unsigned char* ws, LAS char* lds) {
  const int tid = threadIdx.x, lane = tid & 63, w = __builtin_amdgcn_readfirstlane(tid >> 6);
  const int fr = lane & 15, g = lane >> 4;
  const int h = c >> 2, q4 = c & 3, mh = c & 1, nb = c >> 1;
  unsigned* err = (unsigned*)(ws + WS_ERR);
  const __amdgpu_buffer_rsrc_t rs = __builtin_amdgcn_make_buffer_rsrc((void*)(ws + WS_HOP), 0, (int)HOP_BYTES, 0x00020000);
  v4u wq[6][4], wo[16];
  {
    LAS char* stg = lds + CH_STG + w * 16384;
    const int q = fr >> 2, p = lane & 3;
#pragma unroll
    for (int mat = 0; mat < 3; ++mat) {
      const u16* W = mat == 0 ? Wq : (mat == 1 ? Wk : Wv);
#pragma unroll
      for (int i = 0; i < 8; ++i) {
        const int row = i * 16 + (lane >> 2), ch = lane & 3;
        v4u v = *(const v4u*)(W + (size_t)(128 * w + row) * HD + h * 128 + 32 * q4 + ch * 8);
        *(LAS v4u*)(stg + row * 64 + ch * 16) = v;
      }
      __builtin_amdgcn_s_waitcnt(0xC07F);
#pragma unroll
      for (int dt = 0; dt < 2; ++dt)
#pragma unroll
        for (int ks = 0; ks < 4; ++ks) {
          v2u lo = trread(stg + (32 * ks + 8 * g + q) * 64 + (16 * dt + 4 * p) * 2);
          v2u hi = trread(stg + (32 * ks + 8 * g + 4 + q) * 64 + (16 * dt + 4 * p) * 2);
          wq[mat * 2 + dt][ks] = v4u{lo.x, lo.y, hi.x, hi.y};
        }
      __builtin_amdgcn_s_waitcnt(0xC07F);
    }
#pragma unroll
    for (int i = 0; i < 16; ++i) {
      const int row = i * 32 + (lane >> 1), ch = lane & 1;
      v4u v = *(const v4u*)(Wo + (size_t)(512 * w + row) * DM + 16 * nb + ch * 8);
      *(LAS v4u*)(stg + row * 32 + ch * 16) = v;
    }
    __builtin_amdgcn_s_waitcnt(0xC07F);
#pragma unroll
    for (int ks = 0; ks < 16; ++ks) {
      v2u lo = trread(stg + (32 * ks + 8 * g + q) * 32 + (4 * p) * 2);
      v2u hi = trread(stg + (32 * ks + 8 * g + 4 + q) * 32 + (4 * p) * 2);
      wo[ks] = v4u{lo.x, lo.y, hi.x, hi.y};
    }
  }
  const u16* Kx = (const u16*)(ws + WS_K);
  const u16* Vt = (const u16*)(ws + WS_VT);
  u16* VM = (u16*)(ws + WS_VM);
  const float scale = 0.08838834764831845f;
  LAS f32x4* red = (LAS f32x4*)(lds + CH_RED);
  LAS f32x4* red2 = (LAS f32x4*)(lds + CH_RED2);
  LAS u16* vml = (LAS u16*)(lds + CH_VM);

  for (int s = 0; s < NSEG; ++s) {
    v4u mf[2];
    if (s == 0) {
#pragma unroll
      for (int p = 0; p < 2; ++p)
        mf[p] = *(const v4u*)(mem0 + (size_t)(fr & 7) * DM + 128 * w + 32 * (2 * p + (fr >> 3)) + 8 * g);
    } else {
      poll<2>(rs, (int)(HOP_MEM + (size_t)s * 16384) + ((16 * w + 4 * (fr >> 3) + g) * 8 + (fr & 7)) * 16, mf, err);
    }
#pragma unroll
    for (int t6 = 0; t6 < 6; ++t6) {
      f32x4 a0 = {0.f, 0.f, 0.f, 0.f}, a1 = {0.f, 0.f, 0.f, 0.f};
      a0 = mfma16(wq[t6][0], mf[0], a0);
      a1 = mfma16(wq[t6][1], mf[0], a1);
      a0 = mfma16(wq[t6][2], mf[1], a0);
      a1 = mfma16(wq[t6][3], mf[1], a1);
      f32x4 x = (fr & 8) ? a1 : a0;
#pragma unroll
      for (int r = 0; r < 4; ++r) x[r] += row_ror<8>(x[r]);
      if (fr < 8) red[(w * 6 + t6) * 32 + g * 8 + fr] = x;
    }
    __syncthreads();
    if (tid < 192) {
      const int t6 = tid >> 5, gg = (tid >> 3) & 3, m = tid & 7;
      f32x4 x = red[t6 * 32 + (tid & 31)];
#pragma unroll
      for (int ww = 1; ww < 8; ++ww) x += red[(ww * 6 + t6) * 32 + (tid & 31)];
      const unsigned lo = pack2(x[0], x[1]), hi = pack2(x[2], x[3]);
      const int mat = t6 >> 1, dt = t6 & 1;
      if (mat < 2) {
        const int off = (int)(HOP_QK + ((size_t)s * NH + h) * 4096) + ((q4 * 2 + mat) * 8 + m) * 64 + (16 * dt + 4 * gg) * 2;
        __builtin_amdgcn_raw_buffer_store_b64(v2u{nosent(lo), nosent(hi)}, rs, off, 0, 16);
      } else {
        const int d = 16 * dt + 4 * gg;
        vml[(d + 0) * 8 + m] = (u16)(lo & 0xFFFF);
        vml[(d + 1) * 8 + m] = (u16)(lo >> 16);
        vml[(d + 2) * 8 + m] = (u16)(hi & 0xFFFF);
        vml[(d + 3) * 8 + m] = (u16)(hi >> 16);
      }
    }
    __syncthreads();
    if (tid < 32) {
      *(v4u*)(VM + (((size_t)s * NH + h) * 128 + 32 * q4 + tid) * 8) = *(LAS v4u*)(vml + tid * 8);
    }
    if (s == NSEG - 1) break;
    if (w == 0) {
      v4u kx[2][4], vt[2];
#pragma unroll
      for (int t = 0; t < 2; ++t)
#pragma unroll
        for (int j = 0; j < 4; ++j) kx[t][j] = *(const v4u*)(Kx + (size_t)(32 * s + 16 * t + fr) * HD + h * 128 + 32 * j + 8 * g);
#pragma unroll
      for (int dt = 0; dt < 2; ++dt) vt[dt] = *(const v4u*)(Vt + (((size_t)s * NH + h) * 128 + 32 * q4 + 16 * dt + fr) * 32 + 8 * g);
      v4u X[4];
      poll<4>(rs, (int)(HOP_QK + ((size_t)s * NH + h) * 4096) + fr * 64 + g * 16, X, err);
      f32x4 s0 = {0.f, 0.f, 0.f, 0.f}, s1 = s0, s2 = s0;
#pragma unroll
      for (int j = 0; j < 4; ++j) {
        s0 = mfma16(X[j], X[j], s0);
        s1 = mfma16(kx[0][j], X[j], s1);
        s2 = mfma16(kx[1][j], X[j], s2);
      }
      float v[12];
#pragma unroll
      for (int r = 0; r < 4; ++r) {
        v[r] = rbf(rbf(s0[r]) * scale);
        v[4 + r] = rbf(rbf(s1[r]) * scale);
        v[8 + r] = rbf(rbf(s2[r]) * scale);
      }
      float mx = fmaxf(fmaxf(fmaxf(v[4], v[5]), fmaxf(v[6], v[7])), fmaxf(fmaxf(v[8], v[9]), fmaxf(v[10], v[11])));
      if (g >= 2) mx = fmaxf(mx, fmaxf(fmaxf(v[0], v[1]), fmaxf(v[2], v[3])));
      mx = fmaxf(mx, __shfl_xor(mx, 16));
      mx = fmaxf(mx, __shfl_xor(mx, 32));
      float sum = 0.f;
#pragma unroll
      for (int i = 0; i < 12; ++i) {
        v[i] = __expf(v[i] - mx);
        if (i < 4 && g < 2) v[i] = 0.f;
        sum += v[i];
      }
      sum += __shfl_xor(sum, 16);
      sum += __shfl_xor(sum, 32);
      const float inv = 1.0f / sum;
      v4u pt, pm;
      pt.x = pack2(v[4] * inv, v[5] * inv); pt.y = pack2(v[6] * inv, v[7] * inv);
      pt.z = pack2(v[8] * inv, v[9] * inv); pt.w = pack2(v[10] * inv, v[11] * inv);
      pm.x = pack2(v[0] * inv, v[1] * inv); pm.y = pack2(v[2] * inv, v[3] * inv); pm.z = 0u; pm.w = 0u;
#pragma unroll
      for (int dt = 0; dt < 2; ++dt) {
        v4u vmf = {0u, 0u, 0u, 0u};
        if (g >= 2) {
          v2u t = *(LAS v2u*)(vml + (16 * dt + fr) * 8 + 4 * (g - 2));
          vmf.x = t.x; vmf.y = t.y;
        }
        f32x4 o = {0.f, 0.f, 0.f, 0.f};
        o = mfma16(vt[dt], pt, o);
        o = mfma16(vmf, pm, o);
        if (fr < 8) {
          const int k8 = h * 16 + 4 * q4 + 2 * dt + (g >> 1);
          const int off = (int)(HOP_OB + (size_t)s * 65536) + (((fr >> 2) * 512 + k8) * 4 + (fr & 3)) * 16 + (g & 1) * 8;
          __builtin_amdgcn_raw_buffer_store_b64(v2u{nosent(pack2(o[0], o[1])), nosent(pack2(o[2], o[3]))}, rs, off, 0, 16);
        }
      }
    }
    {
      v4u of[4];
      poll<4>(rs, (int)(HOP_OB + (size_t)s * 65536) + ((mh * 512 + 64 * w + 4 * (fr >> 2) + g) * 4 + (fr & 3)) * 16, of, err);
      f32x4 a[4];
#pragma unroll
      for (int r4 = 0; r4 < 4; ++r4) {
        a[r4] = f32x4{0.f, 0.f, 0.f, 0.f};
#pragma unroll
        for (int i = 0; i < 4; ++i) a[r4] = mfma16(wo[4 * i + r4], of[i], a[r4]);
      }
      const int r4 = fr >> 2;
      f32x4 x = r4 == 0 ? a[0] : (r4 == 1 ? a[1] : (r4 == 2 ? a[2] : a[3]));
#pragma unroll
      for (int r = 0; r < 4; ++r) { x[r] += row_ror<8>(x[r]); x[r] += row_ror<4>(x[r]); }
      if (fr < 4) red2[w * 16 + g * 4 + fr] = x;
    }
    __syncthreads();
    if (tid < 16) {
      f32x4 x = red2[tid];
#pragma unroll
      for (int ww = 1; ww < 8; ++ww) x += red2[ww * 16 + tid];
      const int gg = tid >> 2, m4 = tid & 3;
      const int k8 = 2 * nb + (gg >> 1);
      const int off = (int)(HOP_MEM + (size_t)(s + 1) * 16384) + (k8 * 8 + 4 * mh + m4) * 16 + (gg & 1) * 8;
      __builtin_amdgcn_raw_buffer_store_b64(v2u{nosent(pack2(x[0], x[1])), nosent(pack2(x[2], x[3]))}, rs, off, 0, 16);
    }
  }
}

__global__ __launch_bounds__(512, 2) void k_chain(const u16* mem0, const u16* Wq, const u16* Wk, const u16* Wv, const u16* Wo, unsigned char* ws) {
  extern __shared__ __attribute__((aligned(16))) char smem[];
  const int b = blockIdx.x;
  const int xcd = b & 7, i = b >> 3;
  const int c = ((xcd * 4 + (i >> 2)) << 2) | (i & 3);
  chain_role(c, mem0, Wq, Wk, Wv, Wo, ws, (LAS char*)smem);
}

__device__ __forceinline__ void attn_unit(int s, int h, int lane, unsigned char* ws) {
  const int fr = lane & 15, g = lane >> 4;
  const u16* Q = (const u16*)(ws + WS_Q);
  const u16* Kx = (const u16*)(ws + WS_K);
  const u16* Vt = (const u16*)(ws + WS_VT);
  const u16* VM = (const u16*)(ws + WS_VM);
  const u16* QK = (const u16*)(ws + WS_HOP + HOP_QK + ((size_t)s * NH + h) * 4096);
  u16* OT = (u16*)(ws + WS_OT);
  const float scale = 0.08838834764831845f;
  f32x4 sc[3][2];
#pragma unroll
  for (int a = 0; a < 3; ++a)
#pragma unroll
    for (int b = 0; b < 2; ++b) sc[a][b] = f32x4{0.f, 0.f, 0.f, 0.f};
#pragma unroll
  for (int j = 0; j < 4; ++j) {
    v4u qf[2], kf[3];
#pragma unroll
    for (int t = 0; t < 2; ++t) {
      qf[t] = *(const v4u*)(Q + (size_t)(32 * s + 16 * t + fr) * HD + h * 128 + 32 * j + 8 * g);
      kf[1 + t] = *(const v4u*)(Kx + (size_t)(32 * s + 16 * t + fr) * HD + h * 128 + 32 * j + 8 * g);
    }
    kf[0] = *(const v4u*)(QK + ((j * 2 + 1) * 8 + (fr & 7)) * 32 + g * 8);
#pragma unroll
    for (int a = 0; a < 3; ++a)
#pragma unroll
      for (int b = 0; b < 2; ++b) sc[a][b] = mfma16(kf[a], qf[b], sc[a][b]);
  }
  v4u pt[2], pm[2];
#pragma unroll
  for (int b = 0; b < 2; ++b) {
    const int qi = 16 * b + fr;
    float v[12];
    bool ok[12];
#pragma unroll
    for (int r = 0; r < 4; ++r) {
      v[r] = rbf(rbf(sc[0][b][r]) * scale); ok[r] = g < 2;
      v[4 + r] = rbf(rbf(sc[1][b][r]) * scale); ok[4 + r] = (4 * g + r) <= qi;
      v[8 + r] = rbf(rbf(sc[2][b][r]) * scale); ok[8 + r] = (16 + 4 * g + r) <= qi;
    }
    float mx = -3.0e38f;
#pragma unroll
    for (int i = 0; i < 12; ++i) mx = ok[i] ? fmaxf(mx, v[i]) : mx;
    mx = fmaxf(mx, __shfl_xor(mx, 16));
    mx = fmaxf(mx, __shfl_xor(mx, 32));
    float sum = 0.f;
#pragma unroll
    for (int i = 0; i < 12; ++i) { v[i] = ok[i] ? __expf(v[i] - mx) : 0.f; sum += v[i]; }
    sum += __shfl_xor(sum, 16);
    sum += __shfl_xor(sum, 32);
    const float inv = 1.0f / sum;
    pt[b].x = pack2(v[4] * inv, v[5] * inv); pt[b].y = pack2(v[6] * inv, v[7] * inv);
    pt[b].z = pack2(v[8] * inv, v[9] * inv); pt[b].w = pack2(v[10] * inv, v[11] * inv);
    pm[b].x = pack2(v[0] * inv, v[1] * inv); pm[b].y = pack2(v[2] * inv, v[3] * inv); pm[b].z = 0u; pm[b].w = 0u;
  }
#pragma unroll
  for (int dt = 0; dt < 8; ++dt) {
    v4u vtf = *(const v4u*)(Vt + (((size_t)s * NH + h) * 128 + 16 * dt + fr) * 32 + 8 * g);
    v4u vmf = {0u, 0u, 0u, 0u};
    if (g < 2) {
      v2u t = *(const v2u*)(VM + (((size_t)s * NH + h) * 128 + 16 * dt + fr) * 8 + 4 * g);
      vmf.x = t.x; vmf.y = t.y;
    }
#pragma unroll
    for (int b = 0; b < 2; ++b) {
      f32x4 o = {0.f, 0.f, 0.f, 0.f};
      o = mfma16(vtf, pt[b], o);
      o = mfma16(vmf, pm[b], o);
      *(v2u*)(OT + (size_t)(32 * s + 16 * b + fr) * HD + h * 128 + 16 * dt + 4 * g) = v2u{pack2(o[0], o[1]), pack2(o[2], o[3])};
    }
  }
}
__global__ __launch_bounds__(256) void k_attn(unsigned char* ws) {
  const int u = blockIdx.x * 4 + (threadIdx.x >> 6);
  attn_unit(u >> 5, u & 31, threadIdx.x & 63, ws);
}

extern "C" void kernel_launch(void* const* d_in, const int* in_sizes, int n_in, void* d_out, int out_size, void* d_ws, size_t ws_size,
                              hipStream_t stream) {
  const u16* x = (const u16*)d_in[0];
  const u16* mem0 = (const u16*)d_in[1];
  const u16* Wq = (const u16*)d_in[2];
  const u16* Wk = (const u16*)d_in[3];
  const u16* Wv = (const u16*)d_in[4];
  const u16* Wo = (const u16*)d_in[5];
  const u16* w1 = (const u16*)d_in[8];
  const u16* b1 = (const u16*)d_in[9];
  const u16* w2 = (const u16*)d_in[10];
  const u16* b2 = (const u16*)d_in[11];
  unsigned char* ws = (unsigned char*)d_ws;
  u16* out = (u16*)d_out;
  constexpr int L256 = GemmCfg<256>::LDS_BYTES, L64 = GemmCfg<64>::LDS_BYTES;
  (void)hipFuncSetAttribute((const void*)k_qkv, hipFuncAttributeMaxDynamicSharedMemorySize, L256);
  (void)hipFuncSetAttribute((const void*)k_mlp1, hipFuncAttributeMaxDynamicSharedMemorySize, L256);
  (void)hipFuncSetAttribute((const void*)k_mlp2, hipFuncAttributeMaxDynamicSharedMemorySize, L64);
  (void)hipFuncSetAttribute((const void*)k_wo, hipFuncAttributeMaxDynamicSharedMemorySize, L64);
  (void)hipFuncSetAttribute((const void*)k_chain, hipFuncAttributeMaxDynamicSharedMemorySize, CH_LDS);
  k_ln<<<S_LEN / 4, 256, 0, stream>>>(x, ws);
  k_qkv<<<8 * 96, 512, L256, stream>>>(Wq, Wk, Wv, ws);
  k_mlp1<<<8 * 28, 512, L256, stream>>>(w1, b1, ws);
  k_mlp2<<<32 * 8, 512, L64, stream>>>(w2, b2, ws);
  k_chain<<<128, 512, CH_LDS, stream>>>(mem0, Wq, Wk, Wv, Wo, ws);
  k_attn<<<NSEG * NH / 4, 256, 0, stream>>>(ws);
  k_wo<<<32 * 8, 512, L64, stream>>>(Wo, x, out, ws);
}
```

```cpp
#include <hip/hip_runtime.h>
#include <hip/hip_bf16.h>
#include <stdint.h>

constexpr int S_LEN = 2048, DM = 1024, NH = 32, DH = 128, HD = NH * DH;
constexpr int MEMN = 8, SEG = 32, NSEG = S_LEN / SEG;
constexpr int FF = 3584;

typedef unsigned v4u __attribute__((ext_vector_type(4)));
typedef unsigned v2u __attribute__((ext_vector_type(2)));
typedef float f32x4 __attribute__((ext_vector_type(4)));
typedef float f32x2 __attribute__((ext_vector_type(2)));
typedef short bf16x8 __attribute__((ext_vector_type(8)));
typedef short s16x4 __attribute__((ext_vector_type(4)));
typedef __bf16 bfx2 __attribute__((ext_vector_type(2)));
typedef unsigned short u16;
#define LAS __attribute__((address_space(3)))
#define GAS __attribute__((address_space(1)))

constexpr size_t WS_XN = 0;
constexpr size_t WS_Y = WS_XN + (size_t)S_LEN * DM * 2;
constexpr size_t WS_Q = WS_Y + (size_t)S_LEN * DM * 2;
constexpr size_t WS_K = WS_Q + (size_t)S_LEN * HD * 2;
constexpr size_t WS_VT = WS_K + (size_t)S_LEN * HD * 2;
constexpr size_t WS_H = WS_VT + (size_t)S_LEN * HD * 2;
constexpr size_t WS_BO = WS_H + (size_t)S_LEN * FF * 2;
constexpr size_t WS_OT = WS_BO + (size_t)S_LEN * DM * 2;
constexpr size_t WS_VM = WS_OT + (size_t)S_LEN * HD * 2;
constexpr size_t WS_HOP = WS_VM + (size_t)NSEG * HD * MEMN * 2;
constexpr size_t HOP_MEM = 0;
constexpr size_t HOP_QK = HOP_MEM + (size_t)NSEG * 16384;
constexpr size_t HOP_OB = HOP_QK + (size_t)NSEG * NH * 4096;
constexpr size_t HOP_BYTES = HOP_OB + (size_t)NSEG * 65536;
constexpr size_t WS_ERR = WS_HOP + HOP_BYTES;
constexpr size_t WS_TOTAL = WS_ERR + 256;

constexpr unsigned SENT = 0xFFFFFFFFu;
constexpr unsigned SPINCAP = 1u << 20;

__device__ __forceinline__ float bf2f(unsigned b16) { return __builtin_bit_cast(float, b16 << 16); }
__device__ __forceinline__ float bflo(unsigned x) { return __builtin_bit_cast(float, x << 16); }
__device__ __forceinline__ float bfhi(unsigned x) { return __builtin_bit_cast(float, x & 0xFFFF0000u); }
__device__ __forceinline__ unsigned pack2(float a, float b) {
  f32x2 v = {a, b};
  bfx2 r = __builtin_convertvector(v, bfx2);
  return __builtin_bit_cast(unsigned, r);
}
__device__ __forceinline__ float rbf(float a) { return (float)(__bf16)a; }
__device__ __forceinline__ unsigned nosent(unsigned x) { return x == SENT ? 0xFFFFFFFEu : x; }
__device__ __forceinline__ f32x4 mfma16(v4u a, v4u b, f32x4 c) {
  return __builtin_amdgcn_mfma_f32_16x16x32_bf16(__builtin_bit_cast(bf16x8, a), __builtin_bit_cast(bf16x8, b), c, 0, 0, 0);
}
__device__ __forceinline__ v2u trread(LAS const char* p) {
  typedef short v4i16_t __attribute__((ext_vector_type(4)));
  return __builtin_bit_cast(v2u, __builtin_amdgcn_ds_read_tr16_b64_v4i16((LAS v4i16_t*)p));
}
__device__ __forceinline__ float ror(float x, int) = delete;
template <int N> __device__ __forceinline__ float row_ror(float x) {
  return __builtin_bit_cast(float, __builtin_amdgcn_update_dpp(0, __builtin_bit_cast(int, x), 0x120 + N, 0xf, 0xf, false));
}

__device__ __forceinline__ float wave_sum(float v) {
#pragma unroll
  for (int o = 32; o > 0; o >>= 1) v += __shfl_xor(v, o);
  return v;
}
__device__ __forceinline__ void ln16(float (&v)[16]) {
  float s = 0.f;
#pragma unroll
  for (int i = 0; i < 16; ++i) s += v[i];
  s = wave_sum(s);
  const float mean = s * (1.0f / DM);
  float q = 0.f;
#pragma unroll
  for (int i = 0; i < 16; ++i) { float d = v[i] - mean; q += d * d; }
  q = wave_sum(q);
  const float mb = rbf(mean);
  const float vb = rbf(q * (1.0f / DM));
  const float den = rbf(sqrtf(rbf(vb + 1e-5f)));
#pragma unroll
  for (int i = 0; i < 16; ++i) v[i] = rbf(rbf(v[i] - mb) / den);
}
__global__ __launch_bounds__(256) void k_ln(const u16* __restrict__ x, unsigned char* __restrict__ ws) {
  const int tid = threadIdx.x, lane = tid & 63, w = tid >> 6;
  const int row = blockIdx.x * 4 + w;
  {
    v4u s = {SENT, SENT, SENT, SENT};
    v4u* p = (v4u*)(ws + WS_HOP);
    const size_t n = HOP_BYTES / 16;
    for (size_t i = (size_t)blockIdx.x * 256 + tid; i < n; i += (size_t)gridDim.x * 256) p[i] = s;
    if (blockIdx.x == 0 && tid < 64) ((unsigned*)(ws + WS_ERR))[tid] = 0u;
  }
  const v4u* xr = (const v4u*)(x + (size_t)row * DM);
  v4u a = xr[lane], b = xr[64 + lane];
  float v[16];
  v[0] = bflo(a.x); v[1] = bfhi(a.x); v[2] = bflo(a.y); v[3] = bfhi(a.y);
  v[4] = bflo(a.z); v[5] = bfhi(a.z); v[6] = bflo(a.w); v[7] = bfhi(a.w);
  v[8] = bflo(b.x); v[9] = bfhi(b.x); v[10] = bflo(b.y); v[11] = bfhi(b.y);
  v[12] = bflo(b.z); v[13] = bfhi(b.z); v[14] = bflo(b.w); v[15] = bfhi(b.w);
  ln16(v);
  v4u* xn = (v4u*)(ws + WS_XN + (size_t)row * DM * 2);
  xn[lane] = v4u{pack2(v[0], v[1]), pack2(v[2], v[3]), pack2(v[4], v[5]), pack2(v[6], v[7])};
  xn[64 + lane] = v4u{pack2(v[8], v[9]), pack2(v[10], v[11]), pack2(v[12], v[13]), pack2(v[14], v[15])};
  ln16(v);
  v4u* y = (v4u*)(ws + WS_Y + (size_t)row * DM * 2);
  y[lane] = v4u{pack2(v[0], v[1]), pack2(v[2], v[3]), pack2(v[4], v[5]), pack2(v[6], v[7])};
  y[64 + lane] = v4u{pack2(v[8], v[9]), pack2(v[10], v[11]), pack2(v[12], v[13]), pack2(v[14], v[15])};
}

template <int BM> struct GemmCfg {
  static constexpr int BN = 128, BK = 64;
  static constexpr int A_BYTES = BM * BK * 2, B_BYTES = BK * BN * 2, ST_BYTES = A_BYTES + B_BYTES;
  static constexpr int LDS_BYTES = 3 * ST_BYTES;
};

template <int BM, int WM, int WN, bool TOUT, class Epi>
__device__ __forceinline__ void gemm_tile(const u16* __restrict__ A, int lda, const u16* __restrict__ B, int ldb, int K,
                                          int brow, int bcol, LAS char* lds, Epi epi) {
  using C = GemmCfg<BM>;
  constexpr int TM = BM / WM / 16, TN = C::BN / WN / 16;
  constexpr int AI = C::A_BYTES / 8192, BI = C::B_BYTES / 8192;
  const int tid = threadIdx.x, lane = tid & 63, w = __builtin_amdgcn_readfirstlane(tid >> 6);
  const int wr = w / WN, wc = w % WN, fr = lane & 15, fq = lane >> 4;
  const u16* asrc[AI];
  const u16* bsrc[BI];
#pragma unroll
  for (int i = 0; i < AI; ++i) {
    const int ii = w * AI + i, row = ii * 8 + (lane >> 3), ch = (lane & 7) ^ (row & 7);
    asrc[i] = A + (size_t)(brow + row) * lda + ch * 8;
  }
#pragma unroll
  for (int i = 0; i < BI; ++i) {
    const int ii = w * BI + i, row = ii * 4 + (lane >> 4), ch = (lane & 15) ^ (((row & 3) << 2) | ((row >> 2) & 3));
    bsrc[i] = B + (size_t)row * ldb + bcol + ch * 8;
  }
  auto stage = [&](int t, int buf) {
    LAS char* base = lds + buf * C::ST_BYTES;
#pragma unroll
    for (int i = 0; i < AI; ++i)
      __builtin_amdgcn_global_load_lds((const GAS void*)(asrc[i] + t * 64), (LAS void*)(base + (w * AI + i) * 1024), 16, 0, 0);
#pragma unroll
    for (int i = 0; i < BI; ++i)
      __builtin_amdgcn_global_load_lds((const GAS void*)(bsrc[i] + (size_t)t * 64 * ldb), (LAS void*)(base + C::A_BYTES + (w * BI + i) * 1024), 16, 0, 0);
  };
  int aoff[TM][2], boff[TN][2][2];
#pragma unroll
  for (int m = 0; m < TM; ++m)
#pragma unroll
    for (int k = 0; k < 2; ++k) {
      const int row = wr * (TM * 16) + m * 16 + fr, ch = k * 4 + fq;
      aoff[m][k] = row * 128 + ((ch ^ (row & 7)) * 16);
    }
  {
    const int q = fr >> 2, p = lane & 3;
#pragma unroll
    for (int n = 0; n < TN; ++n)
#pragma unroll
      for (int k = 0; k < 2; ++k)
#pragma unroll
        for (int t = 0; t < 2; ++t) {
          const int row = 32 * k + 8 * fq + 4 * t + q, c = wc * TN + n, ch = 2 * c + (p >> 1);
          boff[n][k][t] = C::A_BYTES + 256 * row + 16 * (ch ^ (((row & 3) << 2) | ((row >> 2) & 3))) + 8 * (p & 1);
        }
  }
  f32x4 acc[TM][TN];
#pragma unroll
  for (int m = 0; m < TM; ++m)
#pragma unroll
    for (int n = 0; n < TN; ++n) acc[m][n] = f32x4{0.f, 0.f, 0.f, 0.f};
  const int nt = K / 64;
  stage(0, 0);
  stage(1, 1);
  int rb = 0, sb = 2;
  for (int t = 0; t < nt; ++t) {
    if (t + 1 < nt) {
      if constexpr (AI + BI == 3) asm volatile("s_waitcnt vmcnt(3)" ::: "memory");
      else if constexpr (AI + BI == 4) asm volatile("s_waitcnt vmcnt(4)" ::: "memory");
      else asm volatile("s_waitcnt vmcnt(6)" ::: "memory");
    } else {
      asm volatile("s_waitcnt vmcnt(0)" ::: "memory");
    }
    asm volatile("s_waitcnt lgkmcnt(0)" ::: "memory");
    __builtin_amdgcn_s_barrier();
    asm volatile("" ::: "memory");
    if (t + 2 < nt) stage(t + 2, sb);
    LAS const char* base = lds + rb * C::ST_BYTES;
    v4u af[TM][2], bfr[TN][2];
#pragma unroll
    for (int k = 0; k < 2; ++k) {
#pragma unroll
      for (int n = 0; n < TN; ++n) {
        v2u lo = trread(base + boff[n][k][0]), hi = trread(base + boff[n][k][1]);
        bfr[n][k] = v4u{lo.x, lo.y, hi.x, hi.y};
      }
#pragma unroll
      for (int m = 0; m < TM; ++m) af[m][k] = *(LAS const v4u*)(base + aoff[m][k]);
    }
    __builtin_amdgcn_s_setprio(1);
#pragma unroll
    for (int k = 0; k < 2; ++k)
#pragma unroll
      for (int m = 0; m < TM; ++m)
#pragma unroll
        for (int n = 0; n < TN; ++n)
          acc[m][n] = TOUT ? mfma16(af[m][k], bfr[n][k], acc[m][n]) : mfma16(bfr[n][k], af[m][k], acc[m][n]);
    __builtin_amdgcn_s_setprio(0);
    rb = rb == 2 ? 0 : rb + 1;
    sb = sb == 2 ? 0 : sb + 1;
  }
#pragma unroll
  for (int m = 0; m < TM; ++m)
#pragma unroll
    for (int n = 0; n < TN; ++n) {
      const int r0 = brow + wr * (TM * 16) + m * 16, c0 = bcol + wc * (TN * 16) + n * 16;
      if (TOUT) epi(r0 + fq * 4, c0 + fr, acc[m][n]);
      else epi(r0 + fr, c0 + fq * 4, acc[m][n]);
    }
  asm volatile("s_waitcnt lgkmcnt(0)" ::: "memory");
  __builtin_amdgcn_s_barrier();
}

__device__ __forceinline__ float gelu_tanh(float x) {
  const float u = 0.7978845608028654f * (x + 0.044715f * x * x * x);
  const float e = __expf(2.0f * u);
  const float t = 1.0f - 2.0f / (e + 1.0f);
  return 0.5f * x * (1.0f + t);
}

template <int BM, int WM, int WN>
__device__ __forceinline__ void task_qkv(int rt, int ct, const u16* Wq, const u16* Wk, const u16* Wv, unsigned char* ws, LAS char* lds) {
  const u16* xn = (const u16*)(ws + WS_XN);
  const int which = ct >> 5, bcol = (ct & 31) * 128, brow = rt * BM;
  if (which < 2) {
    u16* out = (u16*)(ws + (which == 0 ? WS_Q : WS_K));
    gemm_tile<BM, WM, WN, false>(xn, DM, which == 0 ? Wq : Wk, HD, DM, brow, bcol, lds, [&](int r, int c, f32x4 v) {
      *(v2u*)(out + (size_t)r * HD + c) = v2u{pack2(v[0], v[1]), pack2(v[2], v[3])};
    });
  } else {
    u16* vt = (u16*)(ws + WS_VT);
    gemm_tile<BM, WM, WN, true>(xn, DM, Wv, HD, DM, brow, bcol, lds, [&](int r, int c, f32x4 v) {
      const int seg = r >> 5, t = r & 31, p = 8 * ((t & 15) >> 2) + 4 * (t >> 4);
      *(v2u*)(vt + ((size_t)seg * HD + c) * 32 + p) = v2u{pack2(v[0], v[1]), pack2(v[2], v[3])};
    });
  }
}
template <int BM, int WM, int WN>
__device__ __forceinline__ void task_mlp1(int rt, int ct, const u16* w1, const u16* b1, unsigned char* ws, LAS char* lds) {
  const u16* y = (const u16*)(ws + WS_Y);
  u16* h = (u16*)(ws + WS_H);
  gemm_tile<BM, WM, WN, false>(y, DM, w1, FF, DM, rt * BM, ct * 128, lds, [&](int r, int c, f32x4 v) {
    const v2u bb = *(const v2u*)(b1 + c);
    const float o0 = gelu_tanh(rbf(rbf(v[0]) + bflo(bb.x))), o1 = gelu_tanh(rbf(rbf(v[1]) + bfhi(bb.x)));
    const float o2 = gelu_tanh(rbf(rbf(v[2]) + bflo(bb.y))), o3 = gelu_tanh(rbf(rbf(v[3]) + bfhi(bb.y)));
    *(v2u*)(h + (size_t)r * FF + c) = v2u{pack2(o0, o1), pack2(o2, o3)};
  });
}
template <int BM, int WM, int WN>
__device__ __forceinline__ void task_mlp2(int rt, int ct, const u16* w2, const u16* b2, unsigned char* ws, LAS char* lds) {
  const u16* h = (const u16*)(ws + WS_H);
  u16* bo = (u16*)(ws + WS_BO);
  gemm_tile<BM, WM, WN, false>(h, FF, w2, DM, FF, rt * BM, ct * 128, lds, [&](int r, int c, f32x4 v) {
    const v2u bb = *(const v2u*)(b2 + c);
    *(v2u*)(bo + (size_t)r * DM + c) = v2u{pack2(rbf(v[0]) + bflo(bb.x), rbf(v[1]) + bfhi(bb.x)), pack2(rbf(v[2]) + bflo(bb.y), rbf(v[3]) + bfhi(bb.y))};
  });
}
template <int BM, int WM, int WN>
__device__ __forceinline__ void task_wo(int rt, int ct, const u16* Wo, const u16* x, u16* out, unsigned char* ws, LAS char* lds) {
  const u16* ot = (const u16*)(ws + WS_OT);
  const u16* bo = (const u16*)(ws + WS_BO);
  gemm_tile<BM, WM, WN, false>(ot, HD, Wo, DM, HD, rt * BM, ct * 128, lds, [&](int r, int c, f32x4 v) {
    const v2u xx = *(const v2u*)(x + (size_t)r * DM + c);
    const v2u bb = *(const v2u*)(bo + (size_t)r * DM + c);
    const float o0 = rbf(bflo(xx.x) + rbf(v[0])) + bflo(bb.x), o1 = rbf(bfhi(xx.x) + rbf(v[1])) + bfhi(bb.x);
    const float o2 = rbf(bflo(xx.y) + rbf(v[2])) + bflo(bb.y), o3 = rbf(bfhi(xx.y) + rbf(v[3])) + bfhi(bb.y);
    *(v2u*)(out + (size_t)r * DM + c) = v2u{pack2(o0, o1), pack2(o2, o3)};
  });
}

__global__ __launch_bounds__(512, 2) void k_qkv(const u16* Wq, const u16* Wk, const u16* Wv, unsigned char* ws) {
  extern __shared__ __attribute__((aligned(16))) char smem[];
  const int b = blockIdx.x;
  task_qkv<256, 4, 2>(b & 7, b >> 3, Wq, Wk, Wv, ws, (LAS char*)smem);
}
__global__ __launch_bounds__(512, 2) void k_mlp1(const u16* w1, const u16* b1, unsigned char* ws) {
  extern __shared__ __attribute__((aligned(16))) char smem[];
  const int b = blockIdx.x;
  task_mlp1<256, 4, 2>(b & 7, b >> 3, w1, b1, ws, (LAS char*)smem);
}
__global__ __launch_bounds__(512, 2) void k_mlp2(const u16* w2, const u16* b2, unsigned char* ws) {
  extern __shared__ __attribute__((aligned(16))) char smem[];
  const int b = blockIdx.x;
  task_mlp2<64, 2, 4>(b & 31, b >> 5, w2, b2, ws, (LAS char*)smem);
}
__global__ __launch_bounds__(512, 2) void k_wo(const u16* Wo, const u16* x, u16* out, unsigned char* ws) {
  extern __shared__ __attribute__((aligned(16))) char smem[];
  const int b = blockIdx.x;
  task_wo<64, 2, 4>(b & 31, b >> 5, Wo, x, out, ws, (LAS char*)smem);
}

__device__ __forceinline__ bool rdy(v4u v) { return max(max(v.x, v.y), max(v.z, v.w)) != SENT; }
__device__ __forceinline__ v4u ld_sc1(__amdgpu_buffer_rsrc_t rs, int off) { return __builtin_amdgcn_raw_buffer_load_b128(rs, off, 0, 16); }

template <int N>
__device__ __forceinline__ void poll(__amdgpu_buffer_rsrc_t rs, int off, v4u (&d)[N], unsigned* err) {
  unsigned spins = 0;
  for (;;) {
    unsigned mx = 0;
#pragma unroll
    for (int i = 0; i < N; ++i) d[i] = ld_sc1(rs, off + i * 1024);
    asm volatile("" ::: "memory");
#pragma unroll
    for (int i = 0; i < N; ++i) mx = max(max(mx, max(d[i].x, d[i].y)), max(d[i].z, d[i].w));
    if (__all(mx != SENT)) break;
    if (++spins > SPINCAP) {
      if ((threadIdx.x & 63) == 0) __hip_atomic_store(err, 1u, __ATOMIC_RELAXED, __HIP_MEMORY_SCOPE_AGENT);
      break;
    }
    if ((spins & 1023u) == 0u && __hip_atomic_load(err, __ATOMIC_RELAXED, __HIP_MEMORY_SCOPE_AGENT) != 0u) break;
  }
}
__device__ __forceinline__ float xor16(float x) {
  auto r = __builtin_amdgcn_permlane16_swap(__builtin_bit_cast(unsigned, x), __builtin_bit_cast(unsigned, x), false, false);
  const int odd = (threadIdx.x >> 4) & 1;
  return __builtin_bit_cast(float, odd ? r[0] : r[1]);
}
__device__ __forceinline__ float xor32(float x) {
  auto r = __builtin_amdgcn_permlane32_swap(__builtin_bit_cast(unsigned, x), __builtin_bit_cast(unsigned, x), false, false);
  const int hi = (threadIdx.x >> 5) & 1;
  return __builtin_bit_cast(float, hi ? r[0] : r[1]);
}

constexpr int CH_RED = 0;
constexpr int CH_VM = 24576;
constexpr int CH_RED2 = 25088;
constexpr int CH_WV = 27136;
constexpr int CH_KV = CH_WV + 65536;
constexpr int CH_LDS = CH_KV + 20480;

__device__ __forceinline__ void chain_role(int c, const u16* __restrict__ mem0, const u16* __restrict__ Wq, const u16* __restrict__ Wk,
                                           const u16* __restrict__ Wv, const u16* __restrict__ Wo, unsigned char* ws, LAS char* lds) {
  const int tid = threadIdx.x, lane = tid & 63, w = __builtin_amdgcn_readfirstlane(tid >> 6);
  const int fr = lane & 15, g = lane >> 4;
  const int h = c >> 2, q4 = c & 3, mh = c & 1, nb = c >> 1;
  unsigned* err = (unsigned*)(ws + WS_ERR);
  const __amdgpu_buffer_rsrc_t rs = __builtin_amdgcn_make_buffer_rsrc((void*)(ws + WS_HOP), 0, (int)HOP_BYTES, 0x00020000);
  v4u wq[4][4], wo[16];
  LAS v4u* wvl = (LAS v4u*)(lds + CH_WV) + w * 512 + lane;
  {
    LAS char* stg = lds + CH_WV + w * 8192;
    const int q = fr >> 2, p = lane & 3;
#pragma unroll
    for (int hf = 0; hf < 2; ++hf) {
#pragma unroll
      for (int i = 0; i < 8; ++i) {
        const int row = i * 32 + (lane >> 1), ch = lane & 1;
        v4u v = *(const v4u*)(Wo + (size_t)(512 * w + 256 * hf + row) * DM + 16 * nb + ch * 8);
        *(LAS v4u*)(stg + row * 32 + ch * 16) = v;
      }
      __builtin_amdgcn_s_waitcnt(0xC07F);
#pragma unroll
      for (int ks = 0; ks < 8; ++ks) {
        v2u lo = trread(stg + (32 * ks + 8 * g + q) * 32 + (4 * p) * 2);
        v2u hi = trread(stg + (32 * ks + 8 * g + 4 + q) * 32 + (4 * p) * 2);
        wo[hf * 8 + ks] = v4u{lo.x, lo.y, hi.x, hi.y};
      }
      __builtin_amdgcn_s_waitcnt(0xC07F);
    }
#pragma unroll
    for (int mat = 0; mat < 3; ++mat) {
      const u16* W = mat == 0 ? Wq : (mat == 1 ? Wk : Wv);
#pragma unroll
      for (int i = 0; i < 8; ++i) {
        const int row = i * 16 + (lane >> 2), ch = lane & 3;
        v4u v = *(const v4u*)(W + (size_t)(128 * w + row) * HD + h * 128 + 32 * q4 + ch * 8);
        *(LAS v4u*)(stg + row * 64 + ch * 16) = v;
      }
      __builtin_amdgcn_s_waitcnt(0xC07F);
      v4u tv[8];
#pragma unroll
      for (int dt = 0; dt < 2; ++dt)
#pragma unroll
        for (int ks = 0; ks < 4; ++ks) {
          v2u lo = trread(stg + (32 * ks + 8 * g + q) * 64 + (16 * dt + 4 * p) * 2);
          v2u hi = trread(stg + (32 * ks + 8 * g + 4 + q) * 64 + (16 * dt + 4 * p) * 2);
          if (mat < 2) wq[(mat & 1) * 2 + dt][ks] = v4u{lo.x, lo.y, hi.x, hi.y};
          else tv[dt * 4 + ks] = v4u{lo.x, lo.y, hi.x, hi.y};
        }
      __builtin_amdgcn_s_waitcnt(0xC07F);
      asm volatile("" ::: "memory");
      if (mat == 2) {
#pragma unroll
        for (int i = 0; i < 8; ++i) wvl[i * 64] = tv[i];
      }
    }
  }
  const u16* Kx = (const u16*)(ws + WS_K);
  const u16* Vt = (const u16*)(ws + WS_VT);
  u16* VM = (u16*)(ws + WS_VM);
  const float scale = 0.08838834764831845f;
  const float L2E = 1.4426950408889634f;
  LAS f32x4* red = (LAS f32x4*)(lds + CH_RED);
  LAS f32x4* red2 = (LAS f32x4*)(lds + CH_RED2);
  LAS u16* vml = (LAS u16*)(lds + CH_VM);
  LAS v4u* kvl = (LAS v4u*)(lds + CH_KV) + lane;
  if (w == 2) {
    v4u t[10];
#pragma unroll
    for (int tt = 0; tt < 2; ++tt)
#pragma unroll
      for (int j = 0; j < 4; ++j) t[tt * 4 + j] = *(const v4u*)(Kx + (size_t)(16 * tt + fr) * HD + h * 128 + 32 * j + 8 * g);
#pragma unroll
    for (int dt = 0; dt < 2; ++dt) t[8 + dt] = *(const v4u*)(Vt + ((size_t)h * 128 + 32 * q4 + 16 * dt + fr) * 32 + 8 * g);
#pragma unroll
    for (int i = 0; i < 10; ++i) kvl[i * 64] = t[i];
  }

#define STAMP(i)
  for (int s = 0; s < NSEG; ++s) {
    v4u mf[2];
    if (s == 0) {
#pragma unroll
      for (int p = 0; p < 2; ++p)
        mf[p] = *(const v4u*)(mem0 + (size_t)(fr & 7) * DM + 128 * w + 32 * (2 * p + (fr >> 3)) + 8 * g);
    } else {
      poll<2>(rs, (int)(HOP_MEM + (size_t)s * 16384) + ((16 * w + 4 * (fr >> 3) + g) * 8 + (fr & 7)) * 16, mf, err);
    }
    STAMP(0)
    {
      f32x4 a0[4], a1[4];
#pragma unroll
      for (int t = 0; t < 4; ++t) {
        a0[t] = mfma16(wq[t][0], mf[0], f32x4{0.f, 0.f, 0.f, 0.f});
        a1[t] = mfma16(wq[t][1], mf[0], f32x4{0.f, 0.f, 0.f, 0.f});
      }
#pragma unroll
      for (int t = 0; t < 4; ++t) {
        a0[t] = mfma16(wq[t][2], mf[1], a0[t]);
        a1[t] = mfma16(wq[t][3], mf[1], a1[t]);
      }
#pragma unroll
      for (int t = 0; t < 4; ++t) {
        f32x4 x = (fr & 8) ? a1[t] : a0[t];
#pragma unroll
        for (int r = 0; r < 4; ++r) x[r] += row_ror<8>(x[r]);
        if (fr < 8) red[(w * 6 + t) * 32 + g * 8 + fr] = x;
      }
    }
    __syncthreads();
    STAMP(1)
    if (tid >= 384) {
      const int u = tid - 384;
      const int t6 = u >> 5, gg = (u >> 3) & 3, m = u & 7;
      f32x4 x = red[t6 * 32 + (u & 31)];
#pragma unroll
      for (int ww = 1; ww < 8; ++ww) x += red[(ww * 6 + t6) * 32 + (u & 31)];
      const unsigned lo = pack2(x[0], x[1]), hi = pack2(x[2], x[3]);
      const int mat = t6 >> 1, dt = t6 & 1;
      const int off = (int)(HOP_QK + ((size_t)s * NH + h) * 4096) + ((q4 * 2 + mat) * 8 + m) * 64 + (16 * dt + 4 * gg) * 2;
      __builtin_amdgcn_raw_buffer_store_b64(v2u{nosent(lo), nosent(hi)}, rs, off, 0, 16);
    }
    {
      f32x4 a0[2], a1[2];
#pragma unroll
      for (int t = 0; t < 2; ++t) {
        a0[t] = mfma16(wvl[(t * 4 + 0) * 64], mf[0], f32x4{0.f, 0.f, 0.f, 0.f});
        a1[t] = mfma16(wvl[(t * 4 + 1) * 64], mf[0], f32x4{0.f, 0.f, 0.f, 0.f});
      }
#pragma unroll
      for (int t = 0; t < 2; ++t) {
        a0[t] = mfma16(wvl[(t * 4 + 2) * 64], mf[1], a0[t]);
        a1[t] = mfma16(wvl[(t * 4 + 3) * 64], mf[1], a1[t]);
      }
#pragma unroll
      for (int t = 0; t < 2; ++t) {
        f32x4 x = (fr & 8) ? a1[t] : a0[t];
#pragma unroll
        for (int r = 0; r < 4; ++r) x[r] += row_ror<8>(x[r]);
        if (fr < 8) red[(w * 6 + 4 + t) * 32 + g * 8 + fr] = x;
      }
    }
    __syncthreads();
    STAMP(2)
    if (w == 1) {
      const int dt = lane >> 5, gg = (lane >> 3) & 3, m = lane & 7;
      f32x4 x = red[(4 + dt) * 32 + (lane & 31)];
#pragma unroll
      for (int ww = 1; ww < 8; ++ww) x += red[(ww * 6 + 4 + dt) * 32 + (lane & 31)];
      const unsigned lo = pack2(x[0], x[1]), hi = pack2(x[2], x[3]);
      const int d = 16 * dt + 4 * gg;
      vml[(d + 0) * 8 + m] = (u16)(lo & 0xFFFF);
      vml[(d + 1) * 8 + m] = (u16)(lo >> 16);
      vml[(d + 2) * 8 + m] = (u16)(hi & 0xFFFF);
      vml[(d + 3) * 8 + m] = (u16)(hi >> 16);
      __builtin_amdgcn_s_waitcnt(0xC07F);
      asm volatile("" ::: "memory");
      if (lane < 32)
        *(v4u*)(VM + (((size_t)s * NH + h) * 128 + 32 * q4 + lane) * 8) = *(LAS v4u*)(vml + lane * 8);
    }
    if (s == NSEG - 1) break;
    if (w == 2 && s + 1 < NSEG - 1) {
      v4u t[10];
      const int sn = s + 1;
#pragma unroll
      for (int tt = 0; tt < 2; ++tt)
#pragma unroll
        for (int j = 0; j < 4; ++j) t[tt * 4 + j] = *(const v4u*)(Kx + (size_t)(32 * sn + 16 * tt + fr) * HD + h * 128 + 32 * j + 8 * g);
#pragma unroll
      for (int dt = 0; dt < 2; ++dt) t[8 + dt] = *(const v4u*)(Vt + (((size_t)sn * NH + h) * 128 + 32 * q4 + 16 * dt + fr) * 32 + 8 * g);
#pragma unroll
      for (int i = 0; i < 10; ++i) kvl[((sn & 1) * 10 + i) * 64] = t[i];
    }
    v4u pt, pm;
    if (w == 0) {
      v4u X[8];
      {
        const int base = (int)(HOP_QK + ((size_t)s * NH + h) * 4096) + (fr & 7) * 64 + g * 16;
        unsigned spins = 0;
        for (;;) {
          unsigned mx = 0;
#pragma unroll
          for (int i = 0; i < 8; ++i) X[i] = ld_sc1(rs, base + i * 512);
          asm volatile("" ::: "memory");
#pragma unroll
          for (int i = 0; i < 8; ++i) mx = max(max(mx, max(X[i].x, X[i].y)), max(X[i].z, X[i].w));
          if (__all(mx != SENT)) break;
          if (++spins > SPINCAP) {
            if (lane == 0) __hip_atomic_store(err, 1u, __ATOMIC_RELAXED, __HIP_MEMORY_SCOPE_AGENT);
            break;
          }
          if ((spins & 1023u) == 0u && __hip_atomic_load(err, __ATOMIC_RELAXED, __HIP_MEMORY_SCOPE_AGENT) != 0u) break;
        }
      }
      STAMP(3)
      f32x4 s0 = {0.f, 0.f, 0.f, 0.f}, s1 = s0, s2 = s0;
#pragma unroll
      for (int j = 0; j < 4; ++j) {
        s1 = mfma16(kvl[((s & 1) * 10 + j) * 64], X[2 * j], s1);
        s2 = mfma16(kvl[((s & 1) * 10 + 4 + j) * 64], X[2 * j], s2);
        s0 = mfma16(X[2 * j + 1], X[2 * j], s0);
      }
      const bool up = fr >= 8;
      f32x4 st = up ? s2 : s1;
      const float m01 = up ? s0[1] : s0[0], m23 = up ? s0[3] : s0[2];
      const float sm = g >= 2 ? m23 : m01;
      unsigned p0 = pack2(st[0], st[1]), p1 = pack2(st[2], st[3]), p2 = pack2(sm, sm);
      p0 = pack2(bflo(p0) * scale, bfhi(p0) * scale);
      p1 = pack2(bflo(p1) * scale, bfhi(p1) * scale);
      p2 = pack2(bflo(p2) * scale, bflo(p2) * scale);
      float v0 = bflo(p0), v1 = bfhi(p0), v2 = bflo(p1), v3 = bfhi(p1), v4 = bflo(p2);
      float mx = fmaxf(fmaxf(fmaxf(v0, v1), fmaxf(v2, v3)), v4);
      mx = fmaxf(mx, row_ror<8>(mx));
      mx = fmaxf(mx, xor16(mx));
      mx = fmaxf(mx, xor32(mx));
      const float nm = -mx * L2E;
      v0 = __builtin_amdgcn_exp2f(__builtin_fmaf(v0, L2E, nm));
      v1 = __builtin_amdgcn_exp2f(__builtin_fmaf(v1, L2E, nm));
      v2 = __builtin_amdgcn_exp2f(__builtin_fmaf(v2, L2E, nm));
      v3 = __builtin_amdgcn_exp2f(__builtin_fmaf(v3, L2E, nm));
      v4 = __builtin_amdgcn_exp2f(__builtin_fmaf(v4, L2E, nm));
      float sum = ((v0 + v1) + (v2 + v3)) + v4;
      sum += row_ror<8>(sum);
      sum += xor16(sum);
      sum += xor32(sum);
      const float inv = __builtin_amdgcn_rcpf(sum);
      pt.x = pack2(v0 * inv, v1 * inv);
      pt.y = pack2(v2 * inv, v3 * inv);
      pt.z = (unsigned)__builtin_amdgcn_update_dpp(0, (int)pt.x, 0x128, 0xf, 0xf, false);
      pt.w = (unsigned)__builtin_amdgcn_update_dpp(0, (int)pt.y, 0x128, 0xf, 0xf, false);
      v4 *= inv;
      pm.x = pack2(v4, row_ror<8>(v4));
      pm.y = 0u; pm.z = 0u; pm.w = 0u;
    }
    __syncthreads();
    if (w == 0) {
      f32x4 o[2];
#pragma unroll
      for (int dt = 0; dt < 2; ++dt) o[dt] = mfma16(kvl[((s & 1) * 10 + 8 + dt) * 64], pt, f32x4{0.f, 0.f, 0.f, 0.f});
#pragma unroll
      for (int dt = 0; dt < 2; ++dt) {
        const unsigned t = *(LAS unsigned*)(vml + (16 * dt + fr) * 8 + 4 * (g & 1) + 2 * (g >> 1));
        o[dt] = mfma16(v4u{t, 0u, 0u, 0u}, pm, o[dt]);
      }
      if (fr < 8) {
#pragma unroll
        for (int dt = 0; dt < 2; ++dt) {
          const int k8 = h * 16 + 4 * q4 + 2 * dt + (g >> 1);
          const int off = (int)(HOP_OB + (size_t)s * 65536) + (((fr >> 2) * 512 + k8) * 4 + (fr & 3)) * 16 + (g & 1) * 8;
          __builtin_amdgcn_raw_buffer_store_b64(v2u{nosent(pack2(o[dt][0], o[dt][1])), nosent(pack2(o[dt][2], o[dt][3]))}, rs, off, 0, 16);
        }
      }
    }
    __syncthreads();
    STAMP(4)
    {
      v4u of[4];
      poll<4>(rs, (int)(HOP_OB + (size_t)s * 65536) + ((mh * 512 + 64 * w + 4 * (fr >> 2) + g) * 4 + (fr & 3)) * 16, of, err);
      STAMP(5)
      f32x4 a[4];
#pragma unroll
      for (int r4 = 0; r4 < 4; ++r4) a[r4] = mfma16(wo[r4], of[0], f32x4{0.f, 0.f, 0.f, 0.f});
#pragma unroll
      for (int i = 1; i < 4; ++i)
#pragma unroll
        for (int r4 = 0; r4 < 4; ++r4) a[r4] = mfma16(wo[4 * i + r4], of[i], a[r4]);
      const int r4 = fr >> 2;
      f32x4 x = r4 == 0 ? a[0] : (r4 == 1 ? a[1] : (r4 == 2 ? a[2] : a[3]));
#pragma unroll
      for (int r = 0; r < 4; ++r) { x[r] += row_ror<8>(x[r]); x[r] += row_ror<4>(x[r]); }
      if (fr < 4) red2[w * 16 + g * 4 + fr] = x;
    }
    __syncthreads();
    if (tid < 16) {
      f32x4 x = red2[tid];
#pragma unroll
      for (int ww = 1; ww < 8; ++ww) x += red2[ww * 16 + tid];
      const int gg = tid >> 2, m4 = tid & 3;
      const int k8 = 2 * nb + (gg >> 1);
      const int off = (int)(HOP_MEM + (size_t)(s + 1) * 16384) + (k8 * 8 + 4 * mh + m4) * 16 + (gg & 1) * 8;
      __builtin_amdgcn_raw_buffer_store_b64(v2u{nosent(pack2(x[0], x[1])), nosent(pack2(x[2], x[3]))}, rs, off, 0, 16);
    }
    STAMP(6)
  }
}

__global__ __launch_bounds__(512, 2) void k_chain(const u16* mem0, const u16* Wq, const u16* Wk, const u16* Wv, const u16* Wo, unsigned char* ws) {
  extern __shared__ __attribute__((aligned(16))) char smem[];
  const int b = blockIdx.x;
  const int xcd = b & 7, i = b >> 3;
  const int c = ((xcd * 4 + (i >> 2)) << 2) | (i & 3);
  chain_role(c, mem0, Wq, Wk, Wv, Wo, ws, (LAS char*)smem);
}

__device__ __forceinline__ void attn_unit(int s, int h, int lane, unsigned char* ws) {
  const int fr = lane & 15, g = lane >> 4;
  const u16* Q = (const u16*)(ws + WS_Q);
  const u16* Kx = (const u16*)(ws + WS_K);
  const u16* Vt = (const u16*)(ws + WS_VT);
  const u16* VM = (const u16*)(ws + WS_VM);
  const u16* QK = (const u16*)(ws + WS_HOP + HOP_QK + ((size_t)s * NH + h) * 4096);
  u16* OT = (u16*)(ws + WS_OT);
  const float scale = 0.08838834764831845f;
  f32x4 sc[3][2];
#pragma unroll
  for (int a = 0; a < 3; ++a)
#pragma unroll
    for (int b = 0; b < 2; ++b) sc[a][b] = f32x4{0.f, 0.f, 0.f, 0.f};
#pragma unroll
  for (int j = 0; j < 4; ++j) {
    v4u qf[2], kf[3];
#pragma unroll
    for (int t = 0; t < 2; ++t) {
      qf[t] = *(const v4u*)(Q + (size_t)(32 * s + 16 * t + fr) * HD + h * 128 + 32 * j + 8 * g);
      kf[1 + t] = *(const v4u*)(Kx + (size_t)(32 * s + 16 * t + fr) * HD + h * 128 + 32 * j + 8 * g);
    }
    kf[0] = *(const v4u*)(QK + ((j * 2 + 1) * 8 + (fr & 7)) * 32 + g * 8);
#pragma unroll
    for (int a = 0; a < 3; ++a)
#pragma unroll
      for (int b = 0; b < 2; ++b) sc[a][b] = mfma16(kf[a], qf[b], sc[a][b]);
  }
  v4u pt[2], pm[2];
#pragma unroll
  for (int b = 0; b < 2; ++b) {
    const int qi = 16 * b + fr;
    float v[12];
    bool ok[12];
#pragma unroll
    for (int r = 0; r < 4; ++r) {
      v[r] = rbf(rbf(sc[0][b][r]) * scale); ok[r] = g < 2;
      v[4 + r] = rbf(rbf(sc[1][b][r]) * scale); ok[4 + r] = (4 * g + r) <= qi;
      v[8 + r] = rbf(rbf(sc[2][b][r]) * scale); ok[8 + r] = (16 + 4 * g + r) <= qi;
    }
    float mx = -3.0e38f;
#pragma unroll
    for (int i = 0; i < 12; ++i) mx = ok[i] ? fmaxf(mx, v[i]) : mx;
    mx = fmaxf(mx, __shfl_xor(mx, 16));
    mx = fmaxf(mx, __shfl_xor(mx, 32));
    float sum = 0.f;
#pragma unroll
    for (int i = 0; i < 12; ++i) { v[i] = ok[i] ? __expf(v[i] - mx) : 0.f; sum += v[i]; }
    sum += __shfl_xor(sum, 16);
    sum += __shfl_xor(sum, 32);
    const float inv = 1.0f / sum;
    pt[b].x = pack2(v[4] * inv, v[5] * inv); pt[b].y = pack2(v[6] * inv, v[7] * inv);
    pt[b].z = pack2(v[8] * inv, v[9] * inv); pt[b].w = pack2(v[10] * inv, v[11] * inv);
    pm[b].x = pack2(v[0] * inv, v[1] * inv); pm[b].y = pack2(v[2] * inv, v[3] * inv); pm[b].z = 0u; pm[b].w = 0u;
  }
#pragma unroll
  for (int dt = 0; dt < 8; ++dt) {
    v4u vtf = *(const v4u*)(Vt + (((size_t)s * NH + h) * 128 + 16 * dt + fr) * 32 + 8 * g);
    v4u vmf = {0u, 0u, 0u, 0u};
    if (g < 2) {
      v2u t = *(const v2u*)(VM + (((size_t)s * NH + h) * 128 + 16 * dt + fr) * 8 + 4 * g);
      vmf.x = t.x; vmf.y = t.y;
    }
#pragma unroll
    for (int b = 0; b < 2; ++b) {
      f32x4 o = {0.f, 0.f, 0.f, 0.f};
      o = mfma16(vtf, pt[b], o);
      o = mfma16(vmf, pm[b], o);
      *(v2u*)(OT + (size_t)(32 * s + 16 * b + fr) * HD + h * 128 + 16 * dt + 4 * g) = v2u{pack2(o[0], o[1]), pack2(o[2], o[3])};
    }
  }
}
__global__ __launch_bounds__(256) void k_attn(unsigned char* ws) {
  const int u = blockIdx.x * 4 + (threadIdx.x >> 6);
  attn_unit(u >> 5, u & 31, threadIdx.x & 63, ws);
}

extern "C" void kernel_launch(void* const* d_in, const int* in_sizes, int n_in, void* d_out, int out_size, void* d_ws, size_t ws_size,
                              hipStream_t stream) {
  const u16* x = (const u16*)d_in[0];
  const u16* mem0 = (const u16*)d_in[1];
  const u16* Wq = (const u16*)d_in[2];
  const u16* Wk = (const u16*)d_in[3];
  const u16* Wv = (const u16*)d_in[4];
  const u16* Wo = (const u16*)d_in[5];
  const u16* w1 = (const u16*)d_in[8];
  const u16* b1 = (const u16*)d_in[9];
  const u16* w2 = (const u16*)d_in[10];
  const u16* b2 = (const u16*)d_in[11];
  unsigned char* ws = (unsigned char*)d_ws;
  u16* out = (u16*)d_out;
  constexpr int L256 = GemmCfg<256>::LDS_BYTES, L64 = GemmCfg<64>::LDS_BYTES;
  (void)hipFuncSetAttribute((const void*)k_qkv, hipFuncAttributeMaxDynamicSharedMemorySize, L256);
  (void)hipFuncSetAttribute((const void*)k_mlp1, hipFuncAttributeMaxDynamicSharedMemorySize, L256);
  (void)hipFuncSetAttribute((const void*)k_mlp2, hipFuncAttributeMaxDynamicSharedMemorySize, L64);
  (void)hipFuncSetAttribute((const void*)k_wo, hipFuncAttributeMaxDynamicSharedMemorySize, L64);
  (void)hipFuncSetAttribute((const void*)k_chain, hipFuncAttributeMaxDynamicSharedMemorySize, CH_LDS);
  k_ln<<<S_LEN / 4, 256, 0, stream>>>(x, ws);
  k_qkv<<<8 * 96, 512, L256, stream>>>(Wq, Wk, Wv, ws);
  k_mlp1<<<8 * 28, 512, L256, stream>>>(w1, b1, ws);
  k_mlp2<<<32 * 8, 512, L64, stream>>>(w2, b2, ws);
  k_chain<<<128, 512, CH_LDS, stream>>>(mem0, Wq, Wk, Wv, Wo, ws);
  k_attn<<<NSEG * NH / 4, 256, 0, stream>>>(ws);
  k_wo<<<32 * 8, 512, L64, stream>>>(Wo, x, out, ws);
}
```

```cpp
#include <hip/hip_runtime.h>
#include <hip/hip_bf16.h>
#include <stdint.h>

constexpr int S_LEN = 2048, DM = 1024, NH = 32, DH = 128, HD = NH * DH;
constexpr int MEMN = 8, SEG = 32, NSEG = S_LEN / SEG;
constexpr int FF = 3584;

typedef unsigned v4u __attribute__((ext_vector_type(4)));
typedef unsigned v2u __attribute__((ext_vector_type(2)));
typedef float f32x4 __attribute__((ext_vector_type(4)));
typedef float f32x2 __attribute__((ext_vector_type(2)));
typedef short bf16x8 __attribute__((ext_vector_type(8)));
typedef short s16x4 __attribute__((ext_vector_type(4)));
typedef __bf16 bfx2 __attribute__((ext_vector_type(2)));
typedef unsigned short u16;
#define LAS __attribute__((address_space(3)))
#define GAS __attribute__((address_space(1)))

constexpr size_t WS_XN = 0;
constexpr size_t WS_Y = WS_XN + (size_t)S_LEN * DM * 2;
constexpr size_t WS_Q = WS_Y + (size_t)S_LEN * DM * 2;
constexpr size_t WS_K = WS_Q + (size_t)S_LEN * HD * 2;
constexpr size_t WS_VT = WS_K + (size_t)S_LEN * HD * 2;
constexpr size_t WS_H = WS_VT + (size_t)S_LEN * HD * 2;
constexpr size_t WS_BO = WS_H + (size_t)S_LEN * FF * 2;
constexpr size_t WS_OT = WS_BO + (size_t)S_LEN * DM * 2;
constexpr size_t WS_VM = WS_OT + (size_t)S_LEN * HD * 2;
constexpr size_t WS_HOP = WS_VM + (size_t)NSEG * HD * MEMN * 2;
constexpr size_t HOP_MEM = 0;
constexpr size_t HOP_QK = HOP_MEM + (size_t)NSEG * 16384;
constexpr size_t HOP_OB = HOP_QK + (size_t)NSEG * NH * 4096;
constexpr size_t HOP_BYTES = HOP_OB + (size_t)NSEG * 65536;
constexpr size_t WS_ERR = WS_HOP + HOP_BYTES;
constexpr size_t WS_TOTAL = WS_ERR + 256;

constexpr unsigned SENT = 0xFFFFFFFFu;
constexpr unsigned SPINCAP = 1u << 20;

__device__ __forceinline__ float bf2f(unsigned b16) { return __builtin_bit_cast(float, b16 << 16); }
__device__ __forceinline__ float bflo(unsigned x) { return __builtin_bit_cast(float, x << 16); }
__device__ __forceinline__ float bfhi(unsigned x) { return __builtin_bit_cast(float, x & 0xFFFF0000u); }
__device__ __forceinline__ unsigned pack2(float a, float b) {
  f32x2 v = {a, b};
  bfx2 r = __builtin_convertvector(v, bfx2);
  return __builtin_bit_cast(unsigned, r);
}
__device__ __forceinline__ float rbf(float a) { return (float)(__bf16)a; }
__device__ __forceinline__ unsigned nosent(unsigned x) { return x == SENT ? 0xFFFFFFFEu : x; }
__device__ __forceinline__ f32x4 mfma16(v4u a, v4u b, f32x4 c) {
  return __builtin_amdgcn_mfma_f32_16x16x32_bf16(__builtin_bit_cast(bf16x8, a), __builtin_bit_cast(bf16x8, b), c, 0, 0, 0);
}
__device__ __forceinline__ v2u trread(LAS const char* p) {
  typedef short v4i16_t __attribute__((ext_vector_type(4)));
  return __builtin_bit_cast(v2u, __builtin_amdgcn_ds_read_tr16_b64_v4i16((LAS v4i16_t*)p));
}
__device__ __forceinline__ float ror(float x, int) = delete;
template <int N> __device__ __forceinline__ float row_ror(float x) {
  return __builtin_bit_cast(float, __builtin_amdgcn_update_dpp(0, __builtin_bit_cast(int, x), 0x120 + N, 0xf, 0xf, false));
}

__device__ __forceinline__ float wave_sum(float v) {
#pragma unroll
  for (int o = 32; o > 0; o >>= 1) v += __shfl_xor(v, o);
  return v;
}
__device__ __forceinline__ void ln16(float (&v)[16]) {
  float s = 0.f;
#pragma unroll
  for (int i = 0; i < 16; ++i) s += v[i];
  s = wave_sum(s);
  const float mean = s * (1.0f / DM);
  float q = 0.f;
#pragma unroll
  for (int i = 0; i < 16; ++i) { float d = v[i] - mean; q += d * d; }
  q = wave_sum(q);
  const float mb = rbf(mean);
  const float vb = rbf(q * (1.0f / DM));
  const float den = rbf(sqrtf(rbf(vb + 1e-5f)));
#pragma unroll
  for (int i = 0; i < 16; ++i) v[i] = rbf(rbf(v[i] - mb) / den);
}
__global__ __launch_bounds__(256) void k_ln(const u16* __restrict__ x, unsigned char* __restrict__ ws) {
  const int tid = threadIdx.x, lane = tid & 63, w = tid >> 6;
  const int row = blockIdx.x * 4 + w;
  {
    v4u s = {SENT, SENT, SENT, SENT};
    v4u* p = (v4u*)(ws + WS_HOP);
    const size_t n = HOP_BYTES / 16;
    for (size_t i = (size_t)blockIdx.x * 256 + tid; i < n; i += (size_t)gridDim.x * 256) p[i] = s;
    if (blockIdx.x == 0 && tid < 64) ((unsigned*)(ws + WS_ERR))[tid] = 0u;
  }
  const v4u* xr = (const v4u*)(x + (size_t)row * DM);
  v4u a = xr[lane], b = xr[64 + lane];
  float v[16];
  v[0] = bflo(a.x); v[1] = bfhi(a.x); v[2] = bflo(a.y); v[3] = bfhi(a.y);
  v[4] = bflo(a.z); v[5] = bfhi(a.z); v[6] = bflo(a.w); v[7] = bfhi(a.w);
  v[8] = bflo(b.x); v[9] = bfhi(b.x); v[10] = bflo(b.y); v[11] = bfhi(b.y);
  v[12] = bflo(b.z); v[13] = bfhi(b.z); v[14] = bflo(b.w); v[15] = bfhi(b.w);
  ln16(v);
  v4u* xn = (v4u*)(ws + WS_XN + (size_t)row * DM * 2);
  xn[lane] = v4u{pack2(v[0], v[1]), pack2(v[2], v[3]), pack2(v[4], v[5]), pack2(v[6], v[7])};
  xn[64 + lane] = v4u{pack2(v[8], v[9]), pack2(v[10], v[11]), pack2(v[12], v[13]), pack2(v[14], v[15])};
  ln16(v);
  v4u* y = (v4u*)(ws + WS_Y + (size_t)row * DM * 2);
  y[lane] = v4u{pack2(v[0], v[1]), pack2(v[2], v[3]), pack2(v[4], v[5]), pack2(v[6], v[7])};
  y[64 + lane] = v4u{pack2(v[8], v[9]), pack2(v[10], v[11]), pack2(v[12], v[13]), pack2(v[14], v[15])};
}

template <int BM> struct GemmCfg {
  static constexpr int BN = 128, BK = 64;
  static constexpr int A_BYTES = BM * BK * 2, B_BYTES = BK * BN * 2, ST_BYTES = A_BYTES + B_BYTES;
  static constexpr int LDS_BYTES = 3 * ST_BYTES;
};

template <int BM, int WM, int WN, bool TOUT, class Epi>
__device__ __forceinline__ void gemm_tile(const u16* __restrict__ A, int lda, const u16* __restrict__ B, int ldb, int K,
                                          int brow, int bcol, LAS char* lds, Epi epi) {
  using C = GemmCfg<BM>;
  constexpr int TM = BM / WM / 16, TN = C::BN / WN / 16;
  constexpr int AI = C::A_BYTES / 8192, BI = C::B_BYTES / 8192;
  const int tid = threadIdx.x, lane = tid & 63, w = __builtin_amdgcn_readfirstlane(tid >> 6);
  const int wr = w / WN, wc = w % WN, fr = lane & 15, fq = lane >> 4;
  const u16* asrc[AI];
  const u16* bsrc[BI];
#pragma unroll
  for (int i = 0; i < AI; ++i) {
    const int ii = w * AI + i, row = ii * 8 + (lane >> 3), ch = (lane & 7) ^ (row & 7);
    asrc[i] = A + (size_t)(brow + row) * lda + ch * 8;
  }
#pragma unroll
  for (int i = 0; i < BI; ++i) {
    const int ii = w * BI + i, row = ii * 4 + (lane >> 4), ch = (lane & 15) ^ (((row & 3) << 2) | ((row >> 2) & 3));
    bsrc[i] = B + (size_t)row * ldb + bcol + ch * 8;
  }
  auto stage = [&](int t, int buf) {
    LAS char* base = lds + buf * C::ST_BYTES;
#pragma unroll
    for (int i = 0; i < AI; ++i)
      __builtin_amdgcn_global_load_lds((const GAS void*)(asrc[i] + t * 64), (LAS void*)(base + (w * AI + i) * 1024), 16, 0, 0);
#pragma unroll
    for (int i = 0; i < BI; ++i)
      __builtin_amdgcn_global_load_lds((const GAS void*)(bsrc[i] + (size_t)t * 64 * ldb), (LAS void*)(base + C::A_BYTES + (w * BI + i) * 1024), 16, 0, 0);
  };
  int aoff[TM][2], boff[TN][2][2];
#pragma unroll
  for (int m = 0; m < TM; ++m)
#pragma unroll
    for (int k = 0; k < 2; ++k) {
      const int row = wr * (TM * 16) + m * 16 + fr, ch = k * 4 + fq;
      aoff[m][k] = row * 128 + ((ch ^ (row & 7)) * 16);
    }
  {
    const int q = fr >> 2, p = lane & 3;
#pragma unroll
    for (int n = 0; n < TN; ++n)
#pragma unroll
      for (int k = 0; k < 2; ++k)
#pragma unroll
        for (int t = 0; t < 2; ++t) {
          const int row = 32 * k + 8 * fq + 4 * t + q, c = wc * TN + n, ch = 2 * c + (p >> 1);
          boff[n][k][t] = C::A_BYTES + 256 * row + 16 * (ch ^ (((row & 3) << 2) | ((row >> 2) & 3))) + 8 * (p & 1);
        }
  }
  f32x4 acc[TM][TN];
#pragma unroll
  for (int m = 0; m < TM; ++m)
#pragma unroll
    for (int n = 0; n < TN; ++n) acc[m][n] = f32x4{0.f, 0.f, 0.f, 0.f};
  const int nt = K / 64;
  stage(0, 0);
  stage(1, 1);
  int rb = 0, sb = 2;
  for (int t = 0; t < nt; ++t) {
    if (t + 1 < nt) {
      if constexpr (AI + BI == 3) asm volatile("s_waitcnt vmcnt(3)" ::: "memory");
      else if constexpr (AI + BI == 4) asm volatile("s_waitcnt vmcnt(4)" ::: "memory");
      else asm volatile("s_waitcnt vmcnt(6)" ::: "memory");
    } else {
      asm volatile("s_waitcnt vmcnt(0)" ::: "memory");
    }
    asm volatile("s_waitcnt lgkmcnt(0)" ::: "memory");
    __builtin_amdgcn_s_barrier();
    asm volatile("" ::: "memory");
    if (t + 2 < nt) stage(t + 2, sb);
    LAS const char* base = lds + rb * C::ST_BYTES;
    v4u af[TM][2], bfr[TN][2];
#pragma unroll
    for (int k = 0; k < 2; ++k) {
#pragma unroll
      for (int n = 0; n < TN; ++n) {
        v2u lo = trread(base + boff[n][k][0]), hi = trread(base + boff[n][k][1]);
        bfr[n][k] = v4u{lo.x, lo.y, hi.x, hi.y};
      }
#pragma unroll
      for (int m = 0; m < TM; ++m) af[m][k] = *(LAS const v4u*)(base + aoff[m][k]);
    }
    __builtin_amdgcn_s_setprio(1);
#pragma unroll
    for (int k = 0; k < 2; ++k)
#pragma unroll
      for (int m = 0; m < TM; ++m)
#pragma unroll
        for (int n = 0; n < TN; ++n)
          acc[m][n] = TOUT ? mfma16(af[m][k], bfr[n][k], acc[m][n]) : mfma16(bfr[n][k], af[m][k], acc[m][n]);
    __builtin_amdgcn_s_setprio(0);
    rb = rb == 2 ? 0 : rb + 1;
    sb = sb == 2 ? 0 : sb + 1;
  }
#pragma unroll
  for (int m = 0; m < TM; ++m)
#pragma unroll
    for (int n = 0; n < TN; ++n) {
      const int r0 = brow + wr * (TM * 16) + m * 16, c0 = bcol + wc * (TN * 16) + n * 16;
      if (TOUT) epi(r0 + fq * 4, c0 + fr, acc[m][n]);
      else epi(r0 + fr, c0 + fq * 4, acc[m][n]);
    }
  asm volatile("s_waitcnt lgkmcnt(0)" ::: "memory");
  __builtin_amdgcn_s_barrier();
}

__device__ __forceinline__ float gelu_tanh(float x) {
  const float u = 0.7978845608028654f * (x + 0.044715f * x * x * x);
  const float e = __expf(2.0f * u);
  const float t = 1.0f - 2.0f / (e + 1.0f);
  return 0.5f * x * (1.0f + t);
}

template <int BM, int WM, int WN>
__device__ __forceinline__ void task_qkv(int rt, int ct, const u16* Wq, const u16* Wk, const u16* Wv, unsigned char* ws, LAS char* lds) {
  const u16* xn = (const u16*)(ws + WS_XN);
  const int which = ct >> 5, bcol = (ct & 31) * 128, brow = rt * BM;
  if (which < 2) {
    u16* out = (u16*)(ws + (which == 0 ? WS_Q : WS_K));
    gemm_tile<BM, WM, WN, false>(xn, DM, which == 0 ? Wq : Wk, HD, DM, brow, bcol, lds, [&](int r, int c, f32x4 v) {
      *(v2u*)(out + (size_t)r * HD + c) = v2u{pack2(v[0], v[1]), pack2(v[2], v[3])};
    });
  } else {
    u16* vt = (u16*)(ws + WS_VT);
    gemm_tile<BM, WM, WN, true>(xn, DM, Wv, HD, DM, brow, bcol, lds, [&](int r, int c, f32x4 v) {
      const int seg = r >> 5, t = r & 31, p = 8 * ((t & 15) >> 2) + 4 * (t >> 4);
      *(v2u*)(vt + ((size_t)seg * HD + c) * 32 + p) = v2u{pack2(v[0], v[1]), pack2(v[2], v[3])};
    });
  }
}
template <int BM, int WM, int WN>
__device__ __forceinline__ void task_mlp1(int rt, int ct, const u16* w1, const u16* b1, unsigned char* ws, LAS char* lds) {
  const u16* y = (const u16*)(ws + WS_Y);
  u16* h = (u16*)(ws + WS_H);
  gemm_tile<BM, WM, WN, false>(y, DM, w1, FF, DM, rt * BM, ct * 128, lds, [&](int r, int c, f32x4 v) {
    const v2u bb = *(const v2u*)(b1 + c);
    const float o0 = gelu_tanh(rbf(rbf(v[0]) + bflo(bb.x))), o1 = gelu_tanh(rbf(rbf(v[1]) + bfhi(bb.x)));
    const float o2 = gelu_tanh(rbf(rbf(v[2]) + bflo(bb.y))), o3 = gelu_tanh(rbf(rbf(v[3]) + bfhi(bb.y)));
    *(v2u*)(h + (size_t)r * FF + c) = v2u{pack2(o0, o1), pack2(o2, o3)};
  });
}
template <int BM, int WM, int WN>
__device__ __forceinline__ void task_mlp2(int rt, int ct, const u16* w2, const u16* b2, unsigned char* ws, LAS char* lds) {
  const u16* h = (const u16*)(ws + WS_H);
  u16* bo = (u16*)(ws + WS_BO);
  gemm_tile<BM, WM, WN, false>(h, FF, w2, DM, FF, rt * BM, ct * 128, lds, [&](int r, int c, f32x4 v) {
    const v2u bb = *(const v2u*)(b2 + c);
    *(v2u*)(bo + (size_t)r * DM + c) = v2u{pack2(rbf(v[0]) + bflo(bb.x), rbf(v[1]) + bfhi(bb.x)), pack2(rbf(v[2]) + bflo(bb.y), rbf(v[3]) + bfhi(bb.y))};
  });
}
template <int BM, int WM, int WN>
__device__ __forceinline__ void task_wo(int rt, int ct, const u16* Wo, const u16* x, u16* out, unsigned char* ws, LAS char* lds) {
  const u16* ot = (const u16*)(ws + WS_OT);
  const u16* bo = (const u16*)(ws + WS_BO);
  gemm_tile<BM, WM, WN, false>(ot, HD, Wo, DM, HD, rt * BM, ct * 128, lds, [&](int r, int c, f32x4 v) {
    const v2u xx = *(const v2u*)(x + (size_t)r * DM + c);
    const v2u bb = *(const v2u*)(bo + (size_t)r * DM + c);
    const float o0 = rbf(bflo(xx.x) + rbf(v[0])) + bflo(bb.x), o1 = rbf(bfhi(xx.x) + rbf(v[1])) + bfhi(bb.x);
    const float o2 = rbf(bflo(xx.y) + rbf(v[2])) + bflo(bb.y), o3 = rbf(bfhi(xx.y) + rbf(v[3])) + bfhi(bb.y);
    *(v2u*)(out + (size_t)r * DM + c) = v2u{pack2(o0, o1), pack2(o2, o3)};
  });
}

__global__ __launch_bounds__(512, 2) void k_qkv(const u16* Wq, const u16* Wk, const u16* Wv, unsigned char* ws) {
  extern __shared__ __attribute__((aligned(16))) char smem[];
  const int b = blockIdx.x;
  task_qkv<256, 4, 2>(b & 7, 32 + (b >> 3), Wq, Wk, Wv, ws, (LAS char*)smem);
}
__global__ __launch_bounds__(512, 2) void k_mlp1(const u16* w1, const u16* b1, unsigned char* ws) {
  extern __shared__ __attribute__((aligned(16))) char smem[];
  const int b = blockIdx.x;
  task_mlp1<256, 4, 2>(b & 7, b >> 3, w1, b1, ws, (LAS char*)smem);
}
__global__ __launch_bounds__(512, 2) void k_mlp2(const u16* w2, const u16* b2, unsigned char* ws) {
  extern __shared__ __attribute__((aligned(16))) char smem[];
  const int b = blockIdx.x;
  task_mlp2<64, 2, 4>(b & 31, b >> 5, w2, b2, ws, (LAS char*)smem);
}
__global__ __launch_bounds__(512, 2) void k_wo(const u16* Wo, const u16* x, u16* out, unsigned char* ws) {
  extern __shared__ __attribute__((aligned(16))) char smem[];
  const int b = blockIdx.x;
  task_wo<64, 2, 4>(b & 31, b >> 5, Wo, x, out, ws, (LAS char*)smem);
}

__device__ __forceinline__ bool rdy(v4u v) { return max(max(v.x, v.y), max(v.z, v.w)) != SENT; }
__device__ __forceinline__ v4u ld_sc1(__amdgpu_buffer_rsrc_t rs, int off) { return __builtin_amdgcn_raw_buffer_load_b128(rs, off, 0, 16); }

template <int N>
__device__ __forceinline__ void poll(__amdgpu_buffer_rsrc_t rs, int off, v4u (&d)[N], unsigned* err) {
  unsigned spins = 0;
  for (;;) {
    unsigned mx = 0;
#pragma unroll
    for (int i = 0; i < N; ++i) d[i] = ld_sc1(rs, off + i * 1024);
    asm volatile("" ::: "memory");
#pragma unroll
    for (int i = 0; i < N; ++i) mx = max(max(mx, max(d[i].x, d[i].y)), max(d[i].z, d[i].w));
    if (__all(mx != SENT)) break;
    if (++spins > SPINCAP) {
      if ((threadIdx.x & 63) == 0) __hip_atomic_store(err, 1u, __ATOMIC_RELAXED, __HIP_MEMORY_SCOPE_AGENT);
      break;
    }
    if ((spins & 1023u) == 0u && __hip_atomic_load(err, __ATOMIC_RELAXED, __HIP_MEMORY_SCOPE_AGENT) != 0u) break;
  }
}
__device__ __forceinline__ float xor16(float x) {
  auto r = __builtin_amdgcn_permlane16_swap(__builtin_bit_cast(unsigned, x), __builtin_bit_cast(unsigned, x), false, false);
  const int odd = (threadIdx.x >> 4) & 1;
  return __builtin_bit_cast(float, odd ? r[0] : r[1]);
}
__device__ __forceinline__ float xor32(float x) {
  auto r = __builtin_amdgcn_permlane32_swap(__builtin_bit_cast(unsigned, x), __builtin_bit_cast(unsigned, x), false, false);
  const int hi = (threadIdx.x >> 5) & 1;
  return __builtin_bit_cast(float, hi ? r[0] : r[1]);
}

constexpr int CH_RED = 0;
constexpr int CH_VM = 24576;
constexpr int CH_RED2 = 25088;
constexpr int CH_WV = 27136;
constexpr int CH_KV = CH_WV + 65536;
constexpr int CH_LDS = CH_KV + 20480;

__device__ __forceinline__ void chain_role(int c, const u16* __restrict__ mem0, const u16* __restrict__ Wq, const u16* __restrict__ Wk,
                                           const u16* __restrict__ Wv, const u16* __restrict__ Wo, unsigned char* ws, LAS char* lds) {
  const int tid = threadIdx.x, lane = tid & 63, w = __builtin_amdgcn_readfirstlane(tid >> 6);
  const int fr = lane & 15, g = lane >> 4;
  const int h = c >> 2, q4 = c & 3, mh = c & 1, nb = c >> 1;
  unsigned* err = (unsigned*)(ws + WS_ERR);
  const __amdgpu_buffer_rsrc_t rs = __builtin_amdgcn_make_buffer_rsrc((void*)(ws + WS_HOP), 0, (int)HOP_BYTES, 0x00020000);
  v4u wq[4][4], wo[16];
  LAS v4u* wvl = (LAS v4u*)(lds + CH_WV) + w * 512 + lane;
  {
    LAS char* stg = lds + CH_WV + w * 8192;
    const int q = fr >> 2, p = lane & 3;
#pragma unroll
    for (int hf = 0; hf < 2; ++hf) {
#pragma unroll
      for (int i = 0; i < 8; ++i) {
        const int row = i * 32 + (lane >> 1), ch = lane & 1;
        v4u v = *(const v4u*)(Wo + (size_t)(512 * w + 256 * hf + row) * DM + 16 * nb + ch * 8);
        *(LAS v4u*)(stg + row * 32 + ch * 16) = v;
      }
      __builtin_amdgcn_s_waitcnt(0xC07F);
#pragma unroll
      for (int ks = 0; ks < 8; ++ks) {
        v2u lo = trread(stg + (32 * ks + 8 * g + q) * 32 + (4 * p) * 2);
        v2u hi = trread(stg + (32 * ks + 8 * g + 4 + q) * 32 + (4 * p) * 2);
        wo[hf * 8 + ks] = v4u{lo.x, lo.y, hi.x, hi.y};
      }
      __builtin_amdgcn_s_waitcnt(0xC07F);
    }
#pragma unroll
    for (int mat = 0; mat < 3; ++mat) {
      const u16* W = mat == 0 ? Wq : (mat == 1 ? Wk : Wv);
#pragma unroll
      for (int i = 0; i < 8; ++i) {
        const int row = i * 16 + (lane >> 2), ch = lane & 3;
        v4u v = *(const v4u*)(W + (size_t)(128 * w + row) * HD + h * 128 + 32 * q4 + ch * 8);
        *(LAS v4u*)(stg + row * 64 + ch * 16) = v;
      }
      __builtin_amdgcn_s_waitcnt(0xC07F);
      v4u tv[8];
#pragma unroll
      for (int dt = 0; dt < 2; ++dt)
#pragma unroll
        for (int ks = 0; ks < 4; ++ks) {
          v2u lo = trread(stg + (32 * ks + 8 * g + q) * 64 + (16 * dt + 4 * p) * 2);
          v2u hi = trread(stg + (32 * ks + 8 * g + 4 + q) * 64 + (16 * dt + 4 * p) * 2);
          if (mat < 2) wq[(mat & 1) * 2 + dt][ks] = v4u{lo.x, lo.y, hi.x, hi.y};
          else tv[dt * 4 + ks] = v4u{lo.x, lo.y, hi.x, hi.y};
        }
      __builtin_amdgcn_s_waitcnt(0xC07F);
      asm volatile("" ::: "memory");
      if (mat == 2) {
#pragma unroll
        for (int i = 0; i < 8; ++i) wvl[i * 64] = tv[i];
      }
    }
  }
  const u16* Kx = (const u16*)(ws + WS_K);
  const u16* Vt = (const u16*)(ws + WS_VT);
  u16* VM = (u16*)(ws + WS_VM);
  const float scale = 0.08838834764831845f;
  const float L2E = 1.4426950408889634f;
  LAS f32x4* red = (LAS f32x4*)(lds + CH_RED);
  LAS f32x4* red2 = (LAS f32x4*)(lds + CH_RED2);
  LAS u16* vml = (LAS u16*)(lds + CH_VM);
  LAS v4u* kvl = (LAS v4u*)(lds + CH_KV) + lane;
  if (w == 2) {
    v4u t[10];
#pragma unroll
    for (int tt = 0; tt < 2; ++tt)
#pragma unroll
      for (int j = 0; j < 4; ++j) t[tt * 4 + j] = *(const v4u*)(Kx + (size_t)(16 * tt + fr) * HD + h * 128 + 32 * j + 8 * g);
#pragma unroll
    for (int dt = 0; dt < 2; ++dt) t[8 + dt] = *(const v4u*)(Vt + ((size_t)h * 128 + 32 * q4 + 16 * dt + fr) * 32 + 8 * g);
#pragma unroll
    for (int i = 0; i < 10; ++i) kvl[i * 64] = t[i];
  }

#define STAMP(i)
  for (int s = 0; s < NSEG; ++s) {
    v4u mf[2];
    if (s == 0) {
#pragma unroll
      for (int p = 0; p < 2; ++p)
        mf[p] = *(const v4u*)(mem0 + (size_t)(fr & 7) * DM + 128 * w + 32 * (2 * p + (fr >> 3)) + 8 * g);
    } else {
      poll<2>(rs, (int)(HOP_MEM + (size_t)s * 16384) + ((16 * w + 4 * (fr >> 3) + g) * 8 + (fr & 7)) * 16, mf, err);
    }
    STAMP(0)
    {
      f32x4 a0[4], a1[4];
#pragma unroll
      for (int t = 0; t < 4; ++t) {
        a0[t] = mfma16(wq[t][0], mf[0], f32x4{0.f, 0.f, 0.f, 0.f});
        a1[t] = mfma16(wq[t][1], mf[0], f32x4{0.f, 0.f, 0.f, 0.f});
      }
#pragma unroll
      for (int t = 0; t < 4; ++t) {
        a0[t] = mfma16(wq[t][2], mf[1], a0[t]);
        a1[t] = mfma16(wq[t][3], mf[1], a1[t]);
      }
#pragma unroll
      for (int t = 0; t < 4; ++t) {
        f32x4 x = (fr & 8) ? a1[t] : a0[t];
#pragma unroll
        for (int r = 0; r < 4; ++r) x[r] += row_ror<8>(x[r]);
        if (fr < 8) red[(w * 6 + t) * 32 + g * 8 + fr] = x;
      }
    }
    __syncthreads();
    STAMP(1)
    if (tid >= 384) {
      const int u = tid - 384;
      const int t6 = u >> 5, gg = (u >> 3) & 3, m = u & 7;
      f32x4 x = red[t6 * 32 + (u & 31)];
#pragma unroll
      for (int ww = 1; ww < 8; ++ww) x += red[(ww * 6 + t6) * 32 + (u & 31)];
      const unsigned lo = pack2(x[0], x[1]), hi = pack2(x[2], x[3]);
      const int mat = t6 >> 1, dt = t6 & 1;
      const int off = (int)(HOP_QK + ((size_t)s * NH + h) * 4096) + ((q4 * 2 + mat) * 8 + m) * 64 + (16 * dt + 4 * gg) * 2;
      __builtin_amdgcn_raw_buffer_store_b64(v2u{nosent(lo), nosent(hi)}, rs, off, 0, 16);
    }
    {
      f32x4 a0[2], a1[2];
#pragma unroll
      for (int t = 0; t < 2; ++t) {
        a0[t] = mfma16(wvl[(t * 4 + 0) * 64], mf[0], f32x4{0.f, 0.f, 0.f, 0.f});
        a1[t] = mfma16(wvl[(t * 4 + 1) * 64], mf[0], f32x4{0.f, 0.f, 0.f, 0.f});
      }
#pragma unroll
      for (int t = 0; t < 2; ++t) {
        a0[t] = mfma16(wvl[(t * 4 + 2) * 64], mf[1], a0[t]);
        a1[t] = mfma16(wvl[(t * 4 + 3) * 64], mf[1], a1[t]);
      }
#pragma unroll
      for (int t = 0; t < 2; ++t) {
        f32x4 x = (fr & 8) ? a1[t] : a0[t];
#pragma unroll
        for (int r = 0; r < 4; ++r) x[r] += row_ror<8>(x[r]);
        if (fr < 8) red[(w * 6 + 4 + t) * 32 + g * 8 + fr] = x;
      }
    }
    __syncthreads();
    STAMP(2)
    if (w == 1) {
      const int dt = lane >> 5, gg = (lane >> 3) & 3, m = lane & 7;
      f32x4 x = red[(4 + dt) * 32 + (lane & 31)];
#pragma unroll
      for (int ww = 1; ww < 8; ++ww) x += red[(ww * 6 + 4 + dt) * 32 + (lane & 31)];
      const unsigned lo = pack2(x[0], x[1]), hi = pack2(x[2], x[3]);
      const int d = 16 * dt + 4 * gg;
      vml[(d + 0) * 8 + m] = (u16)(lo & 0xFFFF);
      vml[(d + 1) * 8 + m] = (u16)(lo >> 16);
      vml[(d + 2) * 8 + m] = (u16)(hi & 0xFFFF);
      vml[(d + 3) * 8 + m] = (u16)(hi >> 16);
      __builtin_amdgcn_s_waitcnt(0xC07F);
      asm volatile("" ::: "memory");
      if (lane < 32)
        *(v4u*)(VM + (((size_t)s * NH + h) * 128 + 32 * q4 + lane) * 8) = *(LAS v4u*)(vml + lane * 8);
    }
    if (s == NSEG - 1) break;
    if (w == 2 && s + 1 < NSEG - 1) {
      v4u t[10];
      const int sn = s + 1;
#pragma unroll
      for (int tt = 0; tt < 2; ++tt)
#pragma unroll
        for (int j = 0; j < 4; ++j) t[tt * 4 + j] = *(const v4u*)(Kx + (size_t)(32 * sn + 16 * tt + fr) * HD + h * 128 + 32 * j + 8 * g);
#pragma unroll
      for (int dt = 0; dt < 2; ++dt) t[8 + dt] = *(const v4u*)(Vt + (((size_t)sn * NH + h) * 128 + 32 * q4 + 16 * dt + fr) * 32 + 8 * g);
#pragma unroll
      for (int i = 0; i < 10; ++i) kvl[((sn & 1) * 10 + i) * 64] = t[i];
    }
    v4u pt, pm;
    if (w == 0) {
      v4u X[8];
      {
        const int base = (int)(HOP_QK + ((size_t)s * NH + h) * 4096) + (fr & 7) * 64 + g * 16;
        unsigned spins = 0;
        for (;;) {
          unsigned mx = 0;
#pragma unroll
          for (int i = 0; i < 8; ++i) X[i] = ld_sc1(rs, base + i * 512);
          asm volatile("" ::: "memory");
#pragma unroll
          for (int i = 0; i < 8; ++i) mx = max(max(mx, max(X[i].x, X[i].y)), max(X[i].z, X[i].w));
          if (__all(mx != SENT)) break;
          if (++spins > SPINCAP) {
            if (lane == 0) __hip_atomic_store(err, 1u, __ATOMIC_RELAXED, __HIP_MEMORY_SCOPE_AGENT);
            break;
          }
          if ((spins & 1023u) == 0u && __hip_atomic_load(err, __ATOMIC_RELAXED, __HIP_MEMORY_SCOPE_AGENT) != 0u) break;
        }
      }
      STAMP(3)
      f32x4 s0 = {0.f, 0.f, 0.f, 0.f}, s1 = s0, s2 = s0;
#pragma unroll
      for (int j = 0; j < 4; ++j) {
        s1 = mfma16(kvl[((s & 1) * 10 + j) * 64], X[2 * j], s1);
        s2 = mfma16(kvl[((s & 1) * 10 + 4 + j) * 64], X[2 * j], s2);
        s0 = mfma16(X[2 * j + 1], X[2 * j], s0);
      }
      const bool up = fr >= 8;
      f32x4 st = up ? s2 : s1;
      const float m01 = up ? s0[1] : s0[0], m23 = up ? s0[3] : s0[2];
      const float sm = g >= 2 ? m23 : m01;
      unsigned p0 = pack2(st[0], st[1]), p1 = pack2(st[2], st[3]), p2 = pack2(sm, sm);
      p0 = pack2(bflo(p0) * scale, bfhi(p0) * scale);
      p1 = pack2(bflo(p1) * scale, bfhi(p1) * scale);
      p2 = pack2(bflo(p2) * scale, bflo(p2) * scale);
      float v0 = bflo(p0), v1 = bfhi(p0), v2 = bflo(p1), v3 = bfhi(p1), v4 = bflo(p2);
      float mx = fmaxf(fmaxf(fmaxf(v0, v1), fmaxf(v2, v3)), v4);
      mx = fmaxf(mx, row_ror<8>(mx));
      mx = fmaxf(mx, xor16(mx));
      mx = fmaxf(mx, xor32(mx));
      const float nm = -mx * L2E;
      v0 = __builtin_amdgcn_exp2f(__builtin_fmaf(v0, L2E, nm));
      v1 = __builtin_amdgcn_exp2f(__builtin_fmaf(v1, L2E, nm));
      v2 = __builtin_amdgcn_exp2f(__builtin_fmaf(v2, L2E, nm));
      v3 = __builtin_amdgcn_exp2f(__builtin_fmaf(v3, L2E, nm));
      v4 = __builtin_amdgcn_exp2f(__builtin_fmaf(v4, L2E, nm));
      float sum = ((v0 + v1) + (v2 + v3)) + v4;
      sum += row_ror<8>(sum);
      sum += xor16(sum);
      sum += xor32(sum);
      const float inv = __builtin_amdgcn_rcpf(sum);
      pt.x = pack2(v0 * inv, v1 * inv);
      pt.y = pack2(v2 * inv, v3 * inv);
      pt.z = (unsigned)__builtin_amdgcn_update_dpp(0, (int)pt.x, 0x128, 0xf, 0xf, false);
      pt.w = (unsigned)__builtin_amdgcn_update_dpp(0, (int)pt.y, 0x128, 0xf, 0xf, false);
      v4 *= inv;
      pm.x = pack2(v4, row_ror<8>(v4));
      pm.y = 0u; pm.z = 0u; pm.w = 0u;
    }
    __syncthreads();
    if (w == 0) {
      f32x4 o[2];
#pragma unroll
      for (int dt = 0; dt < 2; ++dt) o[dt] = mfma16(kvl[((s & 1) * 10 + 8 + dt) * 64], pt, f32x4{0.f, 0.f, 0.f, 0.f});
#pragma unroll
      for (int dt = 0; dt < 2; ++dt) {
        const unsigned t = *(LAS unsigned*)(vml + (16 * dt + fr) * 8 + 4 * (g & 1) + 2 * (g >> 1));
        o[dt] = mfma16(v4u{t, 0u, 0u, 0u}, pm, o[dt]);
      }
      if (fr < 8) {
#pragma unroll
        for (int dt = 0; dt < 2; ++dt) {
          const int k8 = h * 16 + 4 * q4 + 2 * dt + (g >> 1);
          const int off = (int)(HOP_OB + (size_t)s * 65536) + (((fr >> 2) * 512 + k8) * 4 + (fr & 3)) * 16 + (g & 1) * 8;
          __builtin_amdgcn_raw_buffer_store_b64(v2u{nosent(pack2(o[dt][0], o[dt][1])), nosent(pack2(o[dt][2], o[dt][3]))}, rs, off, 0, 16);
        }
      }
    }
    __syncthreads();
    STAMP(4)
    {
      v4u of[4];
      poll<4>(rs, (int)(HOP_OB + (size_t)s * 65536) + ((mh * 512 + 64 * w + 4 * (fr >> 2) + g) * 4 + (fr & 3)) * 16, of, err);
      STAMP(5)
      f32x4 a[4];
#pragma unroll
      for (int r4 = 0; r4 < 4; ++r4) a[r4] = mfma16(wo[r4], of[0], f32x4{0.f, 0.f, 0.f, 0.f});
#pragma unroll
      for (int i = 1; i < 4; ++i)
#pragma unroll
        for (int r4 = 0; r4 < 4; ++r4) a[r4] = mfma16(wo[4 * i + r4], of[i], a[r4]);
      const int r4 = fr >> 2;
      f32x4 x = r4 == 0 ? a[0] : (r4 == 1 ? a[1] : (r4 == 2 ? a[2] : a[3]));
#pragma unroll
      for (int r = 0; r < 4; ++r) { x[r] += row_ror<8>(x[r]); x[r] += row_ror<4>(x[r]); }
      if (fr < 4) red2[w * 16 + g * 4 + fr] = x;
    }
    __syncthreads();
    if (tid < 16) {
      f32x4 x = red2[tid];
#pragma unroll
      for (int ww = 1; ww < 8; ++ww) x += red2[ww * 16 + tid];
      const int gg = tid >> 2, m4 = tid & 3;
      const int k8 = 2 * nb + (gg >> 1);
      const int off = (int)(HOP_MEM + (size_t)(s + 1) * 16384) + (k8 * 8 + 4 * mh + m4) * 16 + (gg & 1) * 8;
      __builtin_amdgcn_raw_buffer_store_b64(v2u{nosent(pack2(x[0], x[1])), nosent(pack2(x[2], x[3]))}, rs, off, 0, 16);
    }
    STAMP(6)
  }
}

__device__ __forceinline__ void grp_signal(unsigned* ctr) {
  asm volatile("s_waitcnt vmcnt(0)" ::: "memory");
  __syncthreads();
  if (threadIdx.x == 0) {
    __builtin_amdgcn_fence(__ATOMIC_RELEASE, "agent");
    asm volatile("s_waitcnt vmcnt(0)" ::: "memory");
    __hip_atomic_fetch_add(ctr, 1u, __ATOMIC_RELAXED, __HIP_MEMORY_SCOPE_AGENT);
  }
}
__device__ __forceinline__ void grp_wait(unsigned* ctr, unsigned want, unsigned* err) {
  if (threadIdx.x == 0) {
    unsigned spins = 0;
    while (__hip_atomic_load(ctr, __ATOMIC_RELAXED, __HIP_MEMORY_SCOPE_AGENT) < want) {
      __builtin_amdgcn_s_sleep(8);
      if (++spins > SPINCAP) { __hip_atomic_store(err, 1u, __ATOMIC_RELAXED, __HIP_MEMORY_SCOPE_AGENT); break; }
      if ((spins & 1023u) == 0u && __hip_atomic_load(err, __ATOMIC_RELAXED, __HIP_MEMORY_SCOPE_AGENT) != 0u) break;
    }
    __builtin_amdgcn_fence(__ATOMIC_ACQUIRE, "agent");
    asm volatile("s_waitcnt vmcnt(0)" ::: "memory");
  }
  __syncthreads();
}

__global__ __launch_bounds__(512, 2) void k_mega(const u16* mem0, const u16* Wq, const u16* Wk, const u16* Wv, const u16* Wo,
                                                 const u16* w1, const u16* b1, const u16* w2, const u16* b2, unsigned char* ws) {
  extern __shared__ __attribute__((aligned(16))) char smem[];
  const int b = blockIdx.x;
  const int xcd = b & 7, i = b >> 4;
  if (((b >> 3) & 1) == 0) {
    const int c = ((xcd * 4 + (i >> 2)) << 2) | (i & 3);
    chain_role(c, mem0, Wq, Wk, Wv, Wo, ws, (LAS char*)smem);
  } else {
    const int gi = i * 8 + xcd;
    unsigned* err = (unsigned*)(ws + WS_ERR);
    unsigned* ctr = err + 16;
    for (int t = gi; t < 8 * 28; t += 128) task_mlp1<256, 4, 2>(t & 7, t >> 3, w1, b1, ws, (LAS char*)smem);
    grp_signal(ctr);
    for (int t = gi; t < 8 * 32; t += 128) task_qkv<256, 4, 2>(t & 7, t >> 3, Wq, Wk, Wv, ws, (LAS char*)smem);
    grp_wait(ctr, 128u, err);
    for (int t = gi; t < 32 * 8; t += 128) task_mlp2<64, 2, 4>(t & 31, t >> 5, w2, b2, ws, (LAS char*)smem);
  }
}

__device__ __forceinline__ void attn_unit(int s, int h, int lane, unsigned char* ws) {
  const int fr = lane & 15, g = lane >> 4;
  const u16* Q = (const u16*)(ws + WS_Q);
  const u16* Kx = (const u16*)(ws + WS_K);
  const u16* Vt = (const u16*)(ws + WS_VT);
  const u16* VM = (const u16*)(ws + WS_VM);
  const u16* QK = (const u16*)(ws + WS_HOP + HOP_QK + ((size_t)s * NH + h) * 4096);
  u16* OT = (u16*)(ws + WS_OT);
  const float scale = 0.08838834764831845f;
  f32x4 sc[3][2];
#pragma unroll
  for (int a = 0; a < 3; ++a)
#pragma unroll
    for (int b = 0; b < 2; ++b) sc[a][b] = f32x4{0.f, 0.f, 0.f, 0.f};
#pragma unroll
  for (int j = 0; j < 4; ++j) {
    v4u qf[2], kf[3];
#pragma unroll
    for (int t = 0; t < 2; ++t) {
      qf[t] = *(const v4u*)(Q + (size_t)(32 * s + 16 * t + fr) * HD + h * 128 + 32 * j + 8 * g);
      kf[1 + t] = *(const v4u*)(Kx + (size_t)(32 * s + 16 * t + fr) * HD + h * 128 + 32 * j + 8 * g);
    }
    kf[0] = *(const v4u*)(QK + ((j * 2 + 1) * 8 + (fr & 7)) * 32 + g * 8);
#pragma unroll
    for (int a = 0; a < 3; ++a)
#pragma unroll
      for (int b = 0; b < 2; ++b) sc[a][b] = mfma16(kf[a], qf[b], sc[a][b]);
  }
  v4u pt[2], pm[2];
#pragma unroll
  for (int b = 0; b < 2; ++b) {
    const int qi = 16 * b + fr;
    float v[12];
    bool ok[12];
#pragma unroll
    for (int r = 0; r < 4; ++r) {
      v[r] = rbf(rbf(sc[0][b][r]) * scale); ok[r] = g < 2;
      v[4 + r] = rbf(rbf(sc[1][b][r]) * scale); ok[4 + r] = (4 * g + r) <= qi;
      v[8 + r] = rbf(rbf(sc[2][b][r]) * scale); ok[8 + r] = (16 + 4 * g + r) <= qi;
    }
    float mx = -3.0e38f;
#pragma unroll
    for (int i = 0; i < 12; ++i) mx = ok[i] ? fmaxf(mx, v[i]) : mx;
    mx = fmaxf(mx, __shfl_xor(mx, 16));
    mx = fmaxf(mx, __shfl_xor(mx, 32));
    float sum = 0.f;
#pragma unroll
    for (int i = 0; i < 12; ++i) { v[i] = ok[i] ? __expf(v[i] - mx) : 0.f; sum += v[i]; }
    sum += __shfl_xor(sum, 16);
    sum += __shfl_xor(sum, 32);
    const float inv = 1.0f / sum;
    pt[b].x = pack2(v[4] * inv, v[5] * inv); pt[b].y = pack2(v[6] * inv, v[7] * inv);
    pt[b].z = pack2(v[8] * inv, v[9] * inv); pt[b].w = pack2(v[10] * inv, v[11] * inv);
    pm[b].x = pack2(v[0] * inv, v[1] * inv); pm[b].y = pack2(v[2] * inv, v[3] * inv); pm[b].z = 0u; pm[b].w = 0u;
  }
#pragma unroll
  for (int dt = 0; dt < 8; ++dt) {
    v4u vtf = *(const v4u*)(Vt + (((size_t)s * NH + h) * 128 + 16 * dt + fr) * 32 + 8 * g);
    v4u vmf = {0u, 0u, 0u, 0u};
    if (g < 2) {
      v2u t = *(const v2u*)(VM + (((size_t)s * NH + h) * 128 + 16 * dt + fr) * 8 + 4 * g);
      vmf.x = t.x; vmf.y = t.y;
    }
#pragma unroll
    for (int b = 0; b < 2; ++b) {
      f32x4 o = {0.f, 0.f, 0.f, 0.f};
      o = mfma16(vtf, pt[b], o);
      o = mfma16(vmf, pm[b], o);
      *(v2u*)(OT + (size_t)(32 * s + 16 * b + fr) * HD + h * 128 + 16 * dt + 4 * g) = v2u{pack2(o[0], o[1]), pack2(o[2], o[3])};
    }
  }
}
__global__ __launch_bounds__(256) void k_attn(unsigned char* ws) {
  const int u = blockIdx.x * 4 + (threadIdx.x >> 6);
  attn_unit(u >> 5, u & 31, threadIdx.x & 63, ws);
}

extern "C" void kernel_launch(void* const* d_in, const int* in_sizes, int n_in, void* d_out, int out_size, void* d_ws, size_t ws_size,
                              hipStream_t stream) {
  const u16* x = (const u16*)d_in[0];
  const u16* mem0 = (const u16*)d_in[1];
  const u16* Wq = (const u16*)d_in[2];
  const u16* Wk = (const u16*)d_in[3];
  const u16* Wv = (const u16*)d_in[4];
  const u16* Wo = (const u16*)d_in[5];
  const u16* w1 = (const u16*)d_in[8];
  const u16* b1 = (const u16*)d_in[9];
  const u16* w2 = (const u16*)d_in[10];
  const u16* b2 = (const u16*)d_in[11];
  unsigned char* ws = (unsigned char*)d_ws;
  u16* out = (u16*)d_out;
  constexpr int L256 = GemmCfg<256>::LDS_BYTES, L64 = GemmCfg<64>::LDS_BYTES;
  (void)hipFuncSetAttribute((const void*)k_qkv, hipFuncAttributeMaxDynamicSharedMemorySize, L256);
  (void)hipFuncSetAttribute((const void*)k_mlp1, hipFuncAttributeMaxDynamicSharedMemorySize, L256);
  (void)hipFuncSetAttribute((const void*)k_mlp2, hipFuncAttributeMaxDynamicSharedMemorySize, L64);
  (void)hipFuncSetAttribute((const void*)k_wo, hipFuncAttributeMaxDynamicSharedMemorySize, L64);
  constexpr int LMEGA = L256 > CH_LDS ? L256 : CH_LDS;
  (void)hipFuncSetAttribute((const void*)k_mega, hipFuncAttributeMaxDynamicSharedMemorySize, LMEGA);
  k_ln<<<S_LEN / 4, 256, 0, stream>>>(x, ws);
  k_qkv<<<8 * 64, 512, L256, stream>>>(Wq, Wk, Wv, ws);
  k_mega<<<256, 512, LMEGA, stream>>>(mem0, Wq, Wk, Wv, Wo, w1, b1, w2, b2, ws);
  k_attn<<<NSEG * NH / 4, 256, 0, stream>>>(ws);
  k_wo<<<32 * 8, 512, L64, stream>>>(Wo, x, out, ws);
}
```

```cpp
#include <hip/hip_runtime.h>
#include <hip/hip_bf16.h>
#include <stdint.h>

constexpr int S_LEN = 2048, DM = 1024, NH = 32, DH = 128, HD = NH * DH;
constexpr int MEMN = 8, SEG = 32, NSEG = S_LEN / SEG;
constexpr int FF = 3584;

typedef unsigned v4u __attribute__((ext_vector_type(4)));
typedef unsigned v2u __attribute__((ext_vector_type(2)));
typedef float f32x4 __attribute__((ext_vector_type(4)));
typedef float f32x2 __attribute__((ext_vector_type(2)));
typedef short bf16x8 __attribute__((ext_vector_type(8)));
typedef short s16x4 __attribute__((ext_vector_type(4)));
typedef __bf16 bfx2 __attribute__((ext_vector_type(2)));
typedef unsigned short u16;
#define LAS __attribute__((address_space(3)))
#define GAS __attribute__((address_space(1)))

constexpr size_t WS_XN = 0;
constexpr size_t WS_Y = WS_XN + (size_t)S_LEN * DM * 2;
constexpr size_t WS_Q = WS_Y + (size_t)S_LEN * DM * 2;
constexpr size_t WS_K = WS_Q + (size_t)S_LEN * HD * 2;
constexpr size_t WS_VT = WS_K + (size_t)S_LEN * HD * 2;
constexpr size_t WS_H = WS_VT + (size_t)S_LEN * HD * 2;
constexpr size_t WS_BO = WS_H + (size_t)S_LEN * FF * 2;
constexpr size_t WS_OT = WS_BO + (size_t)S_LEN * DM * 2;
constexpr size_t WS_HOP = WS_OT + (size_t)S_LEN * HD * 2;
constexpr size_t HOP_MEM = 0;
constexpr size_t HOP_QK = HOP_MEM + (size_t)NSEG * 16384;
constexpr size_t HOP_OB = HOP_QK + (size_t)NSEG * NH * 4096;
constexpr size_t HOP_VM = HOP_OB + (size_t)NSEG * 65536;
constexpr size_t HOP_BYTES = HOP_VM + (size_t)NSEG * HD * MEMN * 2;
constexpr size_t WS_ERR = WS_HOP + HOP_BYTES;
constexpr size_t WS_TOTAL = WS_ERR + 8192;
constexpr int CW_ERR = 0, CW_MLP1 = 16, CW_Q = 32, CW_MLP2 = 48, CW_KV = 64  , CW_AT = 64 + 512  ;

constexpr unsigned SENT = 0xFFFFFFFFu;
constexpr unsigned SPINCAP = 1u << 20;

__device__ __forceinline__ float bf2f(unsigned b16) { return __builtin_bit_cast(float, b16 << 16); }
__device__ __forceinline__ float bflo(unsigned x) { return __builtin_bit_cast(float, x << 16); }
__device__ __forceinline__ float bfhi(unsigned x) { return __builtin_bit_cast(float, x & 0xFFFF0000u); }
__device__ __forceinline__ unsigned pack2(float a, float b) {
  f32x2 v = {a, b};
  bfx2 r = __builtin_convertvector(v, bfx2);
  return __builtin_bit_cast(unsigned, r);
}
__device__ __forceinline__ float rbf(float a) { return (float)(__bf16)a; }
__device__ __forceinline__ unsigned nosent(unsigned x) { return x == SENT ? 0xFFFFFFFEu : x; }
__device__ __forceinline__ f32x4 mfma16(v4u a, v4u b, f32x4 c) {
  return __builtin_amdgcn_mfma_f32_16x16x32_bf16(__builtin_bit_cast(bf16x8, a), __builtin_bit_cast(bf16x8, b), c, 0, 0, 0);
}
__device__ __forceinline__ v2u trread(LAS const char* p) {
  typedef short v4i16_t __attribute__((ext_vector_type(4)));
  return __builtin_bit_cast(v2u, __builtin_amdgcn_ds_read_tr16_b64_v4i16((LAS v4i16_t*)p));
}
__device__ __forceinline__ float ror(float x, int) = delete;
template <int N> __device__ __forceinline__ float row_ror(float x) {
  return __builtin_bit_cast(float, __builtin_amdgcn_update_dpp(0, __builtin_bit_cast(int, x), 0x120 + N, 0xf, 0xf, false));
}

__device__ __forceinline__ float wave_sum(float v) {
#pragma unroll
  for (int o = 32; o > 0; o >>= 1) v += __shfl_xor(v, o);
  return v;
}
__device__ __forceinline__ void ln16(float (&v)[16]) {
  float s = 0.f;
#pragma unroll
  for (int i = 0; i < 16; ++i) s += v[i];
  s = wave_sum(s);
  const float mean = s * (1.0f / DM);
  float q = 0.f;
#pragma unroll
  for (int i = 0; i < 16; ++i) { float d = v[i] - mean; q += d * d; }
  q = wave_sum(q);
  const float mb = rbf(mean);
  const float vb = rbf(q * (1.0f / DM));
  const float den = rbf(sqrtf(rbf(vb + 1e-5f)));
#pragma unroll
  for (int i = 0; i < 16; ++i) v[i] = rbf(rbf(v[i] - mb) / den);
}
__global__ __launch_bounds__(256) void k_ln(const u16* __restrict__ x, unsigned char* __restrict__ ws) {
  const int tid = threadIdx.x, lane = tid & 63, w = tid >> 6;
  const int row = blockIdx.x * 4 + w;
  {
    v4u s = {SENT, SENT, SENT, SENT};
    v4u* p = (v4u*)(ws + WS_HOP);
    const size_t n = HOP_BYTES / 16;
    for (size_t i = (size_t)blockIdx.x * 256 + tid; i < n; i += (size_t)gridDim.x * 256) p[i] = s;
    if (blockIdx.x < 8) ((unsigned*)(ws + WS_ERR))[blockIdx.x * 256 + tid] = 0u;
  }
  const v4u* xr = (const v4u*)(x + (size_t)row * DM);
  v4u a = xr[lane], b = xr[64 + lane];
  float v[16];
  v[0] = bflo(a.x); v[1] = bfhi(a.x); v[2] = bflo(a.y); v[3] = bfhi(a.y);
  v[4] = bflo(a.z); v[5] = bfhi(a.z); v[6] = bflo(a.w); v[7] = bfhi(a.w);
  v[8] = bflo(b.x); v[9] = bfhi(b.x); v[10] = bflo(b.y); v[11] = bfhi(b.y);
  v[12] = bflo(b.z); v[13] = bfhi(b.z); v[14] = bflo(b.w); v[15] = bfhi(b.w);
  ln16(v);
  v4u* xn = (v4u*)(ws + WS_XN + (size_t)row * DM * 2);
  xn[lane] = v4u{pack2(v[0], v[1]), pack2(v[2], v[3]), pack2(v[4], v[5]), pack2(v[6], v[7])};
  xn[64 + lane] = v4u{pack2(v[8], v[9]), pack2(v[10], v[11]), pack2(v[12], v[13]), pack2(v[14], v[15])};
  ln16(v);
  v4u* y = (v4u*)(ws + WS_Y + (size_t)row * DM * 2);
  y[lane] = v4u{pack2(v[0], v[1]), pack2(v[2], v[3]), pack2(v[4], v[5]), pack2(v[6], v[7])};
  y[64 + lane] = v4u{pack2(v[8], v[9]), pack2(v[10], v[11]), pack2(v[12], v[13]), pack2(v[14], v[15])};
}

template <int BM> struct GemmCfg {
  static constexpr int BN = 128, BK = 64;
  static constexpr int A_BYTES = BM * BK * 2, B_BYTES = BK * BN * 2, ST_BYTES = A_BYTES + B_BYTES;
  static constexpr int LDS_BYTES = 3 * ST_BYTES;
};

template <int BM, int WM, int WN, bool TOUT, class Epi>
__device__ __forceinline__ void gemm_tile(const u16* __restrict__ A, int lda, const u16* __restrict__ B, int ldb, int K,
                                          int brow, int bcol, LAS char* lds, Epi epi) {
  using C = GemmCfg<BM>;
  constexpr int TM = BM / WM / 16, TN = C::BN / WN / 16;
  constexpr int AI = C::A_BYTES / 8192, BI = C::B_BYTES / 8192;
  const int tid = threadIdx.x, lane = tid & 63, w = __builtin_amdgcn_readfirstlane(tid >> 6);
  const int wr = w / WN, wc = w % WN, fr = lane & 15, fq = lane >> 4;
  const u16* asrc[AI];
  const u16* bsrc[BI];
#pragma unroll
  for (int i = 0; i < AI; ++i) {
    const int ii = w * AI + i, row = ii * 8 + (lane >> 3), ch = (lane & 7) ^ (row & 7);
    asrc[i] = A + (size_t)(brow + row) * lda + ch * 8;
  }
#pragma unroll
  for (int i = 0; i < BI; ++i) {
    const int ii = w * BI + i, row = ii * 4 + (lane >> 4), ch = (lane & 15) ^ (((row & 3) << 2) | ((row >> 2) & 3));
    bsrc[i] = B + (size_t)row * ldb + bcol + ch * 8;
  }
  auto stage = [&](int t, int buf) {
    LAS char* base = lds + buf * C::ST_BYTES;
#pragma unroll
    for (int i = 0; i < AI; ++i)
      __builtin_amdgcn_global_load_lds((const GAS void*)(asrc[i] + t * 64), (LAS void*)(base + (w * AI + i) * 1024), 16, 0, 0);
#pragma unroll
    for (int i = 0; i < BI; ++i)
      __builtin_amdgcn_global_load_lds((const GAS void*)(bsrc[i] + (size_t)t * 64 * ldb), (LAS void*)(base + C::A_BYTES + (w * BI + i) * 1024), 16, 0, 0);
  };
  int aoff[TM][2], boff[TN][2][2];
#pragma unroll
  for (int m = 0; m < TM; ++m)
#pragma unroll
    for (int k = 0; k < 2; ++k) {
      const int row = wr * (TM * 16) + m * 16 + fr, ch = k * 4 + fq;
      aoff[m][k] = row * 128 + ((ch ^ (row & 7)) * 16);
    }
  {
    const int q = fr >> 2, p = lane & 3;
#pragma unroll
    for (int n = 0; n < TN; ++n)
#pragma unroll
      for (int k = 0; k < 2; ++k)
#pragma unroll
        for (int t = 0; t < 2; ++t) {
          const int row = 32 * k + 8 * fq + 4 * t + q, c = wc * TN + n, ch = 2 * c + (p >> 1);
          boff[n][k][t] = C::A_BYTES + 256 * row + 16 * (ch ^ (((row & 3) << 2) | ((row >> 2) & 3))) + 8 * (p & 1);
        }
  }
  f32x4 acc[TM][TN];
#pragma unroll
  for (int m = 0; m < TM; ++m)
#pragma unroll
    for (int n = 0; n < TN; ++n) acc[m][n] = f32x4{0.f, 0.f, 0.f, 0.f};
  const int nt = K / 64;
  stage(0, 0);
  stage(1, 1);
  int rb = 0, sb = 2;
  for (int t = 0; t < nt; ++t) {
    if (t + 1 < nt) {
      if constexpr (AI + BI == 3) asm volatile("s_waitcnt vmcnt(3)" ::: "memory");
      else if constexpr (AI + BI == 4) asm volatile("s_waitcnt vmcnt(4)" ::: "memory");
      else asm volatile("s_waitcnt vmcnt(6)" ::: "memory");
    } else {
      asm volatile("s_waitcnt vmcnt(0)" ::: "memory");
    }
    asm volatile("s_waitcnt lgkmcnt(0)" ::: "memory");
    __builtin_amdgcn_s_barrier();
    asm volatile("" ::: "memory");
    if (t + 2 < nt) stage(t + 2, sb);
    LAS const char* base = lds + rb * C::ST_BYTES;
    v4u af[TM][2], bfr[TN][2];
#pragma unroll
    for (int k = 0; k < 2; ++k) {
#pragma unroll
      for (int n = 0; n < TN; ++n) {
        v2u lo = trread(base + boff[n][k][0]), hi = trread(base + boff[n][k][1]);
        bfr[n][k] = v4u{lo.x, lo.y, hi.x, hi.y};
      }
#pragma unroll
      for (int m = 0; m < TM; ++m) af[m][k] = *(LAS const v4u*)(base + aoff[m][k]);
    }
    __builtin_amdgcn_s_setprio(1);
#pragma unroll
    for (int k = 0; k < 2; ++k)
#pragma unroll
      for (int m = 0; m < TM; ++m)
#pragma unroll
        for (int n = 0; n < TN; ++n)
          acc[m][n] = TOUT ? mfma16(af[m][k], bfr[n][k], acc[m][n]) : mfma16(bfr[n][k], af[m][k], acc[m][n]);
    __builtin_amdgcn_s_setprio(0);
    rb = rb == 2 ? 0 : rb + 1;
    sb = sb == 2 ? 0 : sb + 1;
  }
#pragma unroll
  for (int m = 0; m < TM; ++m)
#pragma unroll
    for (int n = 0; n < TN; ++n) {
      const int r0 = brow + wr * (TM * 16) + m * 16, c0 = bcol + wc * (TN * 16) + n * 16;
      if (TOUT) epi(r0 + fq * 4, c0 + fr, acc[m][n]);
      else epi(r0 + fr, c0 + fq * 4, acc[m][n]);
    }
  asm volatile("s_waitcnt lgkmcnt(0)" ::: "memory");
  __builtin_amdgcn_s_barrier();
}

__device__ __forceinline__ void wt8(unsigned char* ws, size_t off, v2u v) {
  const __amdgpu_buffer_rsrc_t rs = __builtin_amdgcn_make_buffer_rsrc((void*)ws, 0, (int)WS_TOTAL, 0x00020000);
  __builtin_amdgcn_raw_buffer_store_b64(v, rs, (int)off, 0, 16);
}
__device__ __forceinline__ float gelu_tanh(float x) {
  const float u = 0.7978845608028654f * (x + 0.044715f * x * x * x);
  const float e = __expf(2.0f * u);
  const float t = 1.0f - 2.0f / (e + 1.0f);
  return 0.5f * x * (1.0f + t);
}

template <int BM, int WM, int WN>
__device__ __forceinline__ void task_qkv(int rt, int ct, const u16* Wq, const u16* Wk, const u16* Wv, unsigned char* ws, LAS char* lds) {
  const u16* xn = (const u16*)(ws + WS_XN);
  const int which = ct >> 5, bcol = (ct & 31) * 128, brow = rt * BM;
  if (which < 2) {
    const size_t ob = which == 0 ? WS_Q : WS_K;
    gemm_tile<BM, WM, WN, false>(xn, DM, which == 0 ? Wq : Wk, HD, DM, brow, bcol, lds, [&](int r, int c, f32x4 v) {
      wt8(ws, ob + ((size_t)r * HD + c) * 2, v2u{pack2(v[0], v[1]), pack2(v[2], v[3])});
    });
  } else {
    gemm_tile<BM, WM, WN, true>(xn, DM, Wv, HD, DM, brow, bcol, lds, [&](int r, int c, f32x4 v) {
      const int seg = r >> 5, t = r & 31, p = 8 * ((t & 15) >> 2) + 4 * (t >> 4);
      wt8(ws, WS_VT + (((size_t)seg * HD + c) * 32 + p) * 2, v2u{pack2(v[0], v[1]), pack2(v[2], v[3])});
    });
  }
}
template <int BM, int WM, int WN>
__device__ __forceinline__ void task_mlp1(int rt, int ct, const u16* w1, const u16* b1, unsigned char* ws, LAS char* lds) {
  const u16* y = (const u16*)(ws + WS_Y);
  gemm_tile<BM, WM, WN, false>(y, DM, w1, FF, DM, rt * BM, ct * 128, lds, [&](int r, int c, f32x4 v) {
    const v2u bb = *(const v2u*)(b1 + c);
    const float o0 = gelu_tanh(rbf(rbf(v[0]) + bflo(bb.x))), o1 = gelu_tanh(rbf(rbf(v[1]) + bfhi(bb.x)));
    const float o2 = gelu_tanh(rbf(rbf(v[2]) + bflo(bb.y))), o3 = gelu_tanh(rbf(rbf(v[3]) + bfhi(bb.y)));
    wt8(ws, WS_H + ((size_t)r * FF + c) * 2, v2u{pack2(o0, o1), pack2(o2, o3)});
  });
}
template <int BM, int WM, int WN>
__device__ __forceinline__ void task_mlp2(int rt, int ct, const u16* w2, const u16* b2, unsigned char* ws, LAS char* lds) {
  const u16* h = (const u16*)(ws + WS_H);
  gemm_tile<BM, WM, WN, false>(h, FF, w2, DM, FF, rt * BM, ct * 128, lds, [&](int r, int c, f32x4 v) {
    const v2u bb = *(const v2u*)(b2 + c);
    wt8(ws, WS_BO + ((size_t)r * DM + c) * 2, v2u{pack2(rbf(v[0]) + bflo(bb.x), rbf(v[1]) + bfhi(bb.x)), pack2(rbf(v[2]) + bflo(bb.y), rbf(v[3]) + bfhi(bb.y))});
  });
}
template <int BM, int WM, int WN>
__device__ __forceinline__ void task_wo(int rt, int ct, const u16* Wo, const u16* x, u16* out, unsigned char* ws, LAS char* lds) {
  const u16* ot = (const u16*)(ws + WS_OT);
  const u16* bo = (const u16*)(ws + WS_BO);
  gemm_tile<BM, WM, WN, false>(ot, HD, Wo, DM, HD, rt * BM, ct * 128, lds, [&](int r, int c, f32x4 v) {
    const v2u xx = *(const v2u*)(x + (size_t)r * DM + c);
    const v2u bb = *(const v2u*)(bo + (size_t)r * DM + c);
    const float o0 = rbf(bflo(xx.x) + rbf(v[0])) + bflo(bb.x), o1 = rbf(bfhi(xx.x) + rbf(v[1])) + bfhi(bb.x);
    const float o2 = rbf(bflo(xx.y) + rbf(v[2])) + bflo(bb.y), o3 = rbf(bfhi(xx.y) + rbf(v[3])) + bfhi(bb.y);
    *(v2u*)(out + (size_t)r * DM + c) = v2u{pack2(o0, o1), pack2(o2, o3)};
  });
}

__device__ __forceinline__ bool rdy(v4u v) { return max(max(v.x, v.y), max(v.z, v.w)) != SENT; }
__device__ __forceinline__ v4u ld_sc1(__amdgpu_buffer_rsrc_t rs, int off) { return __builtin_amdgcn_raw_buffer_load_b128(rs, off, 0, 16); }

template <int N>
__device__ __forceinline__ void poll(__amdgpu_buffer_rsrc_t rs, int off, v4u (&d)[N], unsigned* err) {
  unsigned spins = 0;
  for (;;) {
    unsigned mx = 0;
#pragma unroll
    for (int i = 0; i < N; ++i) d[i] = ld_sc1(rs, off + i * 1024);
    asm volatile("" ::: "memory");
#pragma unroll
    for (int i = 0; i < N; ++i) mx = max(max(mx, max(d[i].x, d[i].y)), max(d[i].z, d[i].w));
    if (__all(mx != SENT)) break;
    if (++spins > SPINCAP) {
      if ((threadIdx.x & 63) == 0) __hip_atomic_store(err, 1u, __ATOMIC_RELAXED, __HIP_MEMORY_SCOPE_AGENT);
      break;
    }
    if ((spins & 1023u) == 0u && __hip_atomic_load(err, __ATOMIC_RELAXED, __HIP_MEMORY_SCOPE_AGENT) != 0u) break;
  }
}
__device__ __forceinline__ float xor16(float x) {
  auto r = __builtin_amdgcn_permlane16_swap(__builtin_bit_cast(unsigned, x), __builtin_bit_cast(unsigned, x), false, false);
  const int odd = (threadIdx.x >> 4) & 1;
  return __builtin_bit_cast(float, odd ? r[0] : r[1]);
}
__device__ __forceinline__ float xor32(float x) {
  auto r = __builtin_amdgcn_permlane32_swap(__builtin_bit_cast(unsigned, x), __builtin_bit_cast(unsigned, x), false, false);
  const int hi = (threadIdx.x >> 5) & 1;
  return __builtin_bit_cast(float, hi ? r[0] : r[1]);
}

template <bool ACQ = true>
__device__ __forceinline__ void wave_wait(unsigned* ctr, unsigned want, unsigned* err) {
  unsigned spins = 0;
  while (__hip_atomic_load(ctr, __ATOMIC_RELAXED, __HIP_MEMORY_SCOPE_AGENT) < want) {
    __builtin_amdgcn_s_sleep(4);
    if (++spins > SPINCAP) { __hip_atomic_store(err, 1u, __ATOMIC_RELAXED, __HIP_MEMORY_SCOPE_AGENT); break; }
    if ((spins & 1023u) == 0u && __hip_atomic_load(err, __ATOMIC_RELAXED, __HIP_MEMORY_SCOPE_AGENT) != 0u) break;
  }
  if (ACQ) {
    __builtin_amdgcn_fence(__ATOMIC_ACQUIRE, "agent");
    asm volatile("s_waitcnt vmcnt(0)" ::: "memory");
  } else {
    __builtin_amdgcn_fence(__ATOMIC_ACQUIRE, "wavefront");
  }
}
constexpr int CH_RED = 0;
constexpr int CH_VM = 24576;
constexpr int CH_RED2 = 25088;
constexpr int CH_WV = 27136;
constexpr int CH_KV = CH_WV + 65536;
constexpr int CH_LDS = CH_KV + 20480;

__device__ __forceinline__ void chain_role(int c, const u16* __restrict__ mem0, const u16* __restrict__ Wq, const u16* __restrict__ Wk,
                                           const u16* __restrict__ Wv, const u16* __restrict__ Wo, unsigned char* ws, LAS char* lds) {
  const int tid = threadIdx.x, lane = tid & 63, w = __builtin_amdgcn_readfirstlane(tid >> 6);
  const int fr = lane & 15, g = lane >> 4;
  const int h = c >> 2, q4 = c & 3, mh = c & 1, nb = c >> 1;
  unsigned* err = (unsigned*)(ws + WS_ERR);
  const __amdgpu_buffer_rsrc_t rs = __builtin_amdgcn_make_buffer_rsrc((void*)(ws + WS_HOP), 0, (int)HOP_BYTES, 0x00020000);
  v4u wq[4][4], wo[16];
  LAS v4u* wvl = (LAS v4u*)(lds + CH_WV) + w * 512 + lane;
  {
    LAS char* stg = lds + CH_WV + w * 8192;
    const int q = fr >> 2, p = lane & 3;
#pragma unroll
    for (int hf = 0; hf < 2; ++hf) {
#pragma unroll
      for (int i = 0; i < 8; ++i) {
        const int row = i * 32 + (lane >> 1), ch = lane & 1;
        v4u v = *(const v4u*)(Wo + (size_t)(512 * w + 256 * hf + row) * DM + 16 * nb + ch * 8);
        *(LAS v4u*)(stg + row * 32 + ch * 16) = v;
      }
      __builtin_amdgcn_s_waitcnt(0xC07F);
#pragma unroll
      for (int ks = 0; ks < 8; ++ks) {
        v2u lo = trread(stg + (32 * ks + 8 * g + q) * 32 + (4 * p) * 2);
        v2u hi = trread(stg + (32 * ks + 8 * g + 4 + q) * 32 + (4 * p) * 2);
        wo[hf * 8 + ks] = v4u{lo.x, lo.y, hi.x, hi.y};
      }
      __builtin_amdgcn_s_waitcnt(0xC07F);
    }
#pragma unroll
    for (int mat = 0; mat < 3; ++mat) {
      const u16* W = mat == 0 ? Wq : (mat == 1 ? Wk : Wv);
#pragma unroll
      for (int i = 0; i < 8; ++i) {
        const int row = i * 16 + (lane >> 2), ch = lane & 3;
        v4u v = *(const v4u*)(W + (size_t)(128 * w + row) * HD + h * 128 + 32 * q4 + ch * 8);
        *(LAS v4u*)(stg + row * 64 + ch * 16) = v;
      }
      __builtin_amdgcn_s_waitcnt(0xC07F);
      v4u tv[8];
#pragma unroll
      for (int dt = 0; dt < 2; ++dt)
#pragma unroll
        for (int ks = 0; ks < 4; ++ks) {
          v2u lo = trread(stg + (32 * ks + 8 * g + q) * 64 + (16 * dt + 4 * p) * 2);
          v2u hi = trread(stg + (32 * ks + 8 * g + 4 + q) * 64 + (16 * dt + 4 * p) * 2);
          if (mat < 2) wq[(mat & 1) * 2 + dt][ks] = v4u{lo.x, lo.y, hi.x, hi.y};
          else tv[dt * 4 + ks] = v4u{lo.x, lo.y, hi.x, hi.y};
        }
      __builtin_amdgcn_s_waitcnt(0xC07F);
      asm volatile("" ::: "memory");
      if (mat == 2) {
#pragma unroll
        for (int i = 0; i < 8; ++i) wvl[i * 64] = tv[i];
      }
    }
  }
  const float scale = 0.08838834764831845f;
  const float L2E = 1.4426950408889634f;
  LAS f32x4* red = (LAS f32x4*)(lds + CH_RED);
  LAS f32x4* red2 = (LAS f32x4*)(lds + CH_RED2);
  LAS u16* vml = (LAS u16*)(lds + CH_VM);
  LAS v4u* kvl = (LAS v4u*)(lds + CH_KV) + lane;
  const __amdgpu_buffer_rsrc_t rsw = __builtin_amdgcn_make_buffer_rsrc((void*)ws, 0, (int)WS_TOTAL, 0x00020000);
  auto kv_prefetch = [&](int sn) {
    if ((sn & 1) == 0) wave_wait<false>(err + CW_KV + 16 * (sn >> 1), 64u, err);
    v4u t[10];
#pragma unroll
    for (int tt = 0; tt < 2; ++tt)
#pragma unroll
      for (int j = 0; j < 4; ++j) t[tt * 4 + j] = ld_sc1(rsw, (int)(WS_K + ((size_t)(32 * sn + 16 * tt + fr) * HD + h * 128 + 32 * j + 8 * g) * 2));
#pragma unroll
    for (int dt = 0; dt < 2; ++dt) t[8 + dt] = ld_sc1(rsw, (int)(WS_VT + ((((size_t)sn * NH + h) * 128 + 32 * q4 + 16 * dt + fr) * 32 + 8 * g) * 2));
#pragma unroll
    for (int i = 0; i < 10; ++i) kvl[((sn & 1) * 10 + i) * 64] = t[i];
  };
  if (w == 2) kv_prefetch(0);

#define STAMP(i)
  for (int s = 0; s < NSEG; ++s) {
    v4u mf[2];
    if (s == 0) {
#pragma unroll
      for (int p = 0; p < 2; ++p)
        mf[p] = *(const v4u*)(mem0 + (size_t)(fr & 7) * DM + 128 * w + 32 * (2 * p + (fr >> 3)) + 8 * g);
    } else {
      poll<2>(rs, (int)(HOP_MEM + (size_t)s * 16384) + ((16 * w + 4 * (fr >> 3) + g) * 8 + (fr & 7)) * 16, mf, err);
    }
    STAMP(0)
    {
      f32x4 a0[4], a1[4];
#pragma unroll
      for (int t = 0; t < 4; ++t) {
        a0[t] = mfma16(wq[t][0], mf[0], f32x4{0.f, 0.f, 0.f, 0.f});
        a1[t] = mfma16(wq[t][1], mf[0], f32x4{0.f, 0.f, 0.f, 0.f});
      }
#pragma unroll
      for (int t = 0; t < 4; ++t) {
        a0[t] = mfma16(wq[t][2], mf[1], a0[t]);
        a1[t] = mfma16(wq[t][3], mf[1], a1[t]);
      }
#pragma unroll
      for (int t = 0; t < 4; ++t) {
        f32x4 x = (fr & 8) ? a1[t] : a0[t];
#pragma unroll
        for (int r = 0; r < 4; ++r) x[r] += row_ror<8>(x[r]);
        if (fr < 8) red[(w * 6 + t) * 32 + g * 8 + fr] = x;
      }
    }
    __syncthreads();
    STAMP(1)
    if (tid >= 384) {
      const int u = tid - 384;
      const int t6 = u >> 5, gg = (u >> 3) & 3, m = u & 7;
      f32x4 x = red[t6 * 32 + (u & 31)];
#pragma unroll
      for (int ww = 1; ww < 8; ++ww) x += red[(ww * 6 + t6) * 32 + (u & 31)];
      const unsigned lo = pack2(x[0], x[1]), hi = pack2(x[2], x[3]);
      const int mat = t6 >> 1, dt = t6 & 1;
      const int off = (int)(HOP_QK + ((size_t)s * NH + h) * 4096) + ((q4 * 2 + mat) * 8 + m) * 64 + (16 * dt + 4 * gg) * 2;
      __builtin_amdgcn_raw_buffer_store_b64(v2u{nosent(lo), nosent(hi)}, rs, off, 0, 16);
    }
    {
      f32x4 a0[2], a1[2];
#pragma unroll
      for (int t = 0; t < 2; ++t) {
        a0[t] = mfma16(wvl[(t * 4 + 0) * 64], mf[0], f32x4{0.f, 0.f, 0.f, 0.f});
        a1[t] = mfma16(wvl[(t * 4 + 1) * 64], mf[0], f32x4{0.f, 0.f, 0.f, 0.f});
      }
#pragma unroll
      for (int t = 0; t < 2; ++t) {
        a0[t] = mfma16(wvl[(t * 4 + 2) * 64], mf[1], a0[t]);
        a1[t] = mfma16(wvl[(t * 4 + 3) * 64], mf[1], a1[t]);
      }
#pragma unroll
      for (int t = 0; t < 2; ++t) {
        f32x4 x = (fr & 8) ? a1[t] : a0[t];
#pragma unroll
        for (int r = 0; r < 4; ++r) x[r] += row_ror<8>(x[r]);
        if (fr < 8) red[(w * 6 + 4 + t) * 32 + g * 8 + fr] = x;
      }
    }
    __syncthreads();
    STAMP(2)
    if (w == 1) {
      const int dt = lane >> 5, gg = (lane >> 3) & 3, m = lane & 7;
      f32x4 x = red[(4 + dt) * 32 + (lane & 31)];
#pragma unroll
      for (int ww = 1; ww < 8; ++ww) x += red[(ww * 6 + 4 + dt) * 32 + (lane & 31)];
      const unsigned lo = pack2(x[0], x[1]), hi = pack2(x[2], x[3]);
      const int d = 16 * dt + 4 * gg;
      vml[(d + 0) * 8 + m] = (u16)(lo & 0xFFFF);
      vml[(d + 1) * 8 + m] = (u16)(lo >> 16);
      vml[(d + 2) * 8 + m] = (u16)(hi & 0xFFFF);
      vml[(d + 3) * 8 + m] = (u16)(hi >> 16);
      __builtin_amdgcn_s_waitcnt(0xC07F);
      asm volatile("" ::: "memory");
      if (lane < 32)
      {
        v4u t = *(LAS v4u*)(vml + lane * 8);
        t.x = nosent(t.x); t.y = nosent(t.y); t.z = nosent(t.z); t.w = nosent(t.w);
        __builtin_amdgcn_raw_buffer_store_b128(t, rs, (int)(HOP_VM + (((size_t)s * NH + h) * 128 + 32 * q4 + lane) * 16), 0, 16);
      }
    }
    if (s == NSEG - 1) break;
    if (w == 2 && s + 1 < NSEG - 1) kv_prefetch(s + 1);
    v4u pt, pm;
    if (w == 0) {
      v4u X[8];
      {
        const int base = (int)(HOP_QK + ((size_t)s * NH + h) * 4096) + (fr & 7) * 64 + g * 16;
        unsigned spins = 0;
        for (;;) {
          unsigned mx = 0;
#pragma unroll
          for (int i = 0; i < 8; ++i) X[i] = ld_sc1(rs, base + i * 512);
          asm volatile("" ::: "memory");
#pragma unroll
          for (int i = 0; i < 8; ++i) mx = max(max(mx, max(X[i].x, X[i].y)), max(X[i].z, X[i].w));
          if (__all(mx != SENT)) break;
          if (++spins > SPINCAP) {
            if (lane == 0) __hip_atomic_store(err, 1u, __ATOMIC_RELAXED, __HIP_MEMORY_SCOPE_AGENT);
            break;
          }
          if ((spins & 1023u) == 0u && __hip_atomic_load(err, __ATOMIC_RELAXED, __HIP_MEMORY_SCOPE_AGENT) != 0u) break;
        }
      }
      STAMP(3)
      f32x4 s0 = {0.f, 0.f, 0.f, 0.f}, s1 = s0, s2 = s0;
#pragma unroll
      for (int j = 0; j < 4; ++j) {
        s1 = mfma16(kvl[((s & 1) * 10 + j) * 64], X[2 * j], s1);
        s2 = mfma16(kvl[((s & 1) * 10 + 4 + j) * 64], X[2 * j], s2);
        s0 = mfma16(X[2 * j + 1], X[2 * j], s0);
      }
      const bool up = fr >= 8;
      f32x4 st = up ? s2 : s1;
      const float m01 = up ? s0[1] : s0[0], m23 = up ? s0[3] : s0[2];
      const float sm = g >= 2 ? m23 : m01;
      unsigned p0 = pack2(st[0], st[1]), p1 = pack2(st[2], st[3]), p2 = pack2(sm, sm);
      p0 = pack2(bflo(p0) * scale, bfhi(p0) * scale);
      p1 = pack2(bflo(p1) * scale, bfhi(p1) * scale);
      p2 = pack2(bflo(p2) * scale, bflo(p2) * scale);
      float v0 = bflo(p0), v1 = bfhi(p0), v2 = bflo(p1), v3 = bfhi(p1), v4 = bflo(p2);
      float mx = fmaxf(fmaxf(fmaxf(v0, v1), fmaxf(v2, v3)), v4);
      mx = fmaxf(mx, row_ror<8>(mx));
      mx = fmaxf(mx, xor16(mx));
      mx = fmaxf(mx, xor32(mx));
      const float nm = -mx * L2E;
      v0 = __builtin_amdgcn_exp2f(__builtin_fmaf(v0, L2E, nm));
      v1 = __builtin_amdgcn_exp2f(__builtin_fmaf(v1, L2E, nm));
      v2 = __builtin_amdgcn_exp2f(__builtin_fmaf(v2, L2E, nm));
      v3 = __builtin_amdgcn_exp2f(__builtin_fmaf(v3, L2E, nm));
      v4 = __builtin_amdgcn_exp2f(__builtin_fmaf(v4, L2E, nm));
      float sum = ((v0 + v1) + (v2 + v3)) + v4;
      sum += row_ror<8>(sum);
      sum += xor16(sum);
      sum += xor32(sum);
      const float inv = __builtin_amdgcn_rcpf(sum);
      pt.x = pack2(v0 * inv, v1 * inv);
      pt.y = pack2(v2 * inv, v3 * inv);
      pt.z = (unsigned)__builtin_amdgcn_update_dpp(0, (int)pt.x, 0x128, 0xf, 0xf, false);
      pt.w = (unsigned)__builtin_amdgcn_update_dpp(0, (int)pt.y, 0x128, 0xf, 0xf, false);
      v4 *= inv;
      pm.x = pack2(v4, row_ror<8>(v4));
      pm.y = 0u; pm.z = 0u; pm.w = 0u;
    }
    __syncthreads();
    if (w == 0) {
      f32x4 o[2];
#pragma unroll
      for (int dt = 0; dt < 2; ++dt) o[dt] = mfma16(kvl[((s & 1) * 10 + 8 + dt) * 64], pt, f32x4{0.f, 0.f, 0.f, 0.f});
#pragma unroll
      for (int dt = 0; dt < 2; ++dt) {
        const unsigned t = *(LAS unsigned*)(vml + (16 * dt + fr) * 8 + 4 * (g & 1) + 2 * (g >> 1));
        o[dt] = mfma16(v4u{t, 0u, 0u, 0u}, pm, o[dt]);
      }
      if (fr < 8) {
#pragma unroll
        for (int dt = 0; dt < 2; ++dt) {
          const int k8 = h * 16 + 4 * q4 + 2 * dt + (g >> 1);
          const int off = (int)(HOP_OB + (size_t)s * 65536) + (((fr >> 2) * 512 + k8) * 4 + (fr & 3)) * 16 + (g & 1) * 8;
          __builtin_amdgcn_raw_buffer_store_b64(v2u{nosent(pack2(o[dt][0], o[dt][1])), nosent(pack2(o[dt][2], o[dt][3]))}, rs, off, 0, 16);
        }
      }
    }
    __syncthreads();
    STAMP(4)
    {
      v4u of[4];
      poll<4>(rs, (int)(HOP_OB + (size_t)s * 65536) + ((mh * 512 + 64 * w + 4 * (fr >> 2) + g) * 4 + (fr & 3)) * 16, of, err);
      STAMP(5)
      f32x4 a[4];
#pragma unroll
      for (int r4 = 0; r4 < 4; ++r4) a[r4] = mfma16(wo[r4], of[0], f32x4{0.f, 0.f, 0.f, 0.f});
#pragma unroll
      for (int i = 1; i < 4; ++i)
#pragma unroll
        for (int r4 = 0; r4 < 4; ++r4) a[r4] = mfma16(wo[4 * i + r4], of[i], a[r4]);
      const int r4 = fr >> 2;
      f32x4 x = r4 == 0 ? a[0] : (r4 == 1 ? a[1] : (r4 == 2 ? a[2] : a[3]));
#pragma unroll
      for (int r = 0; r < 4; ++r) { x[r] += row_ror<8>(x[r]); x[r] += row_ror<4>(x[r]); }
      if (fr < 4) red2[w * 16 + g * 4 + fr] = x;
    }
    __syncthreads();
    if (tid < 16) {
      f32x4 x = red2[tid];
#pragma unroll
      for (int ww = 1; ww < 8; ++ww) x += red2[ww * 16 + tid];
      const int gg = tid >> 2, m4 = tid & 3;
      const int k8 = 2 * nb + (gg >> 1);
      const int off = (int)(HOP_MEM + (size_t)(s + 1) * 16384) + (k8 * 8 + 4 * mh + m4) * 16 + (gg & 1) * 8;
      __builtin_amdgcn_raw_buffer_store_b64(v2u{nosent(pack2(x[0], x[1])), nosent(pack2(x[2], x[3]))}, rs, off, 0, 16);
    }
    STAMP(6)
  }
}

__device__ __forceinline__ void attn_unit(int s, int h, int lane, unsigned char* ws) {
  const int fr = lane & 15, g = lane >> 4;
  const u16* Q = (const u16*)(ws + WS_Q);
  const u16* Kx = (const u16*)(ws + WS_K);
  const u16* Vt = (const u16*)(ws + WS_VT);
  const __amdgpu_buffer_rsrc_t rs = __builtin_amdgcn_make_buffer_rsrc((void*)(ws + WS_HOP), 0, (int)HOP_BYTES, 0x00020000);
  unsigned* err = (unsigned*)(ws + WS_ERR);
  v4u km[4];
  poll<4>(rs, (int)(HOP_QK + ((size_t)s * NH + h) * 4096) + 512 + (fr & 7) * 64 + g * 16, km, err);
  v4u vmr[2];
  poll<2>(rs, (int)(HOP_VM + ((size_t)s * NH + h) * 2048) + lane * 16, vmr, err);
  const float scale = 0.08838834764831845f;
  f32x4 sc[3][2];
#pragma unroll
  for (int a = 0; a < 3; ++a)
#pragma unroll
    for (int b = 0; b < 2; ++b) sc[a][b] = f32x4{0.f, 0.f, 0.f, 0.f};
#pragma unroll
  for (int j = 0; j < 4; ++j) {
    v4u qf[2], kf[3];
#pragma unroll
    for (int t = 0; t < 2; ++t) {
      qf[t] = *(const v4u*)(Q + (size_t)(32 * s + 16 * t + fr) * HD + h * 128 + 32 * j + 8 * g);
      kf[1 + t] = *(const v4u*)(Kx + (size_t)(32 * s + 16 * t + fr) * HD + h * 128 + 32 * j + 8 * g);
    }
    kf[0] = km[j];
#pragma unroll
    for (int a = 0; a < 3; ++a)
#pragma unroll
      for (int b = 0; b < 2; ++b) sc[a][b] = mfma16(kf[a], qf[b], sc[a][b]);
  }
  v4u pt[2], pm[2];
#pragma unroll
  for (int b = 0; b < 2; ++b) {
    const int qi = 16 * b + fr;
    float v[12];
    bool ok[12];
#pragma unroll
    for (int r = 0; r < 4; ++r) {
      v[r] = rbf(rbf(sc[0][b][r]) * scale); ok[r] = g < 2;
      v[4 + r] = rbf(rbf(sc[1][b][r]) * scale); ok[4 + r] = (4 * g + r) <= qi;
      v[8 + r] = rbf(rbf(sc[2][b][r]) * scale); ok[8 + r] = (16 + 4 * g + r) <= qi;
    }
    float mx = -3.0e38f;
#pragma unroll
    for (int i = 0; i < 12; ++i) mx = ok[i] ? fmaxf(mx, v[i]) : mx;
    mx = fmaxf(mx, __shfl_xor(mx, 16));
    mx = fmaxf(mx, __shfl_xor(mx, 32));
    float sum = 0.f;
#pragma unroll
    for (int i = 0; i < 12; ++i) { v[i] = ok[i] ? __expf(v[i] - mx) : 0.f; sum += v[i]; }
    sum += __shfl_xor(sum, 16);
    sum += __shfl_xor(sum, 32);
    const float inv = 1.0f / sum;
    pt[b].x = pack2(v[4] * inv, v[5] * inv); pt[b].y = pack2(v[6] * inv, v[7] * inv);
    pt[b].z = pack2(v[8] * inv, v[9] * inv); pt[b].w = pack2(v[10] * inv, v[11] * inv);
    pm[b].x = pack2(v[0] * inv, v[1] * inv); pm[b].y = pack2(v[2] * inv, v[3] * inv); pm[b].z = 0u; pm[b].w = 0u;
  }
#pragma unroll
  for (int dt = 0; dt < 8; ++dt) {
    v4u vtf = *(const v4u*)(Vt + (((size_t)s * NH + h) * 128 + 16 * dt + fr) * 32 + 8 * g);
    v4u vmf;
    {
      const int src = (16 * dt + fr) & 63;
      const v4u vv = vmr[dt >> 2];
      const unsigned a0 = __shfl(vv.x, src), a1 = __shfl(vv.y, src), a2 = __shfl(vv.z, src), a3 = __shfl(vv.w, src);
      vmf.x = g == 0 ? a0 : (g == 1 ? a2 : 0u);
      vmf.y = g == 0 ? a1 : (g == 1 ? a3 : 0u);
      vmf.z = 0u; vmf.w = 0u;
    }
#pragma unroll
    for (int b = 0; b < 2; ++b) {
      f32x4 o = {0.f, 0.f, 0.f, 0.f};
      o = mfma16(vtf, pt[b], o);
      o = mfma16(vmf, pm[b], o);
      wt8(ws, WS_OT + ((size_t)(32 * s + 16 * b + fr) * HD + h * 128 + 16 * dt + 4 * g) * 2, v2u{pack2(o[0], o[1]), pack2(o[2], o[3])});
    }
  }
}
__device__ __forceinline__ void wg_signal(unsigned* ctr, int n) {
  asm volatile("s_waitcnt vmcnt(0)" ::: "memory");
  __syncthreads();
  if (threadIdx.x < n) __hip_atomic_fetch_add(ctr + 16 * threadIdx.x, 1u, __ATOMIC_RELAXED, __HIP_MEMORY_SCOPE_AGENT);
}
__device__ __forceinline__ void wg_wait(unsigned* ctr, unsigned want, unsigned* err) {
  if (threadIdx.x < 64) wave_wait(ctr, want, err);
  __syncthreads();
}

__global__ __launch_bounds__(512, 2) void k_mega(const u16* x, const u16* mem0, const u16* Wq, const u16* Wk, const u16* Wv, const u16* Wo,
                                                 const u16* w1, const u16* b1, const u16* w2, const u16* b2, u16* out, unsigned char* ws) {
  extern __shared__ __attribute__((aligned(16))) char smem[];
  LAS char* lds = (LAS char*)smem;
  const int b = blockIdx.x;
  const int xcd = b & 7, i = b >> 4;
  if (((b >> 3) & 1) == 0) {
    const int c = ((xcd * 4 + (i >> 2)) << 2) | (i & 3);
    chain_role(c, mem0, Wq, Wk, Wv, Wo, ws, lds);
    return;
  }
  const int gi = i * 8 + xcd;
  unsigned* cw = (unsigned*)(ws + WS_ERR);
  unsigned* err = cw + CW_ERR;
  task_qkv<64, 2, 4>(gi >> 6, 32 + (gi & 63), Wq, Wk, Wv, ws, lds);
  wg_signal(cw + CW_KV + 16 * (gi >> 6), 1);
  for (int t = gi; t < 64 + 7 * 64; t += 128) {
    if (t < 64) {
      task_qkv<128, 2, 4>(1, 32 + t, Wq, Wk, Wv, ws, lds);
      wg_signal(cw + CW_KV + 16 * 2, 2);
    } else {
      const int rt = 1 + ((t - 64) >> 6);
      task_qkv<256, 4, 2>(rt, 32 + ((t - 64) & 63), Wq, Wk, Wv, ws, lds);
      wg_signal(cw + CW_KV + 16 * 4 * rt, 4);
    }
  }
  for (int t = gi; t < 8 * 28; t += 128) task_mlp1<256, 4, 2>(t & 7, t >> 3, w1, b1, ws, lds);
  wg_signal(cw + CW_MLP1, 1);
  for (int t = gi; t < 8 * 32; t += 128) task_qkv<256, 4, 2>(t & 7, t >> 3, Wq, Wk, Wv, ws, lds);
  wg_signal(cw + CW_Q, 1);
  wg_wait(cw + CW_MLP1, 128u, err);
  for (int t = gi; t < 32 * 8; t += 128) task_mlp2<64, 2, 4>(t & 31, t >> 5, w2, b2, ws, lds);
  wg_signal(cw + CW_MLP2, 1);
  if (threadIdx.x < 64) {
    const int l = threadIdx.x;
    unsigned* p = l < 32 ? cw + CW_KV + 16 * l : (l == 32 ? cw + CW_Q : cw + CW_MLP2);
    const unsigned want = l < 32 ? 64u : (l < 34 ? 128u : 0u);
    unsigned spins = 0;
    while (!__all(__hip_atomic_load(p, __ATOMIC_RELAXED, __HIP_MEMORY_SCOPE_AGENT) >= want)) {
      __builtin_amdgcn_s_sleep(8);
      if (++spins > SPINCAP) { __hip_atomic_store(err, 1u, __ATOMIC_RELAXED, __HIP_MEMORY_SCOPE_AGENT); break; }
      if ((spins & 1023u) == 0u && __hip_atomic_load(err, __ATOMIC_RELAXED, __HIP_MEMORY_SCOPE_AGENT) != 0u) break;
    }
    __builtin_amdgcn_fence(__ATOMIC_ACQUIRE, "agent");
    asm volatile("s_waitcnt vmcnt(0)" ::: "memory");
  }
  __syncthreads();
  for (int t = gi; t < 32 * 16; t += 128) {
    const int u = t >> 4, r = t & 15;
    if (r < 8) {
      attn_unit(2 * u + (r >> 2), (r & 3) * 8 + (threadIdx.x >> 6), threadIdx.x & 63, ws);
      wg_signal(cw + CW_AT + 16 * u, 1);
    } else {
      wg_wait(cw + CW_AT + 16 * u, 8u, err);
      task_wo<64, 2, 4>(u, r - 8, Wo, x, out, ws, lds);
    }
  }
}

extern "C" void kernel_launch(void* const* d_in, const int* in_sizes, int n_in, void* d_out, int out_size, void* d_ws, size_t ws_size,
                              hipStream_t stream) {
  const u16* x = (const u16*)d_in[0];
  const u16* mem0 = (const u16*)d_in[1];
  const u16* Wq = (const u16*)d_in[2];
  const u16* Wk = (const u16*)d_in[3];
  const u16* Wv = (const u16*)d_in[4];
  const u16* Wo = (const u16*)d_in[5];
  const u16* w1 = (const u16*)d_in[8];
  const u16* b1 = (const u16*)d_in[9];
  const u16* w2 = (const u16*)d_in[10];
  const u16* b2 = (const u16*)d_in[11];
  unsigned char* ws = (unsigned char*)d_ws;
  u16* out = (u16*)d_out;
  constexpr int L256 = GemmCfg<256>::LDS_BYTES;
  constexpr int LMEGA = L256 > CH_LDS ? L256 : CH_LDS;
  (void)hipFuncSetAttribute((const void*)k_mega, hipFuncAttributeMaxDynamicSharedMemorySize, LMEGA);
  k_ln<<<S_LEN / 4, 256, 0, stream>>>(x, ws);
  k_mega<<<256, 512, LMEGA, stream>>>(x, mem0, Wq, Wk, Wv, Wo, w1, b1, w2, b2, out, ws);
}
```

```cpp
#include <hip/hip_runtime.h>
#include <hip/hip_bf16.h>
#include <stdint.h>

constexpr int S_LEN = 2048, DM = 1024, NH = 32, DH = 128, HD = NH * DH;
constexpr int MEMN = 8, SEG = 32, NSEG = S_LEN / SEG;
constexpr int FF = 3584;

typedef unsigned v4u __attribute__((ext_vector_type(4)));
typedef unsigned v2u __attribute__((ext_vector_type(2)));
typedef float f32x4 __attribute__((ext_vector_type(4)));
typedef float f32x2 __attribute__((ext_vector_type(2)));
typedef short bf16x8 __attribute__((ext_vector_type(8)));
typedef short s16x4 __attribute__((ext_vector_type(4)));
typedef __bf16 bfx2 __attribute__((ext_vector_type(2)));
typedef unsigned short u16;
#define LAS __attribute__((address_space(3)))
#define GAS __attribute__((address_space(1)))

constexpr size_t WS_XN = 0;
constexpr size_t WS_Y = WS_XN + (size_t)S_LEN * DM * 2;
constexpr size_t WS_Q = WS_Y + (size_t)S_LEN * DM * 2;
constexpr size_t WS_K = WS_Q + (size_t)S_LEN * HD * 2;
constexpr size_t WS_VT = WS_K + (size_t)S_LEN * HD * 2;
constexpr size_t WS_H = WS_VT + (size_t)S_LEN * HD * 2;
constexpr size_t WS_BO = WS_H + (size_t)S_LEN * FF * 2;
constexpr size_t WS_OT = WS_BO + (size_t)S_LEN * DM * 2;
constexpr size_t WS_SLAB = WS_OT + (size_t)S_LEN * HD * 2;
constexpr size_t WS_HOP = WS_SLAB + (size_t)32 * 8 * 64 * 128 * 4;
constexpr size_t HOP_MEM = 0;
constexpr size_t HOP_QK = HOP_MEM + (size_t)NSEG * 16384;
constexpr size_t HOP_OB = HOP_QK + (size_t)NSEG * NH * 4096;
constexpr size_t HOP_VM = HOP_OB + (size_t)NSEG * 65536;
constexpr size_t HOP_BYTES = HOP_VM + (size_t)NSEG * HD * MEMN * 2;
constexpr size_t WS_ERR = WS_HOP + HOP_BYTES;
constexpr size_t WS_TOTAL = WS_ERR + 8192;
constexpr int CW_ERR = 0, CW_MLP1 = 16, CW_Q = 32, CW_MLP2 = 48, CW_KV = 64  , CW_AT = 64 + 512  , CW_SK = 64 + 1024  ;

#define DLY1 14
#define DLY2 0
#define DLY3 14
#define DLYR 0
constexpr unsigned SENT = 0xFFFFFFFFu;
constexpr unsigned SPINCAP = 1u << 20;

__device__ __forceinline__ float bf2f(unsigned b16) { return __builtin_bit_cast(float, b16 << 16); }
__device__ __forceinline__ float bflo(unsigned x) { return __builtin_bit_cast(float, x << 16); }
__device__ __forceinline__ float bfhi(unsigned x) { return __builtin_bit_cast(float, x & 0xFFFF0000u); }
__device__ __forceinline__ unsigned pack2(float a, float b) {
  f32x2 v = {a, b};
  bfx2 r = __builtin_convertvector(v, bfx2);
  return __builtin_bit_cast(unsigned, r);
}
__device__ __forceinline__ float rbf(float a) { return (float)(__bf16)a; }
__device__ __forceinline__ unsigned nosent(unsigned x) { return x == SENT ? 0xFFFFFFFEu : x; }
__device__ __forceinline__ f32x4 mfma16(v4u a, v4u b, f32x4 c) {
  return __builtin_amdgcn_mfma_f32_16x16x32_bf16(__builtin_bit_cast(bf16x8, a), __builtin_bit_cast(bf16x8, b), c, 0, 0, 0);
}
__device__ __forceinline__ v2u trread(LAS const char* p) {
  typedef short v4i16_t __attribute__((ext_vector_type(4)));
  return __builtin_bit_cast(v2u, __builtin_amdgcn_ds_read_tr16_b64_v4i16((LAS v4i16_t*)p));
}
__device__ __forceinline__ float ror(float x, int) = delete;
template <int N> __device__ __forceinline__ float row_ror(float x) {
  return __builtin_bit_cast(float, __builtin_amdgcn_update_dpp(0, __builtin_bit_cast(int, x), 0x120 + N, 0xf, 0xf, false));
}

__device__ __forceinline__ float wave_sum(float v) {
#pragma unroll
  for (int o = 32; o > 0; o >>= 1) v += __shfl_xor(v, o);
  return v;
}
__device__ __forceinline__ void ln16(float (&v)[16]) {
  float s = 0.f;
#pragma unroll
  for (int i = 0; i < 16; ++i) s += v[i];
  s = wave_sum(s);
  const float mean = s * (1.0f / DM);
  float q = 0.f;
#pragma unroll
  for (int i = 0; i < 16; ++i) { float d = v[i] - mean; q += d * d; }
  q = wave_sum(q);
  const float mb = rbf(mean);
  const float vb = rbf(q * (1.0f / DM));
  const float den = rbf(sqrtf(rbf(vb + 1e-5f)));
#pragma unroll
  for (int i = 0; i < 16; ++i) v[i] = rbf(rbf(v[i] - mb) / den);
}
__global__ __launch_bounds__(256) void k_ln(const u16* __restrict__ x, unsigned char* __restrict__ ws) {
  const int tid = threadIdx.x, lane = tid & 63, w = tid >> 6;
  const int row = blockIdx.x * 4 + w;
  {
    v4u s = {SENT, SENT, SENT, SENT};
    v4u* p = (v4u*)(ws + WS_HOP);
    const size_t n = HOP_BYTES / 16;
    for (size_t i = (size_t)blockIdx.x * 256 + tid; i < n; i += (size_t)gridDim.x * 256) p[i] = s;
    if (blockIdx.x < 8) ((unsigned*)(ws + WS_ERR))[blockIdx.x * 256 + tid] = 0u;
  }
  const v4u* xr = (const v4u*)(x + (size_t)row * DM);
  v4u a = xr[lane], b = xr[64 + lane];
  float v[16];
  v[0] = bflo(a.x); v[1] = bfhi(a.x); v[2] = bflo(a.y); v[3] = bfhi(a.y);
  v[4] = bflo(a.z); v[5] = bfhi(a.z); v[6] = bflo(a.w); v[7] = bfhi(a.w);
  v[8] = bflo(b.x); v[9] = bfhi(b.x); v[10] = bflo(b.y); v[11] = bfhi(b.y);
  v[12] = bflo(b.z); v[13] = bfhi(b.z); v[14] = bflo(b.w); v[15] = bfhi(b.w);
  ln16(v);
  v4u* xn = (v4u*)(ws + WS_XN + (size_t)row * DM * 2);
  xn[lane] = v4u{pack2(v[0], v[1]), pack2(v[2], v[3]), pack2(v[4], v[5]), pack2(v[6], v[7])};
  xn[64 + lane] = v4u{pack2(v[8], v[9]), pack2(v[10], v[11]), pack2(v[12], v[13]), pack2(v[14], v[15])};
  ln16(v);
  v4u* y = (v4u*)(ws + WS_Y + (size_t)row * DM * 2);
  y[lane] = v4u{pack2(v[0], v[1]), pack2(v[2], v[3]), pack2(v[4], v[5]), pack2(v[6], v[7])};
  y[64 + lane] = v4u{pack2(v[8], v[9]), pack2(v[10], v[11]), pack2(v[12], v[13]), pack2(v[14], v[15])};
}

template <int BM> struct GemmCfg {
  static constexpr int BN = 128, BK = 64;
  static constexpr int A_BYTES = BM * BK * 2, B_BYTES = BK * BN * 2, ST_BYTES = A_BYTES + B_BYTES;
  static constexpr int NST = BM == 256 ? 3 : (BM == 128 ? 4 : 6);
  static constexpr int LDS_BYTES = NST * ST_BYTES;
};

template <int N> __device__ __forceinline__ void wait_vm() { asm volatile("s_waitcnt vmcnt(%0)" ::"n"(N) : "memory"); }

template <int BM, int WM, int WN, bool TOUT, class Epi>
__device__ __forceinline__ void gemm_tile(const u16* __restrict__ A, int lda, const u16* __restrict__ B, int ldb, int K,
                                          int brow, int bcol, LAS char* lds, Epi epi) {
  using C = GemmCfg<BM>;
  constexpr int TM = BM / WM / 16, TN = C::BN / WN / 16;
  constexpr int AI = C::A_BYTES / 8192, BI = C::B_BYTES / 8192;
  const int tid = threadIdx.x, lane = tid & 63, w = __builtin_amdgcn_readfirstlane(tid >> 6);
  const int wr = w / WN, wc = w % WN, fr = lane & 15, fq = lane >> 4;
  const u16* asrc[AI];
  const u16* bsrc[BI];
#pragma unroll
  for (int i = 0; i < AI; ++i) {
    const int ii = w * AI + i, row = ii * 8 + (lane >> 3), ch = (lane & 7) ^ (row & 7);
    asrc[i] = A + (size_t)(brow + row) * lda + ch * 8;
  }
#pragma unroll
  for (int i = 0; i < BI; ++i) {
    const int ii = w * BI + i, row = ii * 4 + (lane >> 4), ch = (lane & 15) ^ (((row & 3) << 2) | ((row >> 2) & 3));
    bsrc[i] = B + (size_t)row * ldb + bcol + ch * 8;
  }
  auto stage = [&](int t, int buf) {
    LAS char* base = lds + buf * C::ST_BYTES;
#pragma unroll
    for (int i = 0; i < AI; ++i)
      __builtin_amdgcn_global_load_lds((const GAS void*)(asrc[i] + t * 64), (LAS void*)(base + (w * AI + i) * 1024), 16, 0, 0);
#pragma unroll
    for (int i = 0; i < BI; ++i)
      __builtin_amdgcn_global_load_lds((const GAS void*)(bsrc[i] + (size_t)t * 64 * ldb), (LAS void*)(base + C::A_BYTES + (w * BI + i) * 1024), 16, 0, 0);
  };
  int aoff[TM][2], boff[TN][2][2];
#pragma unroll
  for (int m = 0; m < TM; ++m)
#pragma unroll
    for (int k = 0; k < 2; ++k) {
      const int row = wr * (TM * 16) + m * 16 + fr, ch = k * 4 + fq;
      aoff[m][k] = row * 128 + ((ch ^ (row & 7)) * 16);
    }
  {
    const int q = fr >> 2, p = lane & 3;
#pragma unroll
    for (int n = 0; n < TN; ++n)
#pragma unroll
      for (int k = 0; k < 2; ++k)
#pragma unroll
        for (int t = 0; t < 2; ++t) {
          const int row = 32 * k + 8 * fq + 4 * t + q, c = wc * TN + n, ch = 2 * c + (p >> 1);
          boff[n][k][t] = C::A_BYTES + 256 * row + 16 * (ch ^ (((row & 3) << 2) | ((row >> 2) & 3))) + 8 * (p & 1);
        }
  }
  f32x4 acc[TM][TN];
#pragma unroll
  for (int m = 0; m < TM; ++m)
#pragma unroll
    for (int n = 0; n < TN; ++n) acc[m][n] = f32x4{0.f, 0.f, 0.f, 0.f};
  const int nt = K / 64;
  constexpr int NST = C::NST, PER = AI + BI;
#pragma unroll
  for (int i = 0; i < NST - 1; ++i)
    if (i < nt) stage(i, i);
  int rb = 0, sb = NST - 1;
  for (int t = 0; t < nt; ++t) {
    {
      const int rem = nt - 1 - t;
      if (rem >= NST - 2) wait_vm<(NST - 2) * PER>();
      else if (NST > 5 && rem == 3) wait_vm<3 * PER>();
      else if (NST > 4 && rem == 2) wait_vm<2 * PER>();
      else if (NST > 3 && rem == 1) wait_vm<PER>();
      else wait_vm<0>();
    }
    asm volatile("s_waitcnt lgkmcnt(0)" ::: "memory");
    __builtin_amdgcn_s_barrier();
    asm volatile("" ::: "memory");
    if (t + NST - 1 < nt) stage(t + NST - 1, sb);
    LAS const char* base = lds + rb * C::ST_BYTES;
    v4u af[TM][2], bfr[TN][2];
#pragma unroll
    for (int k = 0; k < 2; ++k) {
#pragma unroll
      for (int n = 0; n < TN; ++n) {
        v2u lo = trread(base + boff[n][k][0]), hi = trread(base + boff[n][k][1]);
        bfr[n][k] = v4u{lo.x, lo.y, hi.x, hi.y};
      }
#pragma unroll
      for (int m = 0; m < TM; ++m) af[m][k] = *(LAS const v4u*)(base + aoff[m][k]);
    }
    __builtin_amdgcn_s_setprio(1);
#pragma unroll
    for (int k = 0; k < 2; ++k)
#pragma unroll
      for (int m = 0; m < TM; ++m)
#pragma unroll
        for (int n = 0; n < TN; ++n)
          acc[m][n] = TOUT ? mfma16(af[m][k], bfr[n][k], acc[m][n]) : mfma16(bfr[n][k], af[m][k], acc[m][n]);
    __builtin_amdgcn_s_setprio(0);
    rb = rb == NST - 1 ? 0 : rb + 1;
    sb = sb == NST - 1 ? 0 : sb + 1;
  }
#pragma unroll
  for (int m = 0; m < TM; ++m)
#pragma unroll
    for (int n = 0; n < TN; ++n) {
      const int r0 = brow + wr * (TM * 16) + m * 16, c0 = bcol + wc * (TN * 16) + n * 16;
      if (TOUT) epi(r0 + fq * 4, c0 + fr, acc[m][n]);
      else epi(r0 + fr, c0 + fq * 4, acc[m][n]);
    }
  asm volatile("s_waitcnt lgkmcnt(0)" ::: "memory");
  __builtin_amdgcn_s_barrier();
}

__device__ __forceinline__ void wt8(unsigned char* ws, size_t off, v2u v) {
  const __amdgpu_buffer_rsrc_t rs = __builtin_amdgcn_make_buffer_rsrc((void*)ws, 0, (int)WS_TOTAL, 0x00020000);
  __builtin_amdgcn_raw_buffer_store_b64(v, rs, (int)off, 0, 16);
}
__device__ __forceinline__ float gelu_tanh(float x) {
  const float u = 0.7978845608028654f * (x + 0.044715f * x * x * x);
  const float e = __expf(2.0f * u);
  const float t = 1.0f - 2.0f / (e + 1.0f);
  return 0.5f * x * (1.0f + t);
}

template <int BM, int WM, int WN>
__device__ __forceinline__ void task_qkv(int rt, int ct, const u16* Wq, const u16* Wk, const u16* Wv, unsigned char* ws, LAS char* lds) {
  const u16* xn = (const u16*)(ws + WS_XN);
  const int which = ct >> 5, bcol = (ct & 31) * 128, brow = rt * BM;
  if (which < 2) {
    const size_t ob = which == 0 ? WS_Q : WS_K;
    gemm_tile<BM, WM, WN, false>(xn, DM, which == 0 ? Wq : Wk, HD, DM, brow, bcol, lds, [&](int r, int c, f32x4 v) {
      wt8(ws, ob + ((size_t)r * HD + c) * 2, v2u{pack2(v[0], v[1]), pack2(v[2], v[3])});
    });
  } else {
    gemm_tile<BM, WM, WN, true>(xn, DM, Wv, HD, DM, brow, bcol, lds, [&](int r, int c, f32x4 v) {
      const int seg = r >> 5, t = r & 31, p = 8 * ((t & 15) >> 2) + 4 * (t >> 4);
      wt8(ws, WS_VT + (((size_t)seg * HD + c) * 32 + p) * 2, v2u{pack2(v[0], v[1]), pack2(v[2], v[3])});
    });
  }
}
template <int BM, int WM, int WN>
__device__ __forceinline__ void task_mlp1(int rt, int ct, const u16* w1, const u16* b1, unsigned char* ws, LAS char* lds) {
  const u16* y = (const u16*)(ws + WS_Y);
  gemm_tile<BM, WM, WN, false>(y, DM, w1, FF, DM, rt * BM, ct * 128, lds, [&](int r, int c, f32x4 v) {
    const v2u bb = *(const v2u*)(b1 + c);
    const float o0 = gelu_tanh(rbf(rbf(v[0]) + bflo(bb.x))), o1 = gelu_tanh(rbf(rbf(v[1]) + bfhi(bb.x)));
    const float o2 = gelu_tanh(rbf(rbf(v[2]) + bflo(bb.y))), o3 = gelu_tanh(rbf(rbf(v[3]) + bfhi(bb.y)));
    wt8(ws, WS_H + ((size_t)r * FF + c) * 2, v2u{pack2(o0, o1), pack2(o2, o3)});
  });
}
template <int BM, int WM, int WN>
__device__ __forceinline__ void task_mlp2(int rt, int ct, const u16* w2, const u16* b2, unsigned char* ws, LAS char* lds) {
  const u16* h = (const u16*)(ws + WS_H);
  gemm_tile<BM, WM, WN, false>(h, FF, w2, DM, FF, rt * BM, ct * 128, lds, [&](int r, int c, f32x4 v) {
    const v2u bb = *(const v2u*)(b2 + c);
    wt8(ws, WS_BO + ((size_t)r * DM + c) * 2, v2u{pack2(rbf(v[0]) + bflo(bb.x), rbf(v[1]) + bfhi(bb.x)), pack2(rbf(v[2]) + bflo(bb.y), rbf(v[3]) + bfhi(bb.y))});
  });
}
template <int BM, int WM, int WN>
__device__ __forceinline__ void task_wo(int rt, int ct, const u16* Wo, const u16* x, u16* out, unsigned char* ws, LAS char* lds) {
  const u16* ot = (const u16*)(ws + WS_OT);
  const u16* bo = (const u16*)(ws + WS_BO);
  gemm_tile<BM, WM, WN, false>(ot, HD, Wo, DM, HD, rt * BM, ct * 128, lds, [&](int r, int c, f32x4 v) {
    const v2u xx = *(const v2u*)(x + (size_t)r * DM + c);
    const v2u bb = *(const v2u*)(bo + (size_t)r * DM + c);
    const float o0 = rbf(bflo(xx.x) + rbf(v[0])) + bflo(bb.x), o1 = rbf(bfhi(xx.x) + rbf(v[1])) + bfhi(bb.x);
    const float o2 = rbf(bflo(xx.y) + rbf(v[2])) + bflo(bb.y), o3 = rbf(bfhi(xx.y) + rbf(v[3])) + bfhi(bb.y);
    *(v2u*)(out + (size_t)r * DM + c) = v2u{pack2(o0, o1), pack2(o2, o3)};
  });
}

__device__ __forceinline__ bool rdy(v4u v) { return max(max(v.x, v.y), max(v.z, v.w)) != SENT; }
__device__ __forceinline__ v4u ld_sc1(__amdgpu_buffer_rsrc_t rs, int off) { return __builtin_amdgcn_raw_buffer_load_b128(rs, off, 0, 16); }

template <int N>
__device__ __forceinline__ void poll(__amdgpu_buffer_rsrc_t rs, int off, v4u (&d)[N], unsigned* err) {
  unsigned spins = 0;
  for (;;) {
    unsigned mx = 0;
#pragma unroll
    for (int i = 0; i < N; ++i) d[i] = ld_sc1(rs, off + i * 1024);
    asm volatile("" ::: "memory");
#pragma unroll
    for (int i = 0; i < N; ++i) mx = max(max(mx, max(d[i].x, d[i].y)), max(d[i].z, d[i].w));
    if (__all(mx != SENT)) break;
    if (DLYR) __builtin_amdgcn_s_sleep(DLYR);
    if (++spins > SPINCAP) {
      if ((threadIdx.x & 63) == 0) __hip_atomic_store(err, 1u, __ATOMIC_RELAXED, __HIP_MEMORY_SCOPE_AGENT);
      break;
    }
    if ((spins & 1023u) == 0u && __hip_atomic_load(err, __ATOMIC_RELAXED, __HIP_MEMORY_SCOPE_AGENT) != 0u) break;
  }
}
__device__ __forceinline__ float xor16(float x) {
  auto r = __builtin_amdgcn_permlane16_swap(__builtin_bit_cast(unsigned, x), __builtin_bit_cast(unsigned, x), false, false);
  const int odd = (threadIdx.x >> 4) & 1;
  return __builtin_bit_cast(float, odd ? r[0] : r[1]);
}
__device__ __forceinline__ float xor32(float x) {
  auto r = __builtin_amdgcn_permlane32_swap(__builtin_bit_cast(unsigned, x), __builtin_bit_cast(unsigned, x), false, false);
  const int hi = (threadIdx.x >> 5) & 1;
  return __builtin_bit_cast(float, hi ? r[0] : r[1]);
}

template <bool ACQ = true>
__device__ __forceinline__ void wave_wait(unsigned* ctr, unsigned want, unsigned* err) {
  unsigned spins = 0;
  while (__hip_atomic_load(ctr, __ATOMIC_RELAXED, __HIP_MEMORY_SCOPE_AGENT) < want) {
    __builtin_amdgcn_s_sleep(4);
    if (++spins > SPINCAP) { __hip_atomic_store(err, 1u, __ATOMIC_RELAXED, __HIP_MEMORY_SCOPE_AGENT); break; }
    if ((spins & 1023u) == 0u && __hip_atomic_load(err, __ATOMIC_RELAXED, __HIP_MEMORY_SCOPE_AGENT) != 0u) break;
  }
  if (ACQ) {
    __builtin_amdgcn_fence(__ATOMIC_ACQUIRE, "agent");
    asm volatile("s_waitcnt vmcnt(0)" ::: "memory");
  } else {
    __builtin_amdgcn_fence(__ATOMIC_ACQUIRE, "wavefront");
  }
}
constexpr int CH_RED = 0;
constexpr int CH_VM = 24576;
constexpr int CH_RED2 = 25088;
constexpr int CH_WV = 27136;
constexpr int CH_KV = CH_WV + 65536;
constexpr int CH_LDS = CH_KV + 20480;

__device__ __forceinline__ void chain_role(int c, const u16* __restrict__ mem0, const u16* __restrict__ Wq, const u16* __restrict__ Wk,
                                           const u16* __restrict__ Wv, const u16* __restrict__ Wo, unsigned char* ws, LAS char* lds) {
  const int tid = threadIdx.x, lane = tid & 63, w = __builtin_amdgcn_readfirstlane(tid >> 6);
  const int fr = lane & 15, g = lane >> 4;
  const int h = c >> 2, q4 = c & 3, mh = c & 1, nb = c >> 1;
  unsigned* err = (unsigned*)(ws + WS_ERR);
  const __amdgpu_buffer_rsrc_t rs = __builtin_amdgcn_make_buffer_rsrc((void*)(ws + WS_HOP), 0, (int)HOP_BYTES, 0x00020000);
  v4u wq[4][4], wo[16];
  LAS v4u* wvl = (LAS v4u*)(lds + CH_WV) + w * 512 + lane;
  {
    LAS char* stg = lds + CH_WV + w * 8192;
    const int q = fr >> 2, p = lane & 3;
#pragma unroll
    for (int hf = 0; hf < 2; ++hf) {
#pragma unroll
      for (int i = 0; i < 8; ++i) {
        const int row = i * 32 + (lane >> 1), ch = lane & 1;
        v4u v = *(const v4u*)(Wo + (size_t)(512 * w + 256 * hf + row) * DM + 16 * nb + ch * 8);
        *(LAS v4u*)(stg + row * 32 + ch * 16) = v;
      }
      __builtin_amdgcn_s_waitcnt(0xC07F);
#pragma unroll
      for (int ks = 0; ks < 8; ++ks) {
        v2u lo = trread(stg + (32 * ks + 8 * g + q) * 32 + (4 * p) * 2);
        v2u hi = trread(stg + (32 * ks + 8 * g + 4 + q) * 32 + (4 * p) * 2);
        wo[hf * 8 + ks] = v4u{lo.x, lo.y, hi.x, hi.y};
      }
      __builtin_amdgcn_s_waitcnt(0xC07F);
    }
#pragma unroll
    for (int mat = 0; mat < 3; ++mat) {
      const u16* W = mat == 0 ? Wq : (mat == 1 ? Wk : Wv);
#pragma unroll
      for (int i = 0; i < 8; ++i) {
        const int row = i * 16 + (lane >> 2), ch = lane & 3;
        v4u v = *(const v4u*)(W + (size_t)(128 * w + row) * HD + h * 128 + 32 * q4 + ch * 8);
        *(LAS v4u*)(stg + row * 64 + ch * 16) = v;
      }
      __builtin_amdgcn_s_waitcnt(0xC07F);
      v4u tv[8];
#pragma unroll
      for (int dt = 0; dt < 2; ++dt)
#pragma unroll
        for (int ks = 0; ks < 4; ++ks) {
          v2u lo = trread(stg + (32 * ks + 8 * g + q) * 64 + (16 * dt + 4 * p) * 2);
          v2u hi = trread(stg + (32 * ks + 8 * g + 4 + q) * 64 + (16 * dt + 4 * p) * 2);
          if (mat < 2) wq[(mat & 1) * 2 + dt][ks] = v4u{lo.x, lo.y, hi.x, hi.y};
          else tv[dt * 4 + ks] = v4u{lo.x, lo.y, hi.x, hi.y};
        }
      __builtin_amdgcn_s_waitcnt(0xC07F);
      asm volatile("" ::: "memory");
      if (mat == 2) {
#pragma unroll
        for (int i = 0; i < 8; ++i) wvl[i * 64] = tv[i];
      }
    }
  }
  const float scale = 0.08838834764831845f;
  const float L2E = 1.4426950408889634f;
  LAS f32x4* red = (LAS f32x4*)(lds + CH_RED);
  LAS f32x4* red2 = (LAS f32x4*)(lds + CH_RED2);
  LAS u16* vml = (LAS u16*)(lds + CH_VM);
  LAS v4u* kvl = (LAS v4u*)(lds + CH_KV) + lane;
  const __amdgpu_buffer_rsrc_t rsw = __builtin_amdgcn_make_buffer_rsrc((void*)ws, 0, (int)WS_TOTAL, 0x00020000);
  auto kv_prefetch = [&](int sn) {
    if ((sn & 1) == 0) wave_wait<false>(err + CW_KV + 16 * (sn >> 1), 64u, err);
    v4u t[10];
#pragma unroll
    for (int tt = 0; tt < 2; ++tt)
#pragma unroll
      for (int j = 0; j < 4; ++j) t[tt * 4 + j] = ld_sc1(rsw, (int)(WS_K + ((size_t)(32 * sn + 16 * tt + fr) * HD + h * 128 + 32 * j + 8 * g) * 2));
#pragma unroll
    for (int dt = 0; dt < 2; ++dt) t[8 + dt] = ld_sc1(rsw, (int)(WS_VT + ((((size_t)sn * NH + h) * 128 + 32 * q4 + 16 * dt + fr) * 32 + 8 * g) * 2));
#pragma unroll
    for (int i = 0; i < 10; ++i) kvl[((sn & 1) * 10 + i) * 64] = t[i];
  };
  if (w == 2) kv_prefetch(0);

#define STAMP(i)
  for (int s = 0; s < NSEG; ++s) {
    v4u mf[2];
    if (s == 0) {
#pragma unroll
      for (int p = 0; p < 2; ++p)
        mf[p] = *(const v4u*)(mem0 + (size_t)(fr & 7) * DM + 128 * w + 32 * (2 * p + (fr >> 3)) + 8 * g);
    } else {
      if (DLY1) __builtin_amdgcn_s_sleep(DLY1);
      poll<2>(rs, (int)(HOP_MEM + (size_t)s * 16384) + ((16 * w + 4 * (fr >> 3) + g) * 8 + (fr & 7)) * 16, mf, err);
    }
    STAMP(0)
    {
      f32x4 a0[4], a1[4];
#pragma unroll
      for (int t = 0; t < 4; ++t) {
        a0[t] = mfma16(wq[t][0], mf[0], f32x4{0.f, 0.f, 0.f, 0.f});
        a1[t] = mfma16(wq[t][1], mf[0], f32x4{0.f, 0.f, 0.f, 0.f});
      }
#pragma unroll
      for (int t = 0; t < 4; ++t) {
        a0[t] = mfma16(wq[t][2], mf[1], a0[t]);
        a1[t] = mfma16(wq[t][3], mf[1], a1[t]);
      }
#pragma unroll
      for (int t = 0; t < 4; ++t) {
        f32x4 x = (fr & 8) ? a1[t] : a0[t];
#pragma unroll
        for (int r = 0; r < 4; ++r) x[r] += row_ror<8>(x[r]);
        if (fr < 8) red[(w * 6 + t) * 32 + g * 8 + fr] = x;
      }
    }
    __syncthreads();
    STAMP(1)
    if (tid >= 384) {
      const int u = tid - 384;
      const int t6 = u >> 5, gg = (u >> 3) & 3, m = u & 7;
      f32x4 x = red[t6 * 32 + (u & 31)];
#pragma unroll
      for (int ww = 1; ww < 8; ++ww) x += red[(ww * 6 + t6) * 32 + (u & 31)];
      const unsigned lo = pack2(x[0], x[1]), hi = pack2(x[2], x[3]);
      const int mat = t6 >> 1, dt = t6 & 1;
      const int off = (int)(HOP_QK + ((size_t)s * NH + h) * 4096) + ((q4 * 2 + mat) * 8 + m) * 64 + (16 * dt + 4 * gg) * 2;
      __builtin_amdgcn_raw_buffer_store_b64(v2u{nosent(lo), nosent(hi)}, rs, off, 0, 16);
    }
    {
      f32x4 a0[2], a1[2];
#pragma unroll
      for (int t = 0; t < 2; ++t) {
        a0[t] = mfma16(wvl[(t * 4 + 0) * 64], mf[0], f32x4{0.f, 0.f, 0.f, 0.f});
        a1[t] = mfma16(wvl[(t * 4 + 1) * 64], mf[0], f32x4{0.f, 0.f, 0.f, 0.f});
      }
#pragma unroll
      for (int t = 0; t < 2; ++t) {
        a0[t] = mfma16(wvl[(t * 4 + 2) * 64], mf[1], a0[t]);
        a1[t] = mfma16(wvl[(t * 4 + 3) * 64], mf[1], a1[t]);
      }
#pragma unroll
      for (int t = 0; t < 2; ++t) {
        f32x4 x = (fr & 8) ? a1[t] : a0[t];
#pragma unroll
        for (int r = 0; r < 4; ++r) x[r] += row_ror<8>(x[r]);
        if (fr < 8) red[(w * 6 + 4 + t) * 32 + g * 8 + fr] = x;
      }
    }
    __syncthreads();
    STAMP(2)
    if (w == 1) {
      const int dt = lane >> 5, gg = (lane >> 3) & 3, m = lane & 7;
      f32x4 x = red[(4 + dt) * 32 + (lane & 31)];
#pragma unroll
      for (int ww = 1; ww < 8; ++ww) x += red[(ww * 6 + 4 + dt) * 32 + (lane & 31)];
      const unsigned lo = pack2(x[0], x[1]), hi = pack2(x[2], x[3]);
      const int d = 16 * dt + 4 * gg;
      vml[(d + 0) * 8 + m] = (u16)(lo & 0xFFFF);
      vml[(d + 1) * 8 + m] = (u16)(lo >> 16);
      vml[(d + 2) * 8 + m] = (u16)(hi & 0xFFFF);
      vml[(d + 3) * 8 + m] = (u16)(hi >> 16);
      __builtin_amdgcn_s_waitcnt(0xC07F);
      asm volatile("" ::: "memory");
      if (lane < 32)
      {
        v4u t = *(LAS v4u*)(vml + lane * 8);
        t.x = nosent(t.x); t.y = nosent(t.y); t.z = nosent(t.z); t.w = nosent(t.w);
        __builtin_amdgcn_raw_buffer_store_b128(t, rs, (int)(HOP_VM + (((size_t)s * NH + h) * 128 + 32 * q4 + lane) * 16), 0, 16);
      }
    }
    if (s == NSEG - 1) break;
    if (w == 2 && s + 1 < NSEG - 1) kv_prefetch(s + 1);
    v4u pt, pm;
    if (w == 0) {
      v4u X[8];
      {
        const int base = (int)(HOP_QK + ((size_t)s * NH + h) * 4096) + (fr & 7) * 64 + g * 16;
        unsigned spins = 0;
        if (DLY2) __builtin_amdgcn_s_sleep(DLY2);
        for (;;) {
          unsigned mx = 0;
#pragma unroll
          for (int i = 0; i < 8; ++i) X[i] = ld_sc1(rs, base + i * 512);
          asm volatile("" ::: "memory");
#pragma unroll
          for (int i = 0; i < 8; ++i) mx = max(max(mx, max(X[i].x, X[i].y)), max(X[i].z, X[i].w));
          if (__all(mx != SENT)) break;
          if (++spins > SPINCAP) {
            if (lane == 0) __hip_atomic_store(err, 1u, __ATOMIC_RELAXED, __HIP_MEMORY_SCOPE_AGENT);
            break;
          }
          if ((spins & 1023u) == 0u && __hip_atomic_load(err, __ATOMIC_RELAXED, __HIP_MEMORY_SCOPE_AGENT) != 0u) break;
        }
      }
      STAMP(3)
      f32x4 s0 = {0.f, 0.f, 0.f, 0.f}, s1 = s0, s2 = s0;
#pragma unroll
      for (int j = 0; j < 4; ++j) {
        s1 = mfma16(kvl[((s & 1) * 10 + j) * 64], X[2 * j], s1);
        s2 = mfma16(kvl[((s & 1) * 10 + 4 + j) * 64], X[2 * j], s2);
        s0 = mfma16(X[2 * j + 1], X[2 * j], s0);
      }
      const bool up = fr >= 8;
      f32x4 st = up ? s2 : s1;
      const float m01 = up ? s0[1] : s0[0], m23 = up ? s0[3] : s0[2];
      const float sm = g >= 2 ? m23 : m01;
      unsigned p0 = pack2(st[0], st[1]), p1 = pack2(st[2], st[3]), p2 = pack2(sm, sm);
      p0 = pack2(bflo(p0) * scale, bfhi(p0) * scale);
      p1 = pack2(bflo(p1) * scale, bfhi(p1) * scale);
      p2 = pack2(bflo(p2) * scale, bflo(p2) * scale);
      float v0 = bflo(p0), v1 = bfhi(p0), v2 = bflo(p1), v3 = bfhi(p1), v4 = bflo(p2);
      float mx = fmaxf(fmaxf(fmaxf(v0, v1), fmaxf(v2, v3)), v4);
      mx = fmaxf(mx, row_ror<8>(mx));
      mx = fmaxf(mx, xor16(mx));
      mx = fmaxf(mx, xor32(mx));
      const float nm = -mx * L2E;
      v0 = __builtin_amdgcn_exp2f(__builtin_fmaf(v0, L2E, nm));
      v1 = __builtin_amdgcn_exp2f(__builtin_fmaf(v1, L2E, nm));
      v2 = __builtin_amdgcn_exp2f(__builtin_fmaf(v2, L2E, nm));
      v3 = __builtin_amdgcn_exp2f(__builtin_fmaf(v3, L2E, nm));
      v4 = __builtin_amdgcn_exp2f(__builtin_fmaf(v4, L2E, nm));
      float sum = ((v0 + v1) + (v2 + v3)) + v4;
      sum += row_ror<8>(sum);
      sum += xor16(sum);
      sum += xor32(sum);
      const float inv = __builtin_amdgcn_rcpf(sum);
      pt.x = pack2(v0 * inv, v1 * inv);
      pt.y = pack2(v2 * inv, v3 * inv);
      pt.z = (unsigned)__builtin_amdgcn_update_dpp(0, (int)pt.x, 0x128, 0xf, 0xf, false);
      pt.w = (unsigned)__builtin_amdgcn_update_dpp(0, (int)pt.y, 0x128, 0xf, 0xf, false);
      v4 *= inv;
      pm.x = pack2(v4, row_ror<8>(v4));
      pm.y = 0u; pm.z = 0u; pm.w = 0u;
    }
    __syncthreads();
    if (w == 0) {
      f32x4 o[2];
#pragma unroll
      for (int dt = 0; dt < 2; ++dt) o[dt] = mfma16(kvl[((s & 1) * 10 + 8 + dt) * 64], pt, f32x4{0.f, 0.f, 0.f, 0.f});
#pragma unroll
      for (int dt = 0; dt < 2; ++dt) {
        const unsigned t = *(LAS unsigned*)(vml + (16 * dt + fr) * 8 + 4 * (g & 1) + 2 * (g >> 1));
        o[dt] = mfma16(v4u{t, 0u, 0u, 0u}, pm, o[dt]);
      }
      if (fr < 8) {
#pragma unroll
        for (int dt = 0; dt < 2; ++dt) {
          const int k8 = h * 16 + 4 * q4 + 2 * dt + (g >> 1);
          const int off = (int)(HOP_OB + (size_t)s * 65536) + (((fr >> 2) * 512 + k8) * 4 + (fr & 3)) * 16 + (g & 1) * 8;
          __builtin_amdgcn_raw_buffer_store_b64(v2u{nosent(pack2(o[dt][0], o[dt][1])), nosent(pack2(o[dt][2], o[dt][3]))}, rs, off, 0, 16);
        }
      }
    }
    __syncthreads();
    STAMP(4)
    {
      v4u of[4];
      if (DLY3) __builtin_amdgcn_s_sleep(DLY3);
      poll<4>(rs, (int)(HOP_OB + (size_t)s * 65536) + ((mh * 512 + 64 * w + 4 * (fr >> 2) + g) * 4 + (fr & 3)) * 16, of, err);
      STAMP(5)
      f32x4 a[4];
#pragma unroll
      for (int r4 = 0; r4 < 4; ++r4) a[r4] = mfma16(wo[r4], of[0], f32x4{0.f, 0.f, 0.f, 0.f});
#pragma unroll
      for (int i = 1; i < 4; ++i)
#pragma unroll
        for (int r4 = 0; r4 < 4; ++r4) a[r4] = mfma16(wo[4 * i + r4], of[i], a[r4]);
      const int r4 = fr >> 2;
      f32x4 x = r4 == 0 ? a[0] : (r4 == 1 ? a[1] : (r4 == 2 ? a[2] : a[3]));
#pragma unroll
      for (int r = 0; r < 4; ++r) { x[r] += row_ror<8>(x[r]); x[r] += row_ror<4>(x[r]); }
      if (fr < 4) red2[w * 16 + g * 4 + fr] = x;
    }
    __syncthreads();
    if (tid < 16) {
      f32x4 x = red2[tid];
#pragma unroll
      for (int ww = 1; ww < 8; ++ww) x += red2[ww * 16 + tid];
      const int gg = tid >> 2, m4 = tid & 3;
      const int k8 = 2 * nb + (gg >> 1);
      const int off = (int)(HOP_MEM + (size_t)(s + 1) * 16384) + (k8 * 8 + 4 * mh + m4) * 16 + (gg & 1) * 8;
      __builtin_amdgcn_raw_buffer_store_b64(v2u{nosent(pack2(x[0], x[1])), nosent(pack2(x[2], x[3]))}, rs, off, 0, 16);
    }
    STAMP(6)
  }
}

__device__ __forceinline__ void attn_unit(int s, int h, int lane, unsigned char* ws) {
  const int fr = lane & 15, g = lane >> 4;
  const u16* Q = (const u16*)(ws + WS_Q);
  const u16* Kx = (const u16*)(ws + WS_K);
  const u16* Vt = (const u16*)(ws + WS_VT);
  const __amdgpu_buffer_rsrc_t rs = __builtin_amdgcn_make_buffer_rsrc((void*)(ws + WS_HOP), 0, (int)HOP_BYTES, 0x00020000);
  unsigned* err = (unsigned*)(ws + WS_ERR);
  v4u km[4];
  poll<4>(rs, (int)(HOP_QK + ((size_t)s * NH + h) * 4096) + 512 + (fr & 7) * 64 + g * 16, km, err);
  v4u vmr[2];
  poll<2>(rs, (int)(HOP_VM + ((size_t)s * NH + h) * 2048) + lane * 16, vmr, err);
  const float scale = 0.08838834764831845f;
  f32x4 sc[3][2];
#pragma unroll
  for (int a = 0; a < 3; ++a)
#pragma unroll
    for (int b = 0; b < 2; ++b) sc[a][b] = f32x4{0.f, 0.f, 0.f, 0.f};
#pragma unroll
  for (int j = 0; j < 4; ++j) {
    v4u qf[2], kf[3];
#pragma unroll
    for (int t = 0; t < 2; ++t) {
      qf[t] = *(const v4u*)(Q + (size_t)(32 * s + 16 * t + fr) * HD + h * 128 + 32 * j + 8 * g);
      kf[1 + t] = *(const v4u*)(Kx + (size_t)(32 * s + 16 * t + fr) * HD + h * 128 + 32 * j + 8 * g);
    }
    kf[0] = km[j];
#pragma unroll
    for (int a = 0; a < 3; ++a)
#pragma unroll
      for (int b = 0; b < 2; ++b) sc[a][b] = mfma16(kf[a], qf[b], sc[a][b]);
  }
  v4u pt[2], pm[2];
#pragma unroll
  for (int b = 0; b < 2; ++b) {
    const int qi = 16 * b + fr;
    float v[12];
    bool ok[12];
#pragma unroll
    for (int r = 0; r < 4; ++r) {
      v[r] = rbf(rbf(sc[0][b][r]) * scale); ok[r] = g < 2;
      v[4 + r] = rbf(rbf(sc[1][b][r]) * scale); ok[4 + r] = (4 * g + r) <= qi;
      v[8 + r] = rbf(rbf(sc[2][b][r]) * scale); ok[8 + r] = (16 + 4 * g + r) <= qi;
    }
    float mx = -3.0e38f;
#pragma unroll
    for (int i = 0; i < 12; ++i) mx = ok[i] ? fmaxf(mx, v[i]) : mx;
    mx = fmaxf(mx, __shfl_xor(mx, 16));
    mx = fmaxf(mx, __shfl_xor(mx, 32));
    float sum = 0.f;
#pragma unroll
    for (int i = 0; i < 12; ++i) { v[i] = ok[i] ? __expf(v[i] - mx) : 0.f; sum += v[i]; }
    sum += __shfl_xor(sum, 16);
    sum += __shfl_xor(sum, 32);
    const float inv = 1.0f / sum;
    pt[b].x = pack2(v[4] * inv, v[5] * inv); pt[b].y = pack2(v[6] * inv, v[7] * inv);
    pt[b].z = pack2(v[8] * inv, v[9] * inv); pt[b].w = pack2(v[10] * inv, v[11] * inv);
    pm[b].x = pack2(v[0] * inv, v[1] * inv); pm[b].y = pack2(v[2] * inv, v[3] * inv); pm[b].z = 0u; pm[b].w = 0u;
  }
#pragma unroll
  for (int dt = 0; dt < 8; ++dt) {
    v4u vtf = *(const v4u*)(Vt + (((size_t)s * NH + h) * 128 + 16 * dt + fr) * 32 + 8 * g);
    v4u vmf;
    {
      const int src = (16 * dt + fr) & 63;
      const v4u vv = vmr[dt >> 2];
      const unsigned a0 = __shfl(vv.x, src), a1 = __shfl(vv.y, src), a2 = __shfl(vv.z, src), a3 = __shfl(vv.w, src);
      vmf.x = g == 0 ? a0 : (g == 1 ? a2 : 0u);
      vmf.y = g == 0 ? a1 : (g == 1 ? a3 : 0u);
      vmf.z = 0u; vmf.w = 0u;
    }
#pragma unroll
    for (int b = 0; b < 2; ++b) {
      f32x4 o = {0.f, 0.f, 0.f, 0.f};
      o = mfma16(vtf, pt[b], o);
      o = mfma16(vmf, pm[b], o);
      wt8(ws, WS_OT + ((size_t)(32 * s + 16 * b + fr) * HD + h * 128 + 16 * dt + 4 * g) * 2, v2u{pack2(o[0], o[1]), pack2(o[2], o[3])});
    }
  }
}
__device__ __forceinline__ void wg_signal(unsigned* ctr, int n) {
  asm volatile("s_waitcnt vmcnt(0)" ::: "memory");
  __syncthreads();
  if (threadIdx.x < n) __hip_atomic_fetch_add(ctr + 16 * threadIdx.x, 1u, __ATOMIC_RELAXED, __HIP_MEMORY_SCOPE_AGENT);
}
__device__ __forceinline__ void wg_wait(unsigned* ctr, unsigned want, unsigned* err) {
  if (threadIdx.x < 64) wave_wait(ctr, want, err);
  __syncthreads();
}

__global__ __launch_bounds__(512, 2) void k_mega(const u16* x, const u16* mem0, const u16* Wq, const u16* Wk, const u16* Wv, const u16* Wo,
                                                 const u16* w1, const u16* b1, const u16* w2, const u16* b2, u16* out, unsigned char* ws) {
  extern __shared__ __attribute__((aligned(16))) char smem[];
  LAS char* lds = (LAS char*)smem;
  const int b = blockIdx.x;
#define ASTAMP(i)
  ASTAMP(0)
  const int xcd = b & 7, i = b >> 4;
  if (((b >> 3) & 1) == 0) {
    const int c = ((xcd * 4 + (i >> 2)) << 2) | (i & 3);
    chain_role(c, mem0, Wq, Wk, Wv, Wo, ws, lds);
    ASTAMP(7)
    return;
  }
  const int gi = i * 8 + xcd;
  unsigned* cw = (unsigned*)(ws + WS_ERR);
  unsigned* err = cw + CW_ERR;
  task_qkv<64, 2, 4>(gi >> 6, 32 + (gi & 63), Wq, Wk, Wv, ws, lds);
  wg_signal(cw + CW_KV + 16 * (gi >> 6), 1);
  for (int t = gi; t < 64 + 7 * 64; t += 128) {
    if (t < 64) {
      task_qkv<128, 2, 4>(1, 32 + t, Wq, Wk, Wv, ws, lds);
      wg_signal(cw + CW_KV + 16 * 2, 2);
    } else {
      const int rt = 1 + ((t - 64) >> 6);
      task_qkv<256, 4, 2>(rt, 32 + ((t - 64) & 63), Wq, Wk, Wv, ws, lds);
      wg_signal(cw + CW_KV + 16 * 4 * rt, 4);
    }
  }
  ASTAMP(1)
  for (int t = gi; t < 8 * 28; t += 128) task_mlp1<256, 4, 2>(t & 7, t >> 3, w1, b1, ws, lds);
  wg_signal(cw + CW_MLP1, 1);
  for (int t = gi; t < 8 * 32; t += 128) task_qkv<256, 4, 2>(t & 7, t >> 3, Wq, Wk, Wv, ws, lds);
  wg_signal(cw + CW_Q, 1);
  ASTAMP(2)
  wg_wait(cw + CW_MLP1, 128u, err);
  task_mlp2<128, 2, 4>(gi >> 3, gi & 7, w2, b2, ws, lds);
  wg_signal(cw + CW_MLP2, 1);
  ASTAMP(3)
  if (threadIdx.x < 64) {
    const int l = threadIdx.x;
    unsigned* p = l < 32 ? cw + CW_KV + 16 * l : (l == 32 ? cw + CW_Q : cw + CW_MLP2);
    const unsigned want = l < 32 ? 64u : (l < 34 ? 128u : 0u);
    unsigned spins = 0;
    while (!__all(__hip_atomic_load(p, __ATOMIC_RELAXED, __HIP_MEMORY_SCOPE_AGENT) >= want)) {
      __builtin_amdgcn_s_sleep(8);
      if (++spins > SPINCAP) { __hip_atomic_store(err, 1u, __ATOMIC_RELAXED, __HIP_MEMORY_SCOPE_AGENT); break; }
      if ((spins & 1023u) == 0u && __hip_atomic_load(err, __ATOMIC_RELAXED, __HIP_MEMORY_SCOPE_AGENT) != 0u) break;
    }
    __builtin_amdgcn_fence(__ATOMIC_ACQUIRE, "agent");
    asm volatile("s_waitcnt vmcnt(0)" ::: "memory");
  }
  __syncthreads();
#define do_attn(sg_, hg_, ci_) { attn_unit((sg_), (hg_) * 8 + (threadIdx.x >> 6), threadIdx.x & 63, ws); wg_signal(cw + CW_AT + 16 * (ci_), 1); }
  for (int t = gi; t < 160; t += 128) do_attn(t >> 2, t & 3, (t >> 4) * 2)
  if (gi < 48) {
    const int pr = 8 + (gi >> 3);
    wg_wait(cw + CW_AT + 16 * 2 * pr, 16u, err);
    task_wo<128, 2, 4>(pr, gi & 7, Wo, x, out, ws, lds);
  } else if (gi < 64) {
    for (int t = 160 + (gi - 48); t < 224; t += 16) do_attn(t >> 2, t & 3, (t >> 4) * 2)
  } else {
    {
      const int pr = (gi - 64) >> 3;
      wg_wait(cw + CW_AT + 16 * 2 * pr, 16u, err);
      task_wo<128, 2, 4>(pr, gi & 7, Wo, x, out, ws, lds);
    }
    const int idx = gi - 64, ct = idx >> 3, ks = idx & 7;
    const __amdgpu_buffer_rsrc_t rsw = __builtin_amdgcn_make_buffer_rsrc((void*)ws, 0, (int)WS_TOTAL, 0x00020000);
    for (int u = 28; u < 32; ++u) {
      if (idx < 8) do_attn(2 * u + (idx >> 2), idx & 3, u)
      wg_wait(cw + CW_AT + 16 * u, 8u, err);
      const int slot = (u - 28) * 8 + ct;
      const size_t sbase = WS_SLAB + (size_t)slot * 8 * 32768;
      gemm_tile<64, 2, 4, false>((const u16*)(ws + WS_OT) + ks * 512, HD, Wo + (size_t)ks * 512 * DM, DM, 512, u * 64, ct * 128, lds,
                                 [&](int r, int c, f32x4 v) {
                                   __builtin_amdgcn_raw_buffer_store_b128(__builtin_bit_cast(v4u, v), rsw,
                                       (int)(sbase + (size_t)ks * 32768 + (((r & 63) * 128 + (c & 127)) * 4)), 0, 16);
                                 });
      wg_signal(cw + CW_SK + 16 * slot, 1);
      if (threadIdx.x < 64) wave_wait<false>(cw + CW_SK + 16 * slot, 8u, err);
      __syncthreads();
      if (threadIdx.x < 256) {
        const int rr = 8 * ks + (threadIdx.x >> 5), cc = (threadIdx.x & 31) * 4;
        v4u pv[8];
#pragma unroll
        for (int k2 = 0; k2 < 8; ++k2) pv[k2] = ld_sc1(rsw, (int)(sbase + (size_t)k2 * 32768 + (rr * 128 + cc) * 4));
        f32x4 v = __builtin_bit_cast(f32x4, pv[0]);
#pragma unroll
        for (int k2 = 1; k2 < 8; ++k2) v += __builtin_bit_cast(f32x4, pv[k2]);
        const int r = u * 64 + rr, c = ct * 128 + cc;
        const v2u xx = *(const v2u*)(x + (size_t)r * DM + c);
        const v2u bb = *(const v2u*)((const u16*)(ws + WS_BO) + (size_t)r * DM + c);
        const float o0 = rbf(bflo(xx.x) + rbf(v[0])) + bflo(bb.x), o1 = rbf(bfhi(xx.x) + rbf(v[1])) + bfhi(bb.x);
        const float o2 = rbf(bflo(xx.y) + rbf(v[2])) + bflo(bb.y), o3 = rbf(bfhi(xx.y) + rbf(v[3])) + bfhi(bb.y);
        *(v2u*)(out + (size_t)r * DM + c) = v2u{pack2(o0, o1), pack2(o2, o3)};
      }
    }
  }
  ASTAMP(7)
}

extern "C" void kernel_launch(void* const* d_in, const int* in_sizes, int n_in, void* d_out, int out_size, void* d_ws, size_t ws_size,
                              hipStream_t stream) {
  const u16* x = (const u16*)d_in[0];
  const u16* mem0 = (const u16*)d_in[1];
  const u16* Wq = (const u16*)d_in[2];
  const u16* Wk = (const u16*)d_in[3];
  const u16* Wv = (const u16*)d_in[4];
  const u16* Wo = (const u16*)d_in[5];
  const u16* w1 = (const u16*)d_in[8];
  const u16* b1 = (const u16*)d_in[9];
  const u16* w2 = (const u16*)d_in[10];
  const u16* b2 = (const u16*)d_in[11];
  unsigned char* ws = (unsigned char*)d_ws;
  u16* out = (u16*)d_out;
  constexpr int L256 = GemmCfg<256>::LDS_BYTES;
  constexpr int LMEGA = L256 > CH_LDS ? L256 : CH_LDS;
  (void)hipFuncSetAttribute((const void*)k_mega, hipFuncAttributeMaxDynamicSharedMemorySize, LMEGA);
  k_ln<<<S_LEN / 4, 256, 0, stream>>>(x, ws);
  k_mega<<<256, 512, LMEGA, stream>>>(x, mem0, Wq, Wk, Wv, Wo, w1, b1, w2, b2, out, ws);
}
```

```cpp
#include <hip/hip_runtime.h>
#include <hip/hip_bf16.h>
#include <stdint.h>

constexpr int S_LEN = 2048, DM = 1024, NH = 32, DH = 128, HD = NH * DH;
constexpr int MEMN = 8, SEG = 32, NSEG = S_LEN / SEG;
constexpr int FF = 3584;

typedef unsigned v4u __attribute__((ext_vector_type(4)));
typedef unsigned v2u __attribute__((ext_vector_type(2)));
typedef float f32x4 __attribute__((ext_vector_type(4)));
typedef float f32x2 __attribute__((ext_vector_type(2)));
typedef short bf16x8 __attribute__((ext_vector_type(8)));
typedef short s16x4 __attribute__((ext_vector_type(4)));
typedef __bf16 bfx2 __attribute__((ext_vector_type(2)));
typedef unsigned short u16;
#define LAS __attribute__((address_space(3)))
#define GAS __attribute__((address_space(1)))

constexpr size_t WS_XN = 0;
constexpr size_t WS_Y = WS_XN + (size_t)S_LEN * DM * 2;
constexpr size_t WS_Q = WS_Y + (size_t)S_LEN * DM * 2;
constexpr size_t WS_K = WS_Q + (size_t)S_LEN * HD * 2;
constexpr size_t WS_VT = WS_K + (size_t)S_LEN * HD * 2;
constexpr size_t WS_H = WS_VT + (size_t)S_LEN * HD * 2;
constexpr size_t WS_BO = WS_H + (size_t)S_LEN * FF * 2;
constexpr size_t WS_OT = WS_BO + (size_t)S_LEN * DM * 2;
constexpr size_t WS_SLAB = WS_OT + (size_t)S_LEN * HD * 2;
constexpr int TAIL_U0 = 20;
constexpr size_t WS_HOP = WS_SLAB + (size_t)96 * 16 * 64 * 128 * 4;
constexpr size_t HOP_MEM = 0;
constexpr size_t HOP_QK = HOP_MEM + (size_t)NSEG * 16384;
constexpr size_t HOP_OB = HOP_QK + (size_t)NSEG * NH * 4096;
constexpr size_t HOP_VM = HOP_OB + (size_t)NSEG * 65536;
constexpr size_t HOP_BYTES = HOP_VM + (size_t)NSEG * HD * MEMN * 2;
constexpr size_t WS_ERR = WS_HOP + HOP_BYTES;
constexpr size_t WS_TOTAL = WS_ERR + 16384;
constexpr int CW_ERR = 0, CW_MLP1 = 16, CW_Q = 32, CW_MLP2 = 48, CW_KV = 64  , CW_AT = 64 + 512  , CW_SK = 64 + 1024  ;

#define DLY1 14
#define DLY2 0
#define DLY3 14
#define DLYR 0
constexpr unsigned SENT = 0xFFFFFFFFu;
constexpr unsigned SPINCAP = 1u << 20;

__device__ __forceinline__ float bf2f(unsigned b16) { return __builtin_bit_cast(float, b16 << 16); }
__device__ __forceinline__ float bflo(unsigned x) { return __builtin_bit_cast(float, x << 16); }
__device__ __forceinline__ float bfhi(unsigned x) { return __builtin_bit_cast(float, x & 0xFFFF0000u); }
__device__ __forceinline__ unsigned pack2(float a, float b) {
  f32x2 v = {a, b};
  bfx2 r = __builtin_convertvector(v, bfx2);
  return __builtin_bit_cast(unsigned, r);
}
__device__ __forceinline__ float rbf(float a) { return (float)(__bf16)a; }
__device__ __forceinline__ unsigned nosent(unsigned x) { return x == SENT ? 0xFFFFFFFEu : x; }
__device__ __forceinline__ f32x4 mfma16(v4u a, v4u b, f32x4 c) {
  return __builtin_amdgcn_mfma_f32_16x16x32_bf16(__builtin_bit_cast(bf16x8, a), __builtin_bit_cast(bf16x8, b), c, 0, 0, 0);
}
__device__ __forceinline__ v2u trread(LAS const char* p) {
  typedef short v4i16_t __attribute__((ext_vector_type(4)));
  return __builtin_bit_cast(v2u, __builtin_amdgcn_ds_read_tr16_b64_v4i16((LAS v4i16_t*)p));
}
__device__ __forceinline__ float ror(float x, int) = delete;
template <int N> __device__ __forceinline__ float row_ror(float x) {
  return __builtin_bit_cast(float, __builtin_amdgcn_update_dpp(0, __builtin_bit_cast(int, x), 0x120 + N, 0xf, 0xf, false));
}

__device__ __forceinline__ float wave_sum(float v) {
#pragma unroll
  for (int o = 32; o > 0; o >>= 1) v += __shfl_xor(v, o);
  return v;
}
__device__ __forceinline__ void ln16(float (&v)[16]) {
  float s = 0.f;
#pragma unroll
  for (int i = 0; i < 16; ++i) s += v[i];
  s = wave_sum(s);
  const float mean = s * (1.0f / DM);
  float q = 0.f;
#pragma unroll
  for (int i = 0; i < 16; ++i) { float d = v[i] - mean; q += d * d; }
  q = wave_sum(q);
  const float mb = rbf(mean);
  const float vb = rbf(q * (1.0f / DM));
  const float den = rbf(sqrtf(rbf(vb + 1e-5f)));
#pragma unroll
  for (int i = 0; i < 16; ++i) v[i] = rbf(rbf(v[i] - mb) / den);
}
__global__ __launch_bounds__(256) void k_ln(const u16* __restrict__ x, unsigned char* __restrict__ ws) {
  const int tid = threadIdx.x, lane = tid & 63, w = tid >> 6;
  const int row = blockIdx.x * 4 + w;
  {
    v4u s = {SENT, SENT, SENT, SENT};
    v4u* p = (v4u*)(ws + WS_HOP);
    const size_t n = HOP_BYTES / 16;
    for (size_t i = (size_t)blockIdx.x * 256 + tid; i < n; i += (size_t)gridDim.x * 256) p[i] = s;
    if (blockIdx.x < 16) ((unsigned*)(ws + WS_ERR))[blockIdx.x * 256 + tid] = 0u;
  }
  const v4u* xr = (const v4u*)(x + (size_t)row * DM);
  v4u a = xr[lane], b = xr[64 + lane];
  float v[16];
  v[0] = bflo(a.x); v[1] = bfhi(a.x); v[2] = bflo(a.y); v[3] = bfhi(a.y);
  v[4] = bflo(a.z); v[5] = bfhi(a.z); v[6] = bflo(a.w); v[7] = bfhi(a.w);
  v[8] = bflo(b.x); v[9] = bfhi(b.x); v[10] = bflo(b.y); v[11] = bfhi(b.y);
  v[12] = bflo(b.z); v[13] = bfhi(b.z); v[14] = bflo(b.w); v[15] = bfhi(b.w);
  ln16(v);
  v4u* xn = (v4u*)(ws + WS_XN + (size_t)row * DM * 2);
  xn[lane] = v4u{pack2(v[0], v[1]), pack2(v[2], v[3]), pack2(v[4], v[5]), pack2(v[6], v[7])};
  xn[64 + lane] = v4u{pack2(v[8], v[9]), pack2(v[10], v[11]), pack2(v[12], v[13]), pack2(v[14], v[15])};
  ln16(v);
  v4u* y = (v4u*)(ws + WS_Y + (size_t)row * DM * 2);
  y[lane] = v4u{pack2(v[0], v[1]), pack2(v[2], v[3]), pack2(v[4], v[5]), pack2(v[6], v[7])};
  y[64 + lane] = v4u{pack2(v[8], v[9]), pack2(v[10], v[11]), pack2(v[12], v[13]), pack2(v[14], v[15])};
}

template <int BM> struct GemmCfg {
  static constexpr int BN = 128, BK = 64;
  static constexpr int A_BYTES = BM * BK * 2, B_BYTES = BK * BN * 2, ST_BYTES = A_BYTES + B_BYTES;
  static constexpr int NST = BM == 256 ? 3 : (BM == 128 ? 4 : 6);
  static constexpr int LDS_BYTES = NST * ST_BYTES;
};

template <int N> __device__ __forceinline__ void wait_vm() { asm volatile("s_waitcnt vmcnt(%0)" ::"n"(N) : "memory"); }

template <int BM, int WM, int WN, bool TOUT, class Epi>
__device__ __forceinline__ void gemm_tile(const u16* __restrict__ A, int lda, const u16* __restrict__ B, int ldb, int K,
                                          int brow, int bcol, LAS char* lds, Epi epi) {
  using C = GemmCfg<BM>;
  constexpr int TM = BM / WM / 16, TN = C::BN / WN / 16;
  constexpr int AI = C::A_BYTES / 8192, BI = C::B_BYTES / 8192;
  const int tid = threadIdx.x, lane = tid & 63, w = __builtin_amdgcn_readfirstlane(tid >> 6);
  const int wr = w / WN, wc = w % WN, fr = lane & 15, fq = lane >> 4;
  const u16* asrc[AI];
  const u16* bsrc[BI];
#pragma unroll
  for (int i = 0; i < AI; ++i) {
    const int ii = w * AI + i, row = ii * 8 + (lane >> 3), ch = (lane & 7) ^ (row & 7);
    asrc[i] = A + (size_t)(brow + row) * lda + ch * 8;
  }
#pragma unroll
  for (int i = 0; i < BI; ++i) {
    const int ii = w * BI + i, row = ii * 4 + (lane >> 4), ch = (lane & 15) ^ (((row & 3) << 2) | ((row >> 2) & 3));
    bsrc[i] = B + (size_t)row * ldb + bcol + ch * 8;
  }
  auto stage = [&](int t, int buf) {
    LAS char* base = lds + buf * C::ST_BYTES;
#pragma unroll
    for (int i = 0; i < AI; ++i)
      __builtin_amdgcn_global_load_lds((const GAS void*)(asrc[i] + t * 64), (LAS void*)(base + (w * AI + i) * 1024), 16, 0, 0);
#pragma unroll
    for (int i = 0; i < BI; ++i)
      __builtin_amdgcn_global_load_lds((const GAS void*)(bsrc[i] + (size_t)t * 64 * ldb), (LAS void*)(base + C::A_BYTES + (w * BI + i) * 1024), 16, 0, 0);
  };
  int aoff[TM][2], boff[TN][2][2];
#pragma unroll
  for (int m = 0; m < TM; ++m)
#pragma unroll
    for (int k = 0; k < 2; ++k) {
      const int row = wr * (TM * 16) + m * 16 + fr, ch = k * 4 + fq;
      aoff[m][k] = row * 128 + ((ch ^ (row & 7)) * 16);
    }
  {
    const int q = fr >> 2, p = lane & 3;
#pragma unroll
    for (int n = 0; n < TN; ++n)
#pragma unroll
      for (int k = 0; k < 2; ++k)
#pragma unroll
        for (int t = 0; t < 2; ++t) {
          const int row = 32 * k + 8 * fq + 4 * t + q, c = wc * TN + n, ch = 2 * c + (p >> 1);
          boff[n][k][t] = C::A_BYTES + 256 * row + 16 * (ch ^ (((row & 3) << 2) | ((row >> 2) & 3))) + 8 * (p & 1);
        }
  }
  f32x4 acc[TM][TN];
#pragma unroll
  for (int m = 0; m < TM; ++m)
#pragma unroll
    for (int n = 0; n < TN; ++n) acc[m][n] = f32x4{0.f, 0.f, 0.f, 0.f};
  const int nt = K / 64;
  constexpr int NST = C::NST, PER = AI + BI;
#pragma unroll
  for (int i = 0; i < NST - 1; ++i)
    if (i < nt) stage(i, i);
  int rb = 0, sb = NST - 1;
  for (int t = 0; t < nt; ++t) {
    {
      const int rem = nt - 1 - t;
      if (rem >= NST - 2) wait_vm<(NST - 2) * PER>();
      else if (NST > 5 && rem == 3) wait_vm<3 * PER>();
      else if (NST > 4 && rem == 2) wait_vm<2 * PER>();
      else if (NST > 3 && rem == 1) wait_vm<PER>();
      else wait_vm<0>();
    }
    asm volatile("s_waitcnt lgkmcnt(0)" ::: "memory");
    __builtin_amdgcn_s_barrier();
    asm volatile("" ::: "memory");
    if (t + NST - 1 < nt) stage(t + NST - 1, sb);
    LAS const char* base = lds + rb * C::ST_BYTES;
    v4u af[TM][2], bfr[TN][2];
#pragma unroll
    for (int k = 0; k < 2; ++k) {
#pragma unroll
      for (int n = 0; n < TN; ++n) {
        v2u lo = trread(base + boff[n][k][0]), hi = trread(base + boff[n][k][1]);
        bfr[n][k] = v4u{lo.x, lo.y, hi.x, hi.y};
      }
#pragma unroll
      for (int m = 0; m < TM; ++m) af[m][k] = *(LAS const v4u*)(base + aoff[m][k]);
    }
    __builtin_amdgcn_s_setprio(1);
#pragma unroll
    for (int k = 0; k < 2; ++k)
#pragma unroll
      for (int m = 0; m < TM; ++m)
#pragma unroll
        for (int n = 0; n < TN; ++n)
          acc[m][n] = TOUT ? mfma16(af[m][k], bfr[n][k], acc[m][n]) : mfma16(bfr[n][k], af[m][k], acc[m][n]);
    __builtin_amdgcn_s_setprio(0);
    rb = rb == NST - 1 ? 0 : rb + 1;
    sb = sb == NST - 1 ? 0 : sb + 1;
  }
#pragma unroll
  for (int m = 0; m < TM; ++m)
#pragma unroll
    for (int n = 0; n < TN; ++n) {
      const int r0 = brow + wr * (TM * 16) + m * 16, c0 = bcol + wc * (TN * 16) + n * 16;
      if (TOUT) epi(r0 + fq * 4, c0 + fr, acc[m][n]);
      else epi(r0 + fr, c0 + fq * 4, acc[m][n]);
    }
  asm volatile("s_waitcnt lgkmcnt(0)" ::: "memory");
  __builtin_amdgcn_s_barrier();
}

__device__ __forceinline__ void wt8(unsigned char* ws, size_t off, v2u v) {
  const __amdgpu_buffer_rsrc_t rs = __builtin_amdgcn_make_buffer_rsrc((void*)ws, 0, (int)WS_TOTAL, 0x00020000);
  __builtin_amdgcn_raw_buffer_store_b64(v, rs, (int)off, 0, 16);
}
__device__ __forceinline__ float gelu_tanh(float x) {
  const float u = 0.7978845608028654f * (x + 0.044715f * x * x * x);
  const float e = __expf(2.0f * u);
  const float t = 1.0f - 2.0f / (e + 1.0f);
  return 0.5f * x * (1.0f + t);
}

template <int BM, int WM, int WN>
__device__ __forceinline__ void task_qkv(int rt, int ct, const u16* Wq, const u16* Wk, const u16* Wv, unsigned char* ws, LAS char* lds) {
  const u16* xn = (const u16*)(ws + WS_XN);
  const int which = ct >> 5, bcol = (ct & 31) * 128, brow = rt * BM;
  if (which < 2) {
    const size_t ob = which == 0 ? WS_Q : WS_K;
    gemm_tile<BM, WM, WN, false>(xn, DM, which == 0 ? Wq : Wk, HD, DM, brow, bcol, lds, [&](int r, int c, f32x4 v) {
      wt8(ws, ob + ((size_t)r * HD + c) * 2, v2u{pack2(v[0], v[1]), pack2(v[2], v[3])});
    });
  } else {
    gemm_tile<BM, WM, WN, true>(xn, DM, Wv, HD, DM, brow, bcol, lds, [&](int r, int c, f32x4 v) {
      const int seg = r >> 5, t = r & 31, p = 8 * ((t & 15) >> 2) + 4 * (t >> 4);
      wt8(ws, WS_VT + (((size_t)seg * HD + c) * 32 + p) * 2, v2u{pack2(v[0], v[1]), pack2(v[2], v[3])});
    });
  }
}
template <int BM, int WM, int WN>
__device__ __forceinline__ void task_mlp1(int rt, int ct, const u16* w1, const u16* b1, unsigned char* ws, LAS char* lds) {
  const u16* y = (const u16*)(ws + WS_Y);
  gemm_tile<BM, WM, WN, false>(y, DM, w1, FF, DM, rt * BM, ct * 128, lds, [&](int r, int c, f32x4 v) {
    const v2u bb = *(const v2u*)(b1 + c);
    const float o0 = gelu_tanh(rbf(rbf(v[0]) + bflo(bb.x))), o1 = gelu_tanh(rbf(rbf(v[1]) + bfhi(bb.x)));
    const float o2 = gelu_tanh(rbf(rbf(v[2]) + bflo(bb.y))), o3 = gelu_tanh(rbf(rbf(v[3]) + bfhi(bb.y)));
    wt8(ws, WS_H + ((size_t)r * FF + c) * 2, v2u{pack2(o0, o1), pack2(o2, o3)});
  });
}
template <int BM, int WM, int WN>
__device__ __forceinline__ void task_mlp2(int rt, int ct, const u16* w2, const u16* b2, unsigned char* ws, LAS char* lds) {
  const u16* h = (const u16*)(ws + WS_H);
  gemm_tile<BM, WM, WN, false>(h, FF, w2, DM, FF, rt * BM, ct * 128, lds, [&](int r, int c, f32x4 v) {
    const v2u bb = *(const v2u*)(b2 + c);
    wt8(ws, WS_BO + ((size_t)r * DM + c) * 2, v2u{pack2(rbf(v[0]) + bflo(bb.x), rbf(v[1]) + bfhi(bb.x)), pack2(rbf(v[2]) + bflo(bb.y), rbf(v[3]) + bfhi(bb.y))});
  });
}
template <int BM, int WM, int WN>
__device__ __forceinline__ void task_wo(int rt, int ct, const u16* Wo, const u16* x, u16* out, unsigned char* ws, LAS char* lds) {
  const u16* ot = (const u16*)(ws + WS_OT);
  const u16* bo = (const u16*)(ws + WS_BO);
  gemm_tile<BM, WM, WN, false>(ot, HD, Wo, DM, HD, rt * BM, ct * 128, lds, [&](int r, int c, f32x4 v) {
    const v2u xx = *(const v2u*)(x + (size_t)r * DM + c);
    const v2u bb = *(const v2u*)(bo + (size_t)r * DM + c);
    const float o0 = rbf(bflo(xx.x) + rbf(v[0])) + bflo(bb.x), o1 = rbf(bfhi(xx.x) + rbf(v[1])) + bfhi(bb.x);
    const float o2 = rbf(bflo(xx.y) + rbf(v[2])) + bflo(bb.y), o3 = rbf(bfhi(xx.y) + rbf(v[3])) + bfhi(bb.y);
    *(v2u*)(out + (size_t)r * DM + c) = v2u{pack2(o0, o1), pack2(o2, o3)};
  });
}

__device__ __forceinline__ bool rdy(v4u v) { return max(max(v.x, v.y), max(v.z, v.w)) != SENT; }
__device__ __forceinline__ v4u ld_sc1(__amdgpu_buffer_rsrc_t rs, int off) { return __builtin_amdgcn_raw_buffer_load_b128(rs, off, 0, 16); }

template <int N>
__device__ __forceinline__ void poll(__amdgpu_buffer_rsrc_t rs, int off, v4u (&d)[N], unsigned* err) {
  unsigned spins = 0;
  for (;;) {
    unsigned mx = 0;
#pragma unroll
    for (int i = 0; i < N; ++i) d[i] = ld_sc1(rs, off + i * 1024);
    asm volatile("" ::: "memory");
#pragma unroll
    for (int i = 0; i < N; ++i) mx = max(max(mx, max(d[i].x, d[i].y)), max(d[i].z, d[i].w));
    if (__all(mx != SENT)) break;
    if (DLYR) __builtin_amdgcn_s_sleep(DLYR);
    if (++spins > SPINCAP) {
      if ((threadIdx.x & 63) == 0) __hip_atomic_store(err, 1u, __ATOMIC_RELAXED, __HIP_MEMORY_SCOPE_AGENT);
      break;
    }
    if ((spins & 1023u) == 0u && __hip_atomic_load(err, __ATOMIC_RELAXED, __HIP_MEMORY_SCOPE_AGENT) != 0u) break;
  }
}
__device__ __forceinline__ float xor16(float x) {
  auto r = __builtin_amdgcn_permlane16_swap(__builtin_bit_cast(unsigned, x), __builtin_bit_cast(unsigned, x), false, false);
  const int odd = (threadIdx.x >> 4) & 1;
  return __builtin_bit_cast(float, odd ? r[0] : r[1]);
}
__device__ __forceinline__ float xor32(float x) {
  auto r = __builtin_amdgcn_permlane32_swap(__builtin_bit_cast(unsigned, x), __builtin_bit_cast(unsigned, x), false, false);
  const int hi = (threadIdx.x >> 5) & 1;
  return __builtin_bit_cast(float, hi ? r[0] : r[1]);
}

template <bool ACQ = true>
__device__ __forceinline__ void wave_wait(unsigned* ctr, unsigned want, unsigned* err) {
  unsigned spins = 0;
  while (__hip_atomic_load(ctr, __ATOMIC_RELAXED, __HIP_MEMORY_SCOPE_AGENT) < want) {
    __builtin_amdgcn_s_sleep(4);
    if (++spins > SPINCAP) { __hip_atomic_store(err, 1u, __ATOMIC_RELAXED, __HIP_MEMORY_SCOPE_AGENT); break; }
    if ((spins & 1023u) == 0u && __hip_atomic_load(err, __ATOMIC_RELAXED, __HIP_MEMORY_SCOPE_AGENT) != 0u) break;
  }
  if (ACQ) {
    __builtin_amdgcn_fence(__ATOMIC_ACQUIRE, "agent");
    asm volatile("s_waitcnt vmcnt(0)" ::: "memory");
  } else {
    __builtin_amdgcn_fence(__ATOMIC_ACQUIRE, "wavefront");
  }
}
constexpr int CH_RED = 0;
constexpr int CH_VM = 24576;
constexpr int CH_RED2 = 25088;
constexpr int CH_WV = 27136;
constexpr int CH_KV = CH_WV + 65536;
constexpr int CH_LDS = CH_KV + 20480;

__device__ __forceinline__ void chain_role(int c, const u16* __restrict__ mem0, const u16* __restrict__ Wq, const u16* __restrict__ Wk,
                                           const u16* __restrict__ Wv, const u16* __restrict__ Wo, unsigned char* ws, LAS char* lds) {
  const int tid = threadIdx.x, lane = tid & 63, w = __builtin_amdgcn_readfirstlane(tid >> 6);
  const int fr = lane & 15, g = lane >> 4;
  const int h = c >> 2, q4 = c & 3, mh = c & 1, nb = c >> 1;
  unsigned* err = (unsigned*)(ws + WS_ERR);
  const __amdgpu_buffer_rsrc_t rs = __builtin_amdgcn_make_buffer_rsrc((void*)(ws + WS_HOP), 0, (int)HOP_BYTES, 0x00020000);
  v4u wq[4][4], wo[16];
  LAS v4u* wvl = (LAS v4u*)(lds + CH_WV) + w * 512 + lane;
  {
    LAS char* stg = lds + CH_WV + w * 8192;
    const int q = fr >> 2, p = lane & 3;
#pragma unroll
    for (int hf = 0; hf < 2; ++hf) {
#pragma unroll
      for (int i = 0; i < 8; ++i) {
        const int row = i * 32 + (lane >> 1), ch = lane & 1;
        v4u v = *(const v4u*)(Wo + (size_t)(512 * w + 256 * hf + row) * DM + 16 * nb + ch * 8);
        *(LAS v4u*)(stg + row * 32 + ch * 16) = v;
      }
      __builtin_amdgcn_s_waitcnt(0xC07F);
#pragma unroll
      for (int ks = 0; ks < 8; ++ks) {
        v2u lo = trread(stg + (32 * ks + 8 * g + q) * 32 + (4 * p) * 2);
        v2u hi = trread(stg + (32 * ks + 8 * g + 4 + q) * 32 + (4 * p) * 2);
        wo[hf * 8 + ks] = v4u{lo.x, lo.y, hi.x, hi.y};
      }
      __builtin_amdgcn_s_waitcnt(0xC07F);
    }
#pragma unroll
    for (int mat = 0; mat < 3; ++mat) {
      const u16* W = mat == 0 ? Wq : (mat == 1 ? Wk : Wv);
#pragma unroll
      for (int i = 0; i < 8; ++i) {
        const int row = i * 16 + (lane >> 2), ch = lane & 3;
        v4u v = *(const v4u*)(W + (size_t)(128 * w + row) * HD + h * 128 + 32 * q4 + ch * 8);
        *(LAS v4u*)(stg + row * 64 + ch * 16) = v;
      }
      __builtin_amdgcn_s_waitcnt(0xC07F);
      v4u tv[8];
#pragma unroll
      for (int dt = 0; dt < 2; ++dt)
#pragma unroll
        for (int ks = 0; ks < 4; ++ks) {
          v2u lo = trread(stg + (32 * ks + 8 * g + q) * 64 + (16 * dt + 4 * p) * 2);
          v2u hi = trread(stg + (32 * ks + 8 * g + 4 + q) * 64 + (16 * dt + 4 * p) * 2);
          if (mat < 2) wq[(mat & 1) * 2 + dt][ks] = v4u{lo.x, lo.y, hi.x, hi.y};
          else tv[dt * 4 + ks] = v4u{lo.x, lo.y, hi.x, hi.y};
        }
      __builtin_amdgcn_s_waitcnt(0xC07F);
      asm volatile("" ::: "memory");
      if (mat == 2) {
#pragma unroll
        for (int i = 0; i < 8; ++i) wvl[i * 64] = tv[i];
      }
    }
  }
  const float scale = 0.08838834764831845f;
  const float L2E = 1.4426950408889634f;
  LAS f32x4* red = (LAS f32x4*)(lds + CH_RED);
  LAS f32x4* red2 = (LAS f32x4*)(lds + CH_RED2);
  LAS u16* vml = (LAS u16*)(lds + CH_VM);
  LAS v4u* kvl = (LAS v4u*)(lds + CH_KV) + lane;
  const __amdgpu_buffer_rsrc_t rsw = __builtin_amdgcn_make_buffer_rsrc((void*)ws, 0, (int)WS_TOTAL, 0x00020000);
  auto kv_prefetch = [&](int sn) {
    if ((sn & 1) == 0) wave_wait<false>(err + CW_KV + 16 * (sn >> 1), 64u, err);
    v4u t[10];
#pragma unroll
    for (int tt = 0; tt < 2; ++tt)
#pragma unroll
      for (int j = 0; j < 4; ++j) t[tt * 4 + j] = ld_sc1(rsw, (int)(WS_K + ((size_t)(32 * sn + 16 * tt + fr) * HD + h * 128 + 32 * j + 8 * g) * 2));
#pragma unroll
    for (int dt = 0; dt < 2; ++dt) t[8 + dt] = ld_sc1(rsw, (int)(WS_VT + ((((size_t)sn * NH + h) * 128 + 32 * q4 + 16 * dt + fr) * 32 + 8 * g) * 2));
#pragma unroll
    for (int i = 0; i < 10; ++i) kvl[((sn & 1) * 10 + i) * 64] = t[i];
  };
  if (w == 2) kv_prefetch(0);

#define STAMP(i)
  for (int s = 0; s < NSEG; ++s) {
    v4u mf[2];
    if (s == 0) {
#pragma unroll
      for (int p = 0; p < 2; ++p)
        mf[p] = *(const v4u*)(mem0 + (size_t)(fr & 7) * DM + 128 * w + 32 * (2 * p + (fr >> 3)) + 8 * g);
    } else {
      if (DLY1) __builtin_amdgcn_s_sleep(DLY1);
      poll<2>(rs, (int)(HOP_MEM + (size_t)s * 16384) + ((16 * w + 4 * (fr >> 3) + g) * 8 + (fr & 7)) * 16, mf, err);
    }
    STAMP(0)
    {
      f32x4 a0[4], a1[4];
#pragma unroll
      for (int t = 0; t < 4; ++t) {
        a0[t] = mfma16(wq[t][0], mf[0], f32x4{0.f, 0.f, 0.f, 0.f});
        a1[t] = mfma16(wq[t][1], mf[0], f32x4{0.f, 0.f, 0.f, 0.f});
      }
#pragma unroll
      for (int t = 0; t < 4; ++t) {
        a0[t] = mfma16(wq[t][2], mf[1], a0[t]);
        a1[t] = mfma16(wq[t][3], mf[1], a1[t]);
      }
#pragma unroll
      for (int t = 0; t < 4; ++t) {
        f32x4 x = (fr & 8) ? a1[t] : a0[t];
#pragma unroll
        for (int r = 0; r < 4; ++r) x[r] += row_ror<8>(x[r]);
        if (fr < 8) red[(w * 6 + t) * 32 + g * 8 + fr] = x;
      }
    }
    __syncthreads();
    STAMP(1)
    if (tid >= 384) {
      const int u = tid - 384;
      const int t6 = u >> 5, gg = (u >> 3) & 3, m = u & 7;
      f32x4 x = red[t6 * 32 + (u & 31)];
#pragma unroll
      for (int ww = 1; ww < 8; ++ww) x += red[(ww * 6 + t6) * 32 + (u & 31)];
      const unsigned lo = pack2(x[0], x[1]), hi = pack2(x[2], x[3]);
      const int mat = t6 >> 1, dt = t6 & 1;
      const int off = (int)(HOP_QK + ((size_t)s * NH + h) * 4096) + ((q4 * 2 + mat) * 8 + m) * 64 + (16 * dt + 4 * gg) * 2;
      __builtin_amdgcn_raw_buffer_store_b64(v2u{nosent(lo), nosent(hi)}, rs, off, 0, 16);
    }
    {
      f32x4 a0[2], a1[2];
#pragma unroll
      for (int t = 0; t < 2; ++t) {
        a0[t] = mfma16(wvl[(t * 4 + 0) * 64], mf[0], f32x4{0.f, 0.f, 0.f, 0.f});
        a1[t] = mfma16(wvl[(t * 4 + 1) * 64], mf[0], f32x4{0.f, 0.f, 0.f, 0.f});
      }
#pragma unroll
      for (int t = 0; t < 2; ++t) {
        a0[t] = mfma16(wvl[(t * 4 + 2) * 64], mf[1], a0[t]);
        a1[t] = mfma16(wvl[(t * 4 + 3) * 64], mf[1], a1[t]);
      }
#pragma unroll
      for (int t = 0; t < 2; ++t) {
        f32x4 x = (fr & 8) ? a1[t] : a0[t];
#pragma unroll
        for (int r = 0; r < 4; ++r) x[r] += row_ror<8>(x[r]);
        if (fr < 8) red[(w * 6 + 4 + t) * 32 + g * 8 + fr] = x;
      }
    }
    __syncthreads();
    STAMP(2)
    if (w == 1) {
      const int dt = lane >> 5, gg = (lane >> 3) & 3, m = lane & 7;
      f32x4 x = red[(4 + dt) * 32 + (lane & 31)];
#pragma unroll
      for (int ww = 1; ww < 8; ++ww) x += red[(ww * 6 + 4 + dt) * 32 + (lane & 31)];
      const unsigned lo = pack2(x[0], x[1]), hi = pack2(x[2], x[3]);
      const int d = 16 * dt + 4 * gg;
      vml[(d + 0) * 8 + m] = (u16)(lo & 0xFFFF);
      vml[(d + 1) * 8 + m] = (u16)(lo >> 16);
      vml[(d + 2) * 8 + m] = (u16)(hi & 0xFFFF);
      vml[(d + 3) * 8 + m] = (u16)(hi >> 16);
      __builtin_amdgcn_s_waitcnt(0xC07F);
      asm volatile("" ::: "memory");
      if (lane < 32)
      {
        v4u t = *(LAS v4u*)(vml + lane * 8);
        t.x = nosent(t.x); t.y = nosent(t.y); t.z = nosent(t.z); t.w = nosent(t.w);
        __builtin_amdgcn_raw_buffer_store_b128(t, rs, (int)(HOP_VM + (((size_t)s * NH + h) * 128 + 32 * q4 + lane) * 16), 0, 16);
      }
    }
    if (s == NSEG - 1) break;
    if (w == 2 && s + 1 < NSEG - 1) kv_prefetch(s + 1);
    v4u pt, pm;
    if (w == 0) {
      v4u X[8];
      {
        const int base = (int)(HOP_QK + ((size_t)s * NH + h) * 4096) + (fr & 7) * 64 + g * 16;
        unsigned spins = 0;
        if (DLY2) __builtin_amdgcn_s_sleep(DLY2);
        for (;;) {
          unsigned mx = 0;
#pragma unroll
          for (int i = 0; i < 8; ++i) X[i] = ld_sc1(rs, base + i * 512);
          asm volatile("" ::: "memory");
#pragma unroll
          for (int i = 0; i < 8; ++i) mx = max(max(mx, max(X[i].x, X[i].y)), max(X[i].z, X[i].w));
          if (__all(mx != SENT)) break;
          if (++spins > SPINCAP) {
            if (lane == 0) __hip_atomic_store(err, 1u, __ATOMIC_RELAXED, __HIP_MEMORY_SCOPE_AGENT);
            break;
          }
          if ((spins & 1023u) == 0u && __hip_atomic_load(err, __ATOMIC_RELAXED, __HIP_MEMORY_SCOPE_AGENT) != 0u) break;
        }
      }
      STAMP(3)
      f32x4 s0 = {0.f, 0.f, 0.f, 0.f}, s1 = s0, s2 = s0;
#pragma unroll
      for (int j = 0; j < 4; ++j) {
        s1 = mfma16(kvl[((s & 1) * 10 + j) * 64], X[2 * j], s1);
        s2 = mfma16(kvl[((s & 1) * 10 + 4 + j) * 64], X[2 * j], s2);
        s0 = mfma16(X[2 * j + 1], X[2 * j], s0);
      }
      const bool up = fr >= 8;
      f32x4 st = up ? s2 : s1;
      const float m01 = up ? s0[1] : s0[0], m23 = up ? s0[3] : s0[2];
      const float sm = g >= 2 ? m23 : m01;
      unsigned p0 = pack2(st[0], st[1]), p1 = pack2(st[2], st[3]), p2 = pack2(sm, sm);
      p0 = pack2(bflo(p0) * scale, bfhi(p0) * scale);
      p1 = pack2(bflo(p1) * scale, bfhi(p1) * scale);
      p2 = pack2(bflo(p2) * scale, bflo(p2) * scale);
      float v0 = bflo(p0), v1 = bfhi(p0), v2 = bflo(p1), v3 = bfhi(p1), v4 = bflo(p2);
      float mx = fmaxf(fmaxf(fmaxf(v0, v1), fmaxf(v2, v3)), v4);
      mx = fmaxf(mx, row_ror<8>(mx));
      mx = fmaxf(mx, xor16(mx));
      mx = fmaxf(mx, xor32(mx));
      const float nm = -mx * L2E;
      v0 = __builtin_amdgcn_exp2f(__builtin_fmaf(v0, L2E, nm));
      v1 = __builtin_amdgcn_exp2f(__builtin_fmaf(v1, L2E, nm));
      v2 = __builtin_amdgcn_exp2f(__builtin_fmaf(v2, L2E, nm));
      v3 = __builtin_amdgcn_exp2f(__builtin_fmaf(v3, L2E, nm));
      v4 = __builtin_amdgcn_exp2f(__builtin_fmaf(v4, L2E, nm));
      float sum = ((v0 + v1) + (v2 + v3)) + v4;
      sum += row_ror<8>(sum);
      sum += xor16(sum);
      sum += xor32(sum);
      const float inv = __builtin_amdgcn_rcpf(sum);
      pt.x = pack2(v0 * inv, v1 * inv);
      pt.y = pack2(v2 * inv, v3 * inv);
      pt.z = (unsigned)__builtin_amdgcn_update_dpp(0, (int)pt.x, 0x128, 0xf, 0xf, false);
      pt.w = (unsigned)__builtin_amdgcn_update_dpp(0, (int)pt.y, 0x128, 0xf, 0xf, false);
      v4 *= inv;
      pm.x = pack2(v4, row_ror<8>(v4));
      pm.y = 0u; pm.z = 0u; pm.w = 0u;
    }
    __syncthreads();
    if (w == 0) {
      f32x4 o[2];
#pragma unroll
      for (int dt = 0; dt < 2; ++dt) o[dt] = mfma16(kvl[((s & 1) * 10 + 8 + dt) * 64], pt, f32x4{0.f, 0.f, 0.f, 0.f});
#pragma unroll
      for (int dt = 0; dt < 2; ++dt) {
        const unsigned t = *(LAS unsigned*)(vml + (16 * dt + fr) * 8 + 4 * (g & 1) + 2 * (g >> 1));
        o[dt] = mfma16(v4u{t, 0u, 0u, 0u}, pm, o[dt]);
      }
      if (fr < 8) {
#pragma unroll
        for (int dt = 0; dt < 2; ++dt) {
          const int k8 = h * 16 + 4 * q4 + 2 * dt + (g >> 1);
          const int off = (int)(HOP_OB + (size_t)s * 65536) + (((fr >> 2) * 512 + k8) * 4 + (fr & 3)) * 16 + (g & 1) * 8;
          __builtin_amdgcn_raw_buffer_store_b64(v2u{nosent(pack2(o[dt][0], o[dt][1])), nosent(pack2(o[dt][2], o[dt][3]))}, rs, off, 0, 16);
        }
      }
    }
    __syncthreads();
    STAMP(4)
    {
      v4u of[4];
      if (DLY3) __builtin_amdgcn_s_sleep(DLY3);
      poll<4>(rs, (int)(HOP_OB + (size_t)s * 65536) + ((mh * 512 + 64 * w + 4 * (fr >> 2) + g) * 4 + (fr & 3)) * 16, of, err);
      STAMP(5)
      f32x4 a[4];
#pragma unroll
      for (int r4 = 0; r4 < 4; ++r4) a[r4] = mfma16(wo[r4], of[0], f32x4{0.f, 0.f, 0.f, 0.f});
#pragma unroll
      for (int i = 1; i < 4; ++i)
#pragma unroll
        for (int r4 = 0; r4 < 4; ++r4) a[r4] = mfma16(wo[4 * i + r4], of[i], a[r4]);
      const int r4 = fr >> 2;
      f32x4 x = r4 == 0 ? a[0] : (r4 == 1 ? a[1] : (r4 == 2 ? a[2] : a[3]));
#pragma unroll
      for (int r = 0; r < 4; ++r) { x[r] += row_ror<8>(x[r]); x[r] += row_ror<4>(x[r]); }
      if (fr < 4) red2[w * 16 + g * 4 + fr] = x;
    }
    __syncthreads();
    if (tid < 16) {
      f32x4 x = red2[tid];
#pragma unroll
      for (int ww = 1; ww < 8; ++ww) x += red2[ww * 16 + tid];
      const int gg = tid >> 2, m4 = tid & 3;
      const int k8 = 2 * nb + (gg >> 1);
      const int off = (int)(HOP_MEM + (size_t)(s + 1) * 16384) + (k8 * 8 + 4 * mh + m4) * 16 + (gg & 1) * 8;
      __builtin_amdgcn_raw_buffer_store_b64(v2u{nosent(pack2(x[0], x[1])), nosent(pack2(x[2], x[3]))}, rs, off, 0, 16);
    }
    STAMP(6)
  }
}

__device__ __forceinline__ void attn_unit(int s, int h, int lane, unsigned char* ws) {
  const int fr = lane & 15, g = lane >> 4;
  const u16* Q = (const u16*)(ws + WS_Q);
  const u16* Kx = (const u16*)(ws + WS_K);
  const u16* Vt = (const u16*)(ws + WS_VT);
  const __amdgpu_buffer_rsrc_t rs = __builtin_amdgcn_make_buffer_rsrc((void*)(ws + WS_HOP), 0, (int)HOP_BYTES, 0x00020000);
  unsigned* err = (unsigned*)(ws + WS_ERR);
  const float scale = 0.08838834764831845f;
  f32x4 sc[3][2];
#pragma unroll
  for (int a = 0; a < 3; ++a)
#pragma unroll
    for (int b = 0; b < 2; ++b) sc[a][b] = f32x4{0.f, 0.f, 0.f, 0.f};
  v4u qf[4][2], vtf[8];
  {
    v4u kf[4][2];
#pragma unroll
    for (int j = 0; j < 4; ++j)
#pragma unroll
      for (int t = 0; t < 2; ++t) {
        qf[j][t] = *(const v4u*)(Q + (size_t)(32 * s + 16 * t + fr) * HD + h * 128 + 32 * j + 8 * g);
        kf[j][t] = *(const v4u*)(Kx + (size_t)(32 * s + 16 * t + fr) * HD + h * 128 + 32 * j + 8 * g);
      }
#pragma unroll
    for (int dt = 0; dt < 8; ++dt) vtf[dt] = *(const v4u*)(Vt + (((size_t)s * NH + h) * 128 + 16 * dt + fr) * 32 + 8 * g);
#pragma unroll
    for (int j = 0; j < 4; ++j)
#pragma unroll
      for (int a = 0; a < 2; ++a)
#pragma unroll
        for (int b = 0; b < 2; ++b) sc[1 + a][b] = mfma16(kf[j][a], qf[j][b], sc[1 + a][b]);
  }
  v4u km[4];
  poll<4>(rs, (int)(HOP_QK + ((size_t)s * NH + h) * 4096) + 512 + (fr & 7) * 64 + g * 16, km, err);
  v4u vmr[2];
  poll<2>(rs, (int)(HOP_VM + ((size_t)s * NH + h) * 2048) + lane * 16, vmr, err);
#pragma unroll
  for (int j = 0; j < 4; ++j)
#pragma unroll
    for (int b = 0; b < 2; ++b) sc[0][b] = mfma16(km[j], qf[j][b], sc[0][b]);
  v4u pt[2], pm[2];
#pragma unroll
  for (int b = 0; b < 2; ++b) {
    const int qi = 16 * b + fr;
    float v[12];
    bool ok[12];
#pragma unroll
    for (int r = 0; r < 4; ++r) {
      v[r] = rbf(rbf(sc[0][b][r]) * scale); ok[r] = g < 2;
      v[4 + r] = rbf(rbf(sc[1][b][r]) * scale); ok[4 + r] = (4 * g + r) <= qi;
      v[8 + r] = rbf(rbf(sc[2][b][r]) * scale); ok[8 + r] = (16 + 4 * g + r) <= qi;
    }
    float mx = -3.0e38f;
#pragma unroll
    for (int i = 0; i < 12; ++i) mx = ok[i] ? fmaxf(mx, v[i]) : mx;
    mx = fmaxf(mx, __shfl_xor(mx, 16));
    mx = fmaxf(mx, __shfl_xor(mx, 32));
    float sum = 0.f;
#pragma unroll
    for (int i = 0; i < 12; ++i) { v[i] = ok[i] ? __expf(v[i] - mx) : 0.f; sum += v[i]; }
    sum += __shfl_xor(sum, 16);
    sum += __shfl_xor(sum, 32);
    const float inv = 1.0f / sum;
    pt[b].x = pack2(v[4] * inv, v[5] * inv); pt[b].y = pack2(v[6] * inv, v[7] * inv);
    pt[b].z = pack2(v[8] * inv, v[9] * inv); pt[b].w = pack2(v[10] * inv, v[11] * inv);
    pm[b].x = pack2(v[0] * inv, v[1] * inv); pm[b].y = pack2(v[2] * inv, v[3] * inv); pm[b].z = 0u; pm[b].w = 0u;
  }
#pragma unroll
  for (int dt = 0; dt < 8; ++dt) {
    v4u vmf;
    {
      const int src = (16 * dt + fr) & 63;
      const v4u vv = vmr[dt >> 2];
      const unsigned a0 = __shfl(vv.x, src), a1 = __shfl(vv.y, src), a2 = __shfl(vv.z, src), a3 = __shfl(vv.w, src);
      vmf.x = g == 0 ? a0 : (g == 1 ? a2 : 0u);
      vmf.y = g == 0 ? a1 : (g == 1 ? a3 : 0u);
      vmf.z = 0u; vmf.w = 0u;
    }
#pragma unroll
    for (int b = 0; b < 2; ++b) {
      f32x4 o = {0.f, 0.f, 0.f, 0.f};
      o = mfma16(vtf[dt], pt[b], o);
      o = mfma16(vmf, pm[b], o);
      wt8(ws, WS_OT + ((size_t)(32 * s + 16 * b + fr) * HD + h * 128 + 16 * dt + 4 * g) * 2, v2u{pack2(o[0], o[1]), pack2(o[2], o[3])});
    }
  }
}
__device__ __forceinline__ void wg_signal(unsigned* ctr, int n) {
  asm volatile("s_waitcnt vmcnt(0)" ::: "memory");
  __syncthreads();
  if (threadIdx.x < n) __hip_atomic_fetch_add(ctr + 16 * threadIdx.x, 1u, __ATOMIC_RELAXED, __HIP_MEMORY_SCOPE_AGENT);
}
__device__ __forceinline__ void wg_wait(unsigned* ctr, unsigned want, unsigned* err) {
  if (threadIdx.x < 64) wave_wait(ctr, want, err);
  __syncthreads();
}

__device__ __forceinline__ void wait_phase_abc(unsigned* cw) {
  unsigned* err = cw + CW_ERR;
  if (threadIdx.x < 64) {
    const int l = threadIdx.x;
    unsigned* p = l < 32 ? cw + CW_KV + 16 * l : (l == 32 ? cw + CW_Q : cw + CW_MLP2);
    const unsigned want = l < 32 ? 64u : (l < 34 ? 128u : 0u);
    unsigned spins = 0;
    while (!__all(__hip_atomic_load(p, __ATOMIC_RELAXED, __HIP_MEMORY_SCOPE_AGENT) >= want)) {
      __builtin_amdgcn_s_sleep(8);
      if (++spins > SPINCAP) { __hip_atomic_store(err, 1u, __ATOMIC_RELAXED, __HIP_MEMORY_SCOPE_AGENT); break; }
      if ((spins & 1023u) == 0u && __hip_atomic_load(err, __ATOMIC_RELAXED, __HIP_MEMORY_SCOPE_AGENT) != 0u) break;
    }
    __builtin_amdgcn_fence(__ATOMIC_ACQUIRE, "agent");
    asm volatile("s_waitcnt vmcnt(0)" ::: "memory");
  }
  __syncthreads();
}
#define do_attn(sg_, hg_, ci_) { attn_unit((sg_), (hg_) * 8 + (threadIdx.x >> 6), threadIdx.x & 63, ws); wg_signal(cw + CW_AT + 16 * (ci_), 1); }

template <int NS>
__device__ __forceinline__ void tail_unit(int u, int idx, const u16* x, const u16* Wo, u16* out, unsigned char* ws, LAS char* lds) {
  unsigned* cw = (unsigned*)(ws + WS_ERR);
  unsigned* err = cw + CW_ERR;
  constexpr int KS = HD / NS, RR = 64 / NS;
  const int ct = idx / NS, ks = idx % NS;
  const __amdgpu_buffer_rsrc_t rsw = __builtin_amdgcn_make_buffer_rsrc((void*)ws, 0, (int)WS_TOTAL, 0x00020000);
  if (ks == 0) do_attn(2 * u + (ct >> 2), ct & 3, u)
  wg_wait(cw + CW_AT + 16 * u, 8u, err);
  const int slot = (u - TAIL_U0) * 8 + ct;
  const size_t sbase = WS_SLAB + (size_t)slot * 16 * 32768;
  gemm_tile<64, 2, 4, false>((const u16*)(ws + WS_OT) + ks * KS, HD, Wo + (size_t)ks * KS * DM, DM, KS, u * 64, ct * 128, lds,
                             [&](int r, int c, f32x4 v) {
                               __builtin_amdgcn_raw_buffer_store_b128(__builtin_bit_cast(v4u, v), rsw,
                                   (int)(sbase + (size_t)ks * 32768 + (((r & 63) * 128 + (c & 127)) * 4)), 0, 16);
                             });
  wg_signal(cw + CW_SK + 16 * slot, 1);
  if (threadIdx.x < 64) wave_wait<false>(cw + CW_SK + 16 * slot, (unsigned)NS, err);
  __syncthreads();
  if (threadIdx.x < RR * 32) {
    const int rr = RR * ks + (threadIdx.x >> 5), cc = (threadIdx.x & 31) * 4;
    v4u pv[NS];
#pragma unroll
    for (int k2 = 0; k2 < NS; ++k2) pv[k2] = ld_sc1(rsw, (int)(sbase + (size_t)k2 * 32768 + (rr * 128 + cc) * 4));
    f32x4 v = __builtin_bit_cast(f32x4, pv[0]);
#pragma unroll
    for (int k2 = 1; k2 < NS; ++k2) v += __builtin_bit_cast(f32x4, pv[k2]);
    const int r = u * 64 + rr, c = ct * 128 + cc;
    const v2u xx = *(const v2u*)(x + (size_t)r * DM + c);
    const v2u bb = *(const v2u*)((const u16*)(ws + WS_BO) + (size_t)r * DM + c);
    const float o0 = rbf(bflo(xx.x) + rbf(v[0])) + bflo(bb.x), o1 = rbf(bfhi(xx.x) + rbf(v[1])) + bfhi(bb.x);
    const float o2 = rbf(bflo(xx.y) + rbf(v[2])) + bflo(bb.y), o3 = rbf(bfhi(xx.y) + rbf(v[3])) + bfhi(bb.y);
    *(v2u*)(out + (size_t)r * DM + c) = v2u{pack2(o0, o1), pack2(o2, o3)};
  }
}

__global__ __launch_bounds__(512, 2) void k_mega(const u16* x, const u16* mem0, const u16* Wq, const u16* Wk, const u16* Wv, const u16* Wo,
                                                 const u16* w1, const u16* b1, const u16* w2, const u16* b2, u16* out, unsigned char* ws) {
  extern __shared__ __attribute__((aligned(16))) char smem[];
  LAS char* lds = (LAS char*)smem;
  const int b = blockIdx.x;
#define ASTAMP(i)
  ASTAMP(0)
  const int xcd = b & 7, i = b >> 4;
  unsigned* cw = (unsigned*)(ws + WS_ERR);
  unsigned* err = cw + CW_ERR;
  if (((b >> 3) & 1) == 0) {
    const int c = ((xcd * 4 + (i >> 2)) << 2) | (i & 3);
    chain_role(c, mem0, Wq, Wk, Wv, Wo, ws, lds);
    ASTAMP(6)
    wait_phase_abc(cw);
    tail_unit<16>(31, xcd * 16 + i, x, Wo, out, ws, lds);
    ASTAMP(7)
    return;
  }
  const int gi = i * 8 + xcd;
  task_qkv<64, 2, 4>(gi >> 6, 32 + (gi & 63), Wq, Wk, Wv, ws, lds);
  wg_signal(cw + CW_KV + 16 * (gi >> 6), 1);
  for (int t = gi; t < 64 + 7 * 64; t += 128) {
    if (t < 64) {
      task_qkv<128, 2, 4>(1, 32 + t, Wq, Wk, Wv, ws, lds);
      wg_signal(cw + CW_KV + 16 * 2, 2);
    } else {
      const int rt = 1 + ((t - 64) >> 6);
      task_qkv<256, 4, 2>(rt, 32 + ((t - 64) & 63), Wq, Wk, Wv, ws, lds);
      wg_signal(cw + CW_KV + 16 * 4 * rt, 4);
    }
  }
  ASTAMP(1)
  for (int t = gi; t < 8 * 28; t += 128) task_mlp1<256, 4, 2>(t & 7, t >> 3, w1, b1, ws, lds);
  wg_signal(cw + CW_MLP1, 1);
  for (int t = gi; t < 8 * 32; t += 128) task_qkv<256, 4, 2>(t & 7, t >> 3, Wq, Wk, Wv, ws, lds);
  wg_signal(cw + CW_Q, 1);
  ASTAMP(2)
  wg_wait(cw + CW_MLP1, 128u, err);
  task_mlp2<128, 2, 4>(gi >> 3, gi & 7, w2, b2, ws, lds);
  wg_signal(cw + CW_MLP2, 1);
  ASTAMP(3)
  wait_phase_abc(cw);
  for (int t = gi; t < 160; t += 128) do_attn(t >> 2, t & 3, (t >> 4) * 2)
  if (gi < 80) {
    const int pr = gi >> 3;
    wg_wait(cw + CW_AT + 16 * 2 * pr, 16u, err);
    task_wo<128, 2, 4>(pr, gi & 7, Wo, x, out, ws, lds);
  }
  for (int u = TAIL_U0 + (gi >> 6); u < 31; u += 2) tail_unit<8>(u, (gi & 7) * 8 + ((gi >> 3) & 7), x, Wo, out, ws, lds);
  ASTAMP(7)
}

extern "C" void kernel_launch(void* const* d_in, const int* in_sizes, int n_in, void* d_out, int out_size, void* d_ws, size_t ws_size,
                              hipStream_t stream) {
  const u16* x = (const u16*)d_in[0];
  const u16* mem0 = (const u16*)d_in[1];
  const u16* Wq = (const u16*)d_in[2];
  const u16* Wk = (const u16*)d_in[3];
  const u16* Wv = (const u16*)d_in[4];
  const u16* Wo = (const u16*)d_in[5];
  const u16* w1 = (const u16*)d_in[8];
  const u16* b1 = (const u16*)d_in[9];
  const u16* w2 = (const u16*)d_in[10];
  const u16* b2 = (const u16*)d_in[11];
  unsigned char* ws = (unsigned char*)d_ws;
  u16* out = (u16*)d_out;
  constexpr int L256 = GemmCfg<256>::LDS_BYTES;
  constexpr int LMEGA = L256 > CH_LDS ? L256 : CH_LDS;
  (void)hipFuncSetAttribute((const void*)k_mega, hipFuncAttributeMaxDynamicSharedMemorySize, LMEGA);
  k_ln<<<S_LEN / 4, 256, 0, stream>>>(x, ws);
  k_mega<<<256, 512, LMEGA, stream>>>(x, mem0, Wq, Wk, Wv, Wo, w1, b1, w2, b2, out, ws);
}
```

```cpp
#include <hip/hip_runtime.h>
#include <hip/hip_bf16.h>
#include <stdint.h>

constexpr int S_LEN = 2048, DM = 1024, NH = 32, DH = 128, HD = NH * DH;
constexpr int MEMN = 8, SEG = 32, NSEG = S_LEN / SEG;
constexpr int FF = 3584;

typedef unsigned v4u __attribute__((ext_vector_type(4)));
typedef unsigned v2u __attribute__((ext_vector_type(2)));
typedef float f32x4 __attribute__((ext_vector_type(4)));
typedef float f32x2 __attribute__((ext_vector_type(2)));
typedef short bf16x8 __attribute__((ext_vector_type(8)));
typedef short s16x4 __attribute__((ext_vector_type(4)));
typedef __bf16 bfx2 __attribute__((ext_vector_type(2)));
typedef unsigned short u16;
#define LAS __attribute__((address_space(3)))
#define GAS __attribute__((address_space(1)))

constexpr size_t WS_XN = 0;
constexpr size_t WS_Y = WS_XN + (size_t)S_LEN * DM * 2;
constexpr size_t WS_Q = WS_Y + (size_t)S_LEN * DM * 2;
constexpr size_t WS_K = WS_Q + (size_t)S_LEN * HD * 2;
constexpr size_t WS_VT = WS_K + (size_t)S_LEN * HD * 2;
constexpr size_t WS_H = WS_VT + (size_t)S_LEN * HD * 2;
constexpr size_t WS_BO = WS_H + (size_t)S_LEN * FF * 2;
constexpr size_t WS_OT = WS_BO + (size_t)S_LEN * DM * 2;
constexpr size_t WS_SLAB = WS_OT + (size_t)S_LEN * HD * 2;
constexpr int TAIL_U0 = 24;
constexpr size_t WS_HOP = WS_SLAB + (size_t)64 * 16 * 64 * 128 * 4;
constexpr size_t HOP_MEM = 0;
constexpr size_t HOP_QK = HOP_MEM + (size_t)NSEG * 16384;
constexpr size_t HOP_OB = HOP_QK + (size_t)NSEG * NH * 4096;
constexpr size_t HOP_VM = HOP_OB + (size_t)NSEG * 65536;
constexpr size_t HOP_BYTES = HOP_VM + (size_t)NSEG * HD * MEMN * 2;
constexpr size_t WS_ERR = WS_HOP + HOP_BYTES;
constexpr size_t WS_TOTAL = WS_ERR + 16384;
constexpr int CW_ERR = 0, CW_MLP1 = 16, CW_Q = 32, CW_MLP2 = 48, CW_KV = 64  , CW_AT = 64 + 512  , CW_SK = 64 + 1024  ;

#define DLY1 14
#define DLY2 0
#define DLY3 14
#define DLYR 0
constexpr unsigned SENT = 0xFFFFFFFFu;
constexpr unsigned SPINCAP = 1u << 20;

__device__ __forceinline__ float bf2f(unsigned b16) { return __builtin_bit_cast(float, b16 << 16); }
__device__ __forceinline__ float bflo(unsigned x) { return __builtin_bit_cast(float, x << 16); }
__device__ __forceinline__ float bfhi(unsigned x) { return __builtin_bit_cast(float, x & 0xFFFF0000u); }
__device__ __forceinline__ unsigned pack2(float a, float b) {
  f32x2 v = {a, b};
  bfx2 r = __builtin_convertvector(v, bfx2);
  return __builtin_bit_cast(unsigned, r);
}
__device__ __forceinline__ float rbf(float a) { return (float)(__bf16)a; }
__device__ __forceinline__ unsigned nosent(unsigned x) { return x == SENT ? 0xFFFFFFFEu : x; }
__device__ __forceinline__ f32x4 mfma16(v4u a, v4u b, f32x4 c) {
  return __builtin_amdgcn_mfma_f32_16x16x32_bf16(__builtin_bit_cast(bf16x8, a), __builtin_bit_cast(bf16x8, b), c, 0, 0, 0);
}
__device__ __forceinline__ v2u trread(LAS const char* p) {
  typedef short v4i16_t __attribute__((ext_vector_type(4)));
  return __builtin_bit_cast(v2u, __builtin_amdgcn_ds_read_tr16_b64_v4i16((LAS v4i16_t*)p));
}
__device__ __forceinline__ float ror(float x, int) = delete;
template <int N> __device__ __forceinline__ float row_ror(float x) {
  return __builtin_bit_cast(float, __builtin_amdgcn_update_dpp(0, __builtin_bit_cast(int, x), 0x120 + N, 0xf, 0xf, false));
}

__device__ __forceinline__ float wave_sum(float v) {
#pragma unroll
  for (int o = 32; o > 0; o >>= 1) v += __shfl_xor(v, o);
  return v;
}
__device__ __forceinline__ void ln16(float (&v)[16]) {
  float s = 0.f;
#pragma unroll
  for (int i = 0; i < 16; ++i) s += v[i];
  s = wave_sum(s);
  const float mean = s * (1.0f / DM);
  float q = 0.f;
#pragma unroll
  for (int i = 0; i < 16; ++i) { float d = v[i] - mean; q += d * d; }
  q = wave_sum(q);
  const float mb = rbf(mean);
  const float vb = rbf(q * (1.0f / DM));
  const float den = rbf(sqrtf(rbf(vb + 1e-5f)));
#pragma unroll
  for (int i = 0; i < 16; ++i) v[i] = rbf(rbf(v[i] - mb) / den);
}
__global__ __launch_bounds__(256) void k_ln(const u16* __restrict__ x, unsigned char* __restrict__ ws) {
  const int tid = threadIdx.x, lane = tid & 63, w = tid >> 6;
  const int row = blockIdx.x * 4 + w;
  {
    v4u s = {SENT, SENT, SENT, SENT};
    v4u* p = (v4u*)(ws + WS_HOP);
    const size_t n = HOP_BYTES / 16;
    for (size_t i = (size_t)blockIdx.x * 256 + tid; i < n; i += (size_t)gridDim.x * 256) p[i] = s;
    if (blockIdx.x < 16) ((unsigned*)(ws + WS_ERR))[blockIdx.x * 256 + tid] = 0u;
  }
  const v4u* xr = (const v4u*)(x + (size_t)row * DM);
  v4u a = xr[lane], b = xr[64 + lane];
  float v[16];
  v[0] = bflo(a.x); v[1] = bfhi(a.x); v[2] = bflo(a.y); v[3] = bfhi(a.y);
  v[4] = bflo(a.z); v[5] = bfhi(a.z); v[6] = bflo(a.w); v[7] = bfhi(a.w);
  v[8] = bflo(b.x); v[9] = bfhi(b.x); v[10] = bflo(b.y); v[11] = bfhi(b.y);
  v[12] = bflo(b.z); v[13] = bfhi(b.z); v[14] = bflo(b.w); v[15] = bfhi(b.w);
  ln16(v);
  v4u* xn = (v4u*)(ws + WS_XN + (size_t)row * DM * 2);
  xn[lane] = v4u{pack2(v[0], v[1]), pack2(v[2], v[3]), pack2(v[4], v[5]), pack2(v[6], v[7])};
  xn[64 + lane] = v4u{pack2(v[8], v[9]), pack2(v[10], v[11]), pack2(v[12], v[13]), pack2(v[14], v[15])};
  ln16(v);
  v4u* y = (v4u*)(ws + WS_Y + (size_t)row * DM * 2);
  y[lane] = v4u{pack2(v[0], v[1]), pack2(v[2], v[3]), pack2(v[4], v[5]), pack2(v[6], v[7])};
  y[64 + lane] = v4u{pack2(v[8], v[9]), pack2(v[10], v[11]), pack2(v[12], v[13]), pack2(v[14], v[15])};
}

template <int BM> struct GemmCfg {
  static constexpr int BN = 128, BK = 64;
  static constexpr int A_BYTES = BM * BK * 2, B_BYTES = BK * BN * 2, ST_BYTES = A_BYTES + B_BYTES;
  static constexpr int NST = BM == 256 ? 3 : (BM == 128 ? 4 : 6);
  static constexpr int LDS_BYTES = NST * ST_BYTES;
};

template <int N> __device__ __forceinline__ void wait_vm() { asm volatile("s_waitcnt vmcnt(%0)" ::"n"(N) : "memory"); }

template <int BM, int WM, int WN, bool TOUT, class Epi>
__device__ __forceinline__ void gemm_tile(const u16* __restrict__ A, int lda, const u16* __restrict__ B, int ldb, int K,
                                          int brow, int bcol, LAS char* lds, Epi epi) {
  using C = GemmCfg<BM>;
  constexpr int TM = BM / WM / 16, TN = C::BN / WN / 16;
  constexpr int AI = C::A_BYTES / 8192, BI = C::B_BYTES / 8192;
  const int tid = threadIdx.x, lane = tid & 63, w = __builtin_amdgcn_readfirstlane(tid >> 6);
  const int wr = w / WN, wc = w % WN, fr = lane & 15, fq = lane >> 4;
  const u16* asrc[AI];
  const u16* bsrc[BI];
#pragma unroll
  for (int i = 0; i < AI; ++i) {
    const int ii = w * AI + i, row = ii * 8 + (lane >> 3), ch = (lane & 7) ^ (row & 7);
    asrc[i] = A + (size_t)(brow + row) * lda + ch * 8;
  }
#pragma unroll
  for (int i = 0; i < BI; ++i) {
    const int ii = w * BI + i, row = ii * 4 + (lane >> 4), ch = (lane & 15) ^ (((row & 3) << 2) | ((row >> 2) & 3));
    bsrc[i] = B + (size_t)row * ldb + bcol + ch * 8;
  }
  auto stage = [&](int t, int buf) {
    LAS char* base = lds + buf * C::ST_BYTES;
#pragma unroll
    for (int i = 0; i < AI; ++i)
      __builtin_amdgcn_global_load_lds((const GAS void*)(asrc[i] + t * 64), (LAS void*)(base + (w * AI + i) * 1024), 16, 0, 0);
#pragma unroll
    for (int i = 0; i < BI; ++i)
      __builtin_amdgcn_global_load_lds((const GAS void*)(bsrc[i] + (size_t)t * 64 * ldb), (LAS void*)(base + C::A_BYTES + (w * BI + i) * 1024), 16, 0, 0);
  };
  int aoff[TM][2], boff[TN][2][2];
#pragma unroll
  for (int m = 0; m < TM; ++m)
#pragma unroll
    for (int k = 0; k < 2; ++k) {
      const int row = wr * (TM * 16) + m * 16 + fr, ch = k * 4 + fq;
      aoff[m][k] = row * 128 + ((ch ^ (row & 7)) * 16);
    }
  {
    const int q = fr >> 2, p = lane & 3;
#pragma unroll
    for (int n = 0; n < TN; ++n)
#pragma unroll
      for (int k = 0; k < 2; ++k)
#pragma unroll
        for (int t = 0; t < 2; ++t) {
          const int row = 32 * k + 8 * fq + 4 * t + q, c = wc * TN + n, ch = 2 * c + (p >> 1);
          boff[n][k][t] = C::A_BYTES + 256 * row + 16 * (ch ^ (((row & 3) << 2) | ((row >> 2) & 3))) + 8 * (p & 1);
        }
  }
  f32x4 acc[TM][TN];
#pragma unroll
  for (int m = 0; m < TM; ++m)
#pragma unroll
    for (int n = 0; n < TN; ++n) acc[m][n] = f32x4{0.f, 0.f, 0.f, 0.f};
  const int nt = K / 64;
  constexpr int NST = C::NST, PER = AI + BI;
#pragma unroll
  for (int i = 0; i < NST - 1; ++i)
    if (i < nt) stage(i, i);
  int rb = 0, sb = NST - 1;
  for (int t = 0; t < nt; ++t) {
    {
      const int rem = nt - 1 - t;
      if (rem >= NST - 2) wait_vm<(NST - 2) * PER>();
      else if (NST > 5 && rem == 3) wait_vm<3 * PER>();
      else if (NST > 4 && rem == 2) wait_vm<2 * PER>();
      else if (NST > 3 && rem == 1) wait_vm<PER>();
      else wait_vm<0>();
    }
    asm volatile("s_waitcnt lgkmcnt(0)" ::: "memory");
    __builtin_amdgcn_s_barrier();
    asm volatile("" ::: "memory");
    if (t + NST - 1 < nt) stage(t + NST - 1, sb);
    LAS const char* base = lds + rb * C::ST_BYTES;
    v4u af[TM][2], bfr[TN][2];
#pragma unroll
    for (int k = 0; k < 2; ++k) {
#pragma unroll
      for (int n = 0; n < TN; ++n) {
        v2u lo = trread(base + boff[n][k][0]), hi = trread(base + boff[n][k][1]);
        bfr[n][k] = v4u{lo.x, lo.y, hi.x, hi.y};
      }
#pragma unroll
      for (int m = 0; m < TM; ++m) af[m][k] = *(LAS const v4u*)(base + aoff[m][k]);
    }
    __builtin_amdgcn_s_setprio(1);
#pragma unroll
    for (int k = 0; k < 2; ++k)
#pragma unroll
      for (int m = 0; m < TM; ++m)
#pragma unroll
        for (int n = 0; n < TN; ++n)
          acc[m][n] = TOUT ? mfma16(af[m][k], bfr[n][k], acc[m][n]) : mfma16(bfr[n][k], af[m][k], acc[m][n]);
    __builtin_amdgcn_s_setprio(0);
    rb = rb == NST - 1 ? 0 : rb + 1;
    sb = sb == NST - 1 ? 0 : sb + 1;
  }
#pragma unroll
  for (int m = 0; m < TM; ++m)
#pragma unroll
    for (int n = 0; n < TN; ++n) {
      const int r0 = brow + wr * (TM * 16) + m * 16, c0 = bcol + wc * (TN * 16) + n * 16;
      if (TOUT) epi(r0 + fq * 4, c0 + fr, acc[m][n]);
      else epi(r0 + fr, c0 + fq * 4, acc[m][n]);
    }
  asm volatile("s_waitcnt lgkmcnt(0)" ::: "memory");
  __builtin_amdgcn_s_barrier();
}

__device__ __forceinline__ void wt8(unsigned char* ws, size_t off, v2u v) {
  const __amdgpu_buffer_rsrc_t rs = __builtin_amdgcn_make_buffer_rsrc((void*)ws, 0, (int)WS_TOTAL, 0x00020000);
  __builtin_amdgcn_raw_buffer_store_b64(v, rs, (int)off, 0, 16);
}
__device__ __forceinline__ float gelu_tanh(float x) {
  const float u = 0.7978845608028654f * (x + 0.044715f * x * x * x);
  const float e = __expf(2.0f * u);
  const float t = 1.0f - 2.0f / (e + 1.0f);
  return 0.5f * x * (1.0f + t);
}

template <int BM, int WM, int WN>
__device__ __forceinline__ void task_qkv(int rt, int ct, const u16* Wq, const u16* Wk, const u16* Wv, unsigned char* ws, LAS char* lds) {
  const u16* xn = (const u16*)(ws + WS_XN);
  const int which = ct >> 5, bcol = (ct & 31) * 128, brow = rt * BM;
  if (which < 2) {
    const size_t ob = which == 0 ? WS_Q : WS_K;
    gemm_tile<BM, WM, WN, false>(xn, DM, which == 0 ? Wq : Wk, HD, DM, brow, bcol, lds, [&](int r, int c, f32x4 v) {
      wt8(ws, ob + ((size_t)r * HD + c) * 2, v2u{pack2(v[0], v[1]), pack2(v[2], v[3])});
    });
  } else {
    gemm_tile<BM, WM, WN, true>(xn, DM, Wv, HD, DM, brow, bcol, lds, [&](int r, int c, f32x4 v) {
      const int seg = r >> 5, t = r & 31, p = 8 * ((t & 15) >> 2) + 4 * (t >> 4);
      wt8(ws, WS_VT + (((size_t)seg * HD + c) * 32 + p) * 2, v2u{pack2(v[0], v[1]), pack2(v[2], v[3])});
    });
  }
}
template <int BM, int WM, int WN>
__device__ __forceinline__ void task_mlp1(int rt, int ct, const u16* w1, const u16* b1, unsigned char* ws, LAS char* lds) {
  const u16* y = (const u16*)(ws + WS_Y);
  gemm_tile<BM, WM, WN, false>(y, DM, w1, FF, DM, rt * BM, ct * 128, lds, [&](int r, int c, f32x4 v) {
    const v2u bb = *(const v2u*)(b1 + c);
    const float o0 = gelu_tanh(rbf(rbf(v[0]) + bflo(bb.x))), o1 = gelu_tanh(rbf(rbf(v[1]) + bfhi(bb.x)));
    const float o2 = gelu_tanh(rbf(rbf(v[2]) + bflo(bb.y))), o3 = gelu_tanh(rbf(rbf(v[3]) + bfhi(bb.y)));
    wt8(ws, WS_H + ((size_t)r * FF + c) * 2, v2u{pack2(o0, o1), pack2(o2, o3)});
  });
}
template <int BM, int WM, int WN>
__device__ __forceinline__ void task_mlp2(int rt, int ct, const u16* w2, const u16* b2, unsigned char* ws, LAS char* lds) {
  const u16* h = (const u16*)(ws + WS_H);
  gemm_tile<BM, WM, WN, false>(h, FF, w2, DM, FF, rt * BM, ct * 128, lds, [&](int r, int c, f32x4 v) {
    const v2u bb = *(const v2u*)(b2 + c);
    wt8(ws, WS_BO + ((size_t)r * DM + c) * 2, v2u{pack2(rbf(v[0]) + bflo(bb.x), rbf(v[1]) + bfhi(bb.x)), pack2(rbf(v[2]) + bflo(bb.y), rbf(v[3]) + bfhi(bb.y))});
  });
}
template <int BM, int WM, int WN>
__device__ __forceinline__ void task_wo(int rt, int ct, const u16* Wo, const u16* x, u16* out, unsigned char* ws, LAS char* lds) {
  const u16* ot = (const u16*)(ws + WS_OT);
  const u16* bo = (const u16*)(ws + WS_BO);
  gemm_tile<BM, WM, WN, false>(ot, HD, Wo, DM, HD, rt * BM, ct * 128, lds, [&](int r, int c, f32x4 v) {
    const v2u xx = *(const v2u*)(x + (size_t)r * DM + c);
    const v2u bb = *(const v2u*)(bo + (size_t)r * DM + c);
    const float o0 = rbf(bflo(xx.x) + rbf(v[0])) + bflo(bb.x), o1 = rbf(bfhi(xx.x) + rbf(v[1])) + bfhi(bb.x);
    const float o2 = rbf(bflo(xx.y) + rbf(v[2])) + bflo(bb.y), o3 = rbf(bfhi(xx.y) + rbf(v[3])) + bfhi(bb.y);
    *(v2u*)(out + (size_t)r * DM + c) = v2u{pack2(o0, o1), pack2(o2, o3)};
  });
}

__device__ __forceinline__ bool rdy(v4u v) { return max(max(v.x, v.y), max(v.z, v.w)) != SENT; }
__device__ __forceinline__ v4u ld_sc1(__amdgpu_buffer_rsrc_t rs, int off) { return __builtin_amdgcn_raw_buffer_load_b128(rs, off, 0, 16); }

template <int N>
__device__ __forceinline__ void poll(__amdgpu_buffer_rsrc_t rs, int off, v4u (&d)[N], unsigned* err) {
  unsigned spins = 0;
  for (;;) {
    unsigned mx = 0;
#pragma unroll
    for (int i = 0; i < N; ++i) d[i] = ld_sc1(rs, off + i * 1024);
    asm volatile("" ::: "memory");
#pragma unroll
    for (int i = 0; i < N; ++i) mx = max(max(mx, max(d[i].x, d[i].y)), max(d[i].z, d[i].w));
    if (__all(mx != SENT)) break;
    if (DLYR) __builtin_amdgcn_s_sleep(DLYR);
    if (++spins > SPINCAP) {
      if ((threadIdx.x & 63) == 0) __hip_atomic_store(err, 1u, __ATOMIC_RELAXED, __HIP_MEMORY_SCOPE_AGENT);
      break;
    }
    if ((spins & 1023u) == 0u && __hip_atomic_load(err, __ATOMIC_RELAXED, __HIP_MEMORY_SCOPE_AGENT) != 0u) break;
  }
}
__device__ __forceinline__ float xor16(float x) {
  auto r = __builtin_amdgcn_permlane16_swap(__builtin_bit_cast(unsigned, x), __builtin_bit_cast(unsigned, x), false, false);
  const int odd = (threadIdx.x >> 4) & 1;
  return __builtin_bit_cast(float, odd ? r[0] : r[1]);
}
__device__ __forceinline__ float xor32(float x) {
  auto r = __builtin_amdgcn_permlane32_swap(__builtin_bit_cast(unsigned, x), __builtin_bit_cast(unsigned, x), false, false);
  const int hi = (threadIdx.x >> 5) & 1;
  return __builtin_bit_cast(float, hi ? r[0] : r[1]);
}

template <bool ACQ = true>
__device__ __forceinline__ void wave_wait(unsigned* ctr, unsigned want, unsigned* err) {
  unsigned spins = 0;
  while (__hip_atomic_load(ctr, __ATOMIC_RELAXED, __HIP_MEMORY_SCOPE_AGENT) < want) {
    __builtin_amdgcn_s_sleep(4);
    if (++spins > SPINCAP) { __hip_atomic_store(err, 1u, __ATOMIC_RELAXED, __HIP_MEMORY_SCOPE_AGENT); break; }
    if ((spins & 1023u) == 0u && __hip_atomic_load(err, __ATOMIC_RELAXED, __HIP_MEMORY_SCOPE_AGENT) != 0u) break;
  }
  if (ACQ) {
    __builtin_amdgcn_fence(__ATOMIC_ACQUIRE, "agent");
    asm volatile("s_waitcnt vmcnt(0)" ::: "memory");
  } else {
    __builtin_amdgcn_fence(__ATOMIC_ACQUIRE, "wavefront");
  }
}
constexpr int CH_RED = 0;
constexpr int CH_VM = 24576;
constexpr int CH_RED2 = 25088;
constexpr int CH_WV = 27136;
constexpr int CH_KV = CH_WV + 65536;
constexpr int CH_LDS = CH_KV + 20480;

__device__ __forceinline__ void chain_role(int c, const u16* __restrict__ mem0, const u16* __restrict__ Wq, const u16* __restrict__ Wk,
                                           const u16* __restrict__ Wv, const u16* __restrict__ Wo, unsigned char* ws, LAS char* lds) {
  const int tid = threadIdx.x, lane = tid & 63, w = __builtin_amdgcn_readfirstlane(tid >> 6);
  const int fr = lane & 15, g = lane >> 4;
  const int h = c >> 2, q4 = c & 3, mh = c & 1, nb = c >> 1;
  unsigned* err = (unsigned*)(ws + WS_ERR);
  const __amdgpu_buffer_rsrc_t rs = __builtin_amdgcn_make_buffer_rsrc((void*)(ws + WS_HOP), 0, (int)HOP_BYTES, 0x00020000);
  v4u wq[4][4], wo[16];
  LAS v4u* wvl = (LAS v4u*)(lds + CH_WV) + w * 512 + lane;
  {
    LAS char* stg = lds + CH_WV + w * 8192;
    const int q = fr >> 2, p = lane & 3;
#pragma unroll
    for (int hf = 0; hf < 2; ++hf) {
#pragma unroll
      for (int i = 0; i < 8; ++i) {
        const int row = i * 32 + (lane >> 1), ch = lane & 1;
        v4u v = *(const v4u*)(Wo + (size_t)(512 * w + 256 * hf + row) * DM + 16 * nb + ch * 8);
        *(LAS v4u*)(stg + row * 32 + ch * 16) = v;
      }
      __builtin_amdgcn_s_waitcnt(0xC07F);
#pragma unroll
      for (int ks = 0; ks < 8; ++ks) {
        v2u lo = trread(stg + (32 * ks + 8 * g + q) * 32 + (4 * p) * 2);
        v2u hi = trread(stg + (32 * ks + 8 * g + 4 + q) * 32 + (4 * p) * 2);
        wo[hf * 8 + ks] = v4u{lo.x, lo.y, hi.x, hi.y};
      }
      __builtin_amdgcn_s_waitcnt(0xC07F);
    }
#pragma unroll
    for (int mat = 0; mat < 3; ++mat) {
      const u16* W = mat == 0 ? Wq : (mat == 1 ? Wk : Wv);
#pragma unroll
      for (int i = 0; i < 8; ++i) {
        const int row = i * 16 + (lane >> 2), ch = lane & 3;
        v4u v = *(const v4u*)(W + (size_t)(128 * w + row) * HD + h * 128 + 32 * q4 + ch * 8);
        *(LAS v4u*)(stg + row * 64 + ch * 16) = v;
      }
      __builtin_amdgcn_s_waitcnt(0xC07F);
      v4u tv[8];
#pragma unroll
      for (int dt = 0; dt < 2; ++dt)
#pragma unroll
        for (int ks = 0; ks < 4; ++ks) {
          v2u lo = trread(stg + (32 * ks + 8 * g + q) * 64 + (16 * dt + 4 * p) * 2);
          v2u hi = trread(stg + (32 * ks + 8 * g + 4 + q) * 64 + (16 * dt + 4 * p) * 2);
          if (mat < 2) wq[(mat & 1) * 2 + dt][ks] = v4u{lo.x, lo.y, hi.x, hi.y};
          else tv[dt * 4 + ks] = v4u{lo.x, lo.y, hi.x, hi.y};
        }
      __builtin_amdgcn_s_waitcnt(0xC07F);
      asm volatile("" ::: "memory");
      if (mat == 2) {
#pragma unroll
        for (int i = 0; i < 8; ++i) wvl[i * 64] = tv[i];
      }
    }
  }
  const float scale = 0.08838834764831845f;
  const float L2E = 1.4426950408889634f;
  LAS f32x4* red = (LAS f32x4*)(lds + CH_RED);
  LAS f32x4* red2 = (LAS f32x4*)(lds + CH_RED2);
  LAS u16* vml = (LAS u16*)(lds + CH_VM);
  LAS v4u* kvl = (LAS v4u*)(lds + CH_KV) + lane;
  const __amdgpu_buffer_rsrc_t rsw = __builtin_amdgcn_make_buffer_rsrc((void*)ws, 0, (int)WS_TOTAL, 0x00020000);
  auto kv_prefetch = [&](int sn) {
    if ((sn & 1) == 0) wave_wait<false>(err + CW_KV + 16 * (sn >> 1), 64u, err);
    v4u t[10];
#pragma unroll
    for (int tt = 0; tt < 2; ++tt)
#pragma unroll
      for (int j = 0; j < 4; ++j) t[tt * 4 + j] = ld_sc1(rsw, (int)(WS_K + ((size_t)(32 * sn + 16 * tt + fr) * HD + h * 128 + 32 * j + 8 * g) * 2));
#pragma unroll
    for (int dt = 0; dt < 2; ++dt) t[8 + dt] = ld_sc1(rsw, (int)(WS_VT + ((((size_t)sn * NH + h) * 128 + 32 * q4 + 16 * dt + fr) * 32 + 8 * g) * 2));
#pragma unroll
    for (int i = 0; i < 10; ++i) kvl[((sn & 1) * 10 + i) * 64] = t[i];
  };
  if (w == 2) kv_prefetch(0);

#define STAMP(i)
  for (int s = 0; s < NSEG; ++s) {
    v4u mf[2];
    if (s == 0) {
#pragma unroll
      for (int p = 0; p < 2; ++p)
        mf[p] = *(const v4u*)(mem0 + (size_t)(fr & 7) * DM + 128 * w + 32 * (2 * p + (fr >> 3)) + 8 * g);
    } else {
      if (DLY1) __builtin_amdgcn_s_sleep(DLY1);
      poll<2>(rs, (int)(HOP_MEM + (size_t)s * 16384) + ((16 * w + 4 * (fr >> 3) + g) * 8 + (fr & 7)) * 16, mf, err);
    }
    STAMP(0)
    {
      f32x4 a0[4], a1[4];
#pragma unroll
      for (int t = 0; t < 4; ++t) {
        a0[t] = mfma16(wq[t][0], mf[0], f32x4{0.f, 0.f, 0.f, 0.f});
        a1[t] = mfma16(wq[t][1], mf[0], f32x4{0.f, 0.f, 0.f, 0.f});
      }
#pragma unroll
      for (int t = 0; t < 4; ++t) {
        a0[t] = mfma16(wq[t][2], mf[1], a0[t]);
        a1[t] = mfma16(wq[t][3], mf[1], a1[t]);
      }
#pragma unroll
      for (int t = 0; t < 4; ++t) {
        f32x4 x = (fr & 8) ? a1[t] : a0[t];
#pragma unroll
        for (int r = 0; r < 4; ++r) x[r] += row_ror<8>(x[r]);
        if (fr < 8) red[(w * 6 + t) * 32 + g * 8 + fr] = x;
      }
    }
    __syncthreads();
    STAMP(1)
    if (tid >= 384) {
      const int u = tid - 384;
      const int t6 = u >> 5, gg = (u >> 3) & 3, m = u & 7;
      f32x4 x = red[t6 * 32 + (u & 31)];
#pragma unroll
      for (int ww = 1; ww < 8; ++ww) x += red[(ww * 6 + t6) * 32 + (u & 31)];
      const unsigned lo = pack2(x[0], x[1]), hi = pack2(x[2], x[3]);
      const int mat = t6 >> 1, dt = t6 & 1;
      const int off = (int)(HOP_QK + ((size_t)s * NH + h) * 4096) + ((q4 * 2 + mat) * 8 + m) * 64 + (16 * dt + 4 * gg) * 2;
      __builtin_amdgcn_raw_buffer_store_b64(v2u{nosent(lo), nosent(hi)}, rs, off, 0, 16);
    }
    {
      f32x4 a0[2], a1[2];
#pragma unroll
      for (int t = 0; t < 2; ++t) {
        a0[t] = mfma16(wvl[(t * 4 + 0) * 64], mf[0], f32x4{0.f, 0.f, 0.f, 0.f});
        a1[t] = mfma16(wvl[(t * 4 + 1) * 64], mf[0], f32x4{0.f, 0.f, 0.f, 0.f});
      }
#pragma unroll
      for (int t = 0; t < 2; ++t) {
        a0[t] = mfma16(wvl[(t * 4 + 2) * 64], mf[1], a0[t]);
        a1[t] = mfma16(wvl[(t * 4 + 3) * 64], mf[1], a1[t]);
      }
#pragma unroll
      for (int t = 0; t < 2; ++t) {
        f32x4 x = (fr & 8) ? a1[t] : a0[t];
#pragma unroll
        for (int r = 0; r < 4; ++r) x[r] += row_ror<8>(x[r]);
        if (fr < 8) red[(w * 6 + 4 + t) * 32 + g * 8 + fr] = x;
      }
    }
    __syncthreads();
    STAMP(2)
    if (w == 1) {
      const int dt = lane >> 5, gg = (lane >> 3) & 3, m = lane & 7;
      f32x4 x = red[(4 + dt) * 32 + (lane & 31)];
#pragma unroll
      for (int ww = 1; ww < 8; ++ww) x += red[(ww * 6 + 4 + dt) * 32 + (lane & 31)];
      const unsigned lo = pack2(x[0], x[1]), hi = pack2(x[2], x[3]);
      const int d = 16 * dt + 4 * gg;
      vml[(d + 0) * 8 + m] = (u16)(lo & 0xFFFF);
      vml[(d + 1) * 8 + m] = (u16)(lo >> 16);
      vml[(d + 2) * 8 + m] = (u16)(hi & 0xFFFF);
      vml[(d + 3) * 8 + m] = (u16)(hi >> 16);
      __builtin_amdgcn_s_waitcnt(0xC07F);
      asm volatile("" ::: "memory");
      if (lane < 32)
      {
        v4u t = *(LAS v4u*)(vml + lane * 8);
        t.x = nosent(t.x); t.y = nosent(t.y); t.z = nosent(t.z); t.w = nosent(t.w);
        __builtin_amdgcn_raw_buffer_store_b128(t, rs, (int)(HOP_VM + (((size_t)s * NH + h) * 128 + 32 * q4 + lane) * 16), 0, 16);
      }
    }
    if (s == NSEG - 1) break;
    if (w == 2 && s + 1 < NSEG - 1) kv_prefetch(s + 1);
    v4u pt, pm;
    if (w == 0) {
      v4u X[8];
      {
        const int base = (int)(HOP_QK + ((size_t)s * NH + h) * 4096) + (fr & 7) * 64 + g * 16;
        unsigned spins = 0;
        if (DLY2) __builtin_amdgcn_s_sleep(DLY2);
        for (;;) {
          unsigned mx = 0;
#pragma unroll
          for (int i = 0; i < 8; ++i) X[i] = ld_sc1(rs, base + i * 512);
          asm volatile("" ::: "memory");
#pragma unroll
          for (int i = 0; i < 8; ++i) mx = max(max(mx, max(X[i].x, X[i].y)), max(X[i].z, X[i].w));
          if (__all(mx != SENT)) break;
          if (++spins > SPINCAP) {
            if (lane == 0) __hip_atomic_store(err, 1u, __ATOMIC_RELAXED, __HIP_MEMORY_SCOPE_AGENT);
            break;
          }
          if ((spins & 1023u) == 0u && __hip_atomic_load(err, __ATOMIC_RELAXED, __HIP_MEMORY_SCOPE_AGENT) != 0u) break;
        }
      }
      STAMP(3)
      f32x4 s0 = {0.f, 0.f, 0.f, 0.f}, s1 = s0, s2 = s0;
#pragma unroll
      for (int j = 0; j < 4; ++j) {
        s1 = mfma16(kvl[((s & 1) * 10 + j) * 64], X[2 * j], s1);
        s2 = mfma16(kvl[((s & 1) * 10 + 4 + j) * 64], X[2 * j], s2);
        s0 = mfma16(X[2 * j + 1], X[2 * j], s0);
      }
      const bool up = fr >= 8;
      f32x4 st = up ? s2 : s1;
      const float m01 = up ? s0[1] : s0[0], m23 = up ? s0[3] : s0[2];
      const float sm = g >= 2 ? m23 : m01;
      unsigned p0 = pack2(st[0], st[1]), p1 = pack2(st[2], st[3]), p2 = pack2(sm, sm);
      p0 = pack2(bflo(p0) * scale, bfhi(p0) * scale);
      p1 = pack2(bflo(p1) * scale, bfhi(p1) * scale);
      p2 = pack2(bflo(p2) * scale, bflo(p2) * scale);
      float v0 = bflo(p0), v1 = bfhi(p0), v2 = bflo(p1), v3 = bfhi(p1), v4 = bflo(p2);
      float mx = fmaxf(fmaxf(fmaxf(v0, v1), fmaxf(v2, v3)), v4);
      mx = fmaxf(mx, row_ror<8>(mx));
      mx = fmaxf(mx, xor16(mx));
      mx = fmaxf(mx, xor32(mx));
      const float nm = -mx * L2E;
      v0 = __builtin_amdgcn_exp2f(__builtin_fmaf(v0, L2E, nm));
      v1 = __builtin_amdgcn_exp2f(__builtin_fmaf(v1, L2E, nm));
      v2 = __builtin_amdgcn_exp2f(__builtin_fmaf(v2, L2E, nm));
      v3 = __builtin_amdgcn_exp2f(__builtin_fmaf(v3, L2E, nm));
      v4 = __builtin_amdgcn_exp2f(__builtin_fmaf(v4, L2E, nm));
      float sum = ((v0 + v1) + (v2 + v3)) + v4;
      sum += row_ror<8>(sum);
      sum += xor16(sum);
      sum += xor32(sum);
      const float inv = __builtin_amdgcn_rcpf(sum);
      pt.x = pack2(v0 * inv, v1 * inv);
      pt.y = pack2(v2 * inv, v3 * inv);
      pt.z = (unsigned)__builtin_amdgcn_update_dpp(0, (int)pt.x, 0x128, 0xf, 0xf, false);
      pt.w = (unsigned)__builtin_amdgcn_update_dpp(0, (int)pt.y, 0x128, 0xf, 0xf, false);
      v4 *= inv;
      pm.x = pack2(v4, row_ror<8>(v4));
      pm.y = 0u; pm.z = 0u; pm.w = 0u;
    }
    __syncthreads();
    if (w == 0) {
      f32x4 o[2];
#pragma unroll
      for (int dt = 0; dt < 2; ++dt) o[dt] = mfma16(kvl[((s & 1) * 10 + 8 + dt) * 64], pt, f32x4{0.f, 0.f, 0.f, 0.f});
#pragma unroll
      for (int dt = 0; dt < 2; ++dt) {
        const unsigned t = *(LAS unsigned*)(vml + (16 * dt + fr) * 8 + 4 * (g & 1) + 2 * (g >> 1));
        o[dt] = mfma16(v4u{t, 0u, 0u, 0u}, pm, o[dt]);
      }
      if (fr < 8) {
#pragma unroll
        for (int dt = 0; dt < 2; ++dt) {
          const int k8 = h * 16 + 4 * q4 + 2 * dt + (g >> 1);
          const int off = (int)(HOP_OB + (size_t)s * 65536) + (((fr >> 2) * 512 + k8) * 4 + (fr & 3)) * 16 + (g & 1) * 8;
          __builtin_amdgcn_raw_buffer_store_b64(v2u{nosent(pack2(o[dt][0], o[dt][1])), nosent(pack2(o[dt][2], o[dt][3]))}, rs, off, 0, 16);
        }
      }
    }
    __syncthreads();
    STAMP(4)
    {
      v4u of[4];
      if (DLY3) __builtin_amdgcn_s_sleep(DLY3);
      poll<4>(rs, (int)(HOP_OB + (size_t)s * 65536) + ((mh * 512 + 64 * w + 4 * (fr >> 2) + g) * 4 + (fr & 3)) * 16, of, err);
      STAMP(5)
      f32x4 a[4];
#pragma unroll
      for (int r4 = 0; r4 < 4; ++r4) a[r4] = mfma16(wo[r4], of[0], f32x4{0.f, 0.f, 0.f, 0.f});
#pragma unroll
      for (int i = 1; i < 4; ++i)
#pragma unroll
        for (int r4 = 0; r4 < 4; ++r4) a[r4] = mfma16(wo[4 * i + r4], of[i], a[r4]);
      const int r4 = fr >> 2;
      f32x4 x = r4 == 0 ? a[0] : (r4 == 1 ? a[1] : (r4 == 2 ? a[2] : a[3]));
#pragma unroll
      for (int r = 0; r < 4; ++r) { x[r] += row_ror<8>(x[r]); x[r] += row_ror<4>(x[r]); }
      if (fr < 4) red2[w * 16 + g * 4 + fr] = x;
    }
    __syncthreads();
    if (tid < 16) {
      f32x4 x = red2[tid];
#pragma unroll
      for (int ww = 1; ww < 8; ++ww) x += red2[ww * 16 + tid];
      const int gg = tid >> 2, m4 = tid & 3;
      const int k8 = 2 * nb + (gg >> 1);
      const int off = (int)(HOP_MEM + (size_t)(s + 1) * 16384) + (k8 * 8 + 4 * mh + m4) * 16 + (gg & 1) * 8;
      __builtin_amdgcn_raw_buffer_store_b64(v2u{nosent(pack2(x[0], x[1])), nosent(pack2(x[2], x[3]))}, rs, off, 0, 16);
    }
    STAMP(6)
  }
}

__device__ __forceinline__ void attn_unit(int s, int h, int lane, unsigned char* ws) {
  const int fr = lane & 15, g = lane >> 4;
  const u16* Q = (const u16*)(ws + WS_Q);
  const u16* Kx = (const u16*)(ws + WS_K);
  const u16* Vt = (const u16*)(ws + WS_VT);
  const __amdgpu_buffer_rsrc_t rs = __builtin_amdgcn_make_buffer_rsrc((void*)(ws + WS_HOP), 0, (int)HOP_BYTES, 0x00020000);
  unsigned* err = (unsigned*)(ws + WS_ERR);
  const float scale = 0.08838834764831845f;
  f32x4 sc[3][2];
#pragma unroll
  for (int a = 0; a < 3; ++a)
#pragma unroll
    for (int b = 0; b < 2; ++b) sc[a][b] = f32x4{0.f, 0.f, 0.f, 0.f};
  v4u qf[4][2], vtf[8];
  {
    v4u kf[4][2];
#pragma unroll
    for (int j = 0; j < 4; ++j)
#pragma unroll
      for (int t = 0; t < 2; ++t) {
        qf[j][t] = *(const v4u*)(Q + (size_t)(32 * s + 16 * t + fr) * HD + h * 128 + 32 * j + 8 * g);
        kf[j][t] = *(const v4u*)(Kx + (size_t)(32 * s + 16 * t + fr) * HD + h * 128 + 32 * j + 8 * g);
      }
#pragma unroll
    for (int dt = 0; dt < 8; ++dt) vtf[dt] = *(const v4u*)(Vt + (((size_t)s * NH + h) * 128 + 16 * dt + fr) * 32 + 8 * g);
#pragma unroll
    for (int j = 0; j < 4; ++j)
#pragma unroll
      for (int a = 0; a < 2; ++a)
#pragma unroll
        for (int b = 0; b < 2; ++b) sc[1 + a][b] = mfma16(kf[j][a], qf[j][b], sc[1 + a][b]);
  }
  v4u km[4];
  v4u vmr[2];
  {
    const int o1 = (int)(HOP_QK + ((size_t)s * NH + h) * 4096) + 512 + (fr & 7) * 64 + g * 16;
    const int o2 = (int)(HOP_VM + ((size_t)s * NH + h) * 2048) + lane * 16;
    unsigned spins = 0;
    for (;;) {
      unsigned mx = 0;
#pragma unroll
      for (int i = 0; i < 4; ++i) km[i] = ld_sc1(rs, o1 + i * 1024);
#pragma unroll
      for (int i = 0; i < 2; ++i) vmr[i] = ld_sc1(rs, o2 + i * 1024);
      asm volatile("" ::: "memory");
#pragma unroll
      for (int i = 0; i < 4; ++i) mx = max(max(mx, max(km[i].x, km[i].y)), max(km[i].z, km[i].w));
#pragma unroll
      for (int i = 0; i < 2; ++i) mx = max(max(mx, max(vmr[i].x, vmr[i].y)), max(vmr[i].z, vmr[i].w));
      if (__all(mx != SENT)) break;
      __builtin_amdgcn_s_sleep(16);
      if (++spins > SPINCAP) {
        if (lane == 0) __hip_atomic_store(err, 1u, __ATOMIC_RELAXED, __HIP_MEMORY_SCOPE_AGENT);
        break;
      }
      if ((spins & 1023u) == 0u && __hip_atomic_load(err, __ATOMIC_RELAXED, __HIP_MEMORY_SCOPE_AGENT) != 0u) break;
    }
  }
#pragma unroll
  for (int j = 0; j < 4; ++j)
#pragma unroll
    for (int b = 0; b < 2; ++b) sc[0][b] = mfma16(km[j], qf[j][b], sc[0][b]);
  v4u pt[2], pm[2];
#pragma unroll
  for (int b = 0; b < 2; ++b) {
    const int qi = 16 * b + fr;
    float v[12];
    bool ok[12];
#pragma unroll
    for (int r = 0; r < 4; ++r) {
      v[r] = rbf(rbf(sc[0][b][r]) * scale); ok[r] = g < 2;
      v[4 + r] = rbf(rbf(sc[1][b][r]) * scale); ok[4 + r] = (4 * g + r) <= qi;
      v[8 + r] = rbf(rbf(sc[2][b][r]) * scale); ok[8 + r] = (16 + 4 * g + r) <= qi;
    }
    float mx = -3.0e38f;
#pragma unroll
    for (int i = 0; i < 12; ++i) mx = ok[i] ? fmaxf(mx, v[i]) : mx;
    mx = fmaxf(mx, __shfl_xor(mx, 16));
    mx = fmaxf(mx, __shfl_xor(mx, 32));
    float sum = 0.f;
#pragma unroll
    for (int i = 0; i < 12; ++i) { v[i] = ok[i] ? __expf(v[i] - mx) : 0.f; sum += v[i]; }
    sum += __shfl_xor(sum, 16);
    sum += __shfl_xor(sum, 32);
    const float inv = 1.0f / sum;
    pt[b].x = pack2(v[4] * inv, v[5] * inv); pt[b].y = pack2(v[6] * inv, v[7] * inv);
    pt[b].z = pack2(v[8] * inv, v[9] * inv); pt[b].w = pack2(v[10] * inv, v[11] * inv);
    pm[b].x = pack2(v[0] * inv, v[1] * inv); pm[b].y = pack2(v[2] * inv, v[3] * inv); pm[b].z = 0u; pm[b].w = 0u;
  }
#pragma unroll
  for (int dt = 0; dt < 8; ++dt) {
    v4u vmf;
    {
      const int src = (16 * dt + fr) & 63;
      const v4u vv = vmr[dt >> 2];
      const unsigned a0 = __shfl(vv.x, src), a1 = __shfl(vv.y, src), a2 = __shfl(vv.z, src), a3 = __shfl(vv.w, src);
      vmf.x = g == 0 ? a0 : (g == 1 ? a2 : 0u);
      vmf.y = g == 0 ? a1 : (g == 1 ? a3 : 0u);
      vmf.z = 0u; vmf.w = 0u;
    }
#pragma unroll
    for (int b = 0; b < 2; ++b) {
      f32x4 o = {0.f, 0.f, 0.f, 0.f};
      o = mfma16(vtf[dt], pt[b], o);
      o = mfma16(vmf, pm[b], o);
      wt8(ws, WS_OT + ((size_t)(32 * s + 16 * b + fr) * HD + h * 128 + 16 * dt + 4 * g) * 2, v2u{pack2(o[0], o[1]), pack2(o[2], o[3])});
    }
  }
}
__device__ __forceinline__ void wg_signal(unsigned* ctr, int n) {
  asm volatile("s_waitcnt vmcnt(0)" ::: "memory");
  __syncthreads();
  if (threadIdx.x < n) __hip_atomic_fetch_add(ctr + 16 * threadIdx.x, 1u, __ATOMIC_RELAXED, __HIP_MEMORY_SCOPE_AGENT);
}
__device__ __forceinline__ void wg_wait(unsigned* ctr, unsigned want, unsigned* err) {
  if (threadIdx.x < 64) wave_wait(ctr, want, err);
  __syncthreads();
}

__device__ __forceinline__ void wait_phase_abc(unsigned* cw) {
  unsigned* err = cw + CW_ERR;
  if (threadIdx.x < 64) {
    const int l = threadIdx.x;
    unsigned* p = l < 32 ? cw + CW_KV + 16 * l : (l == 32 ? cw + CW_Q : cw + CW_MLP2);
    const unsigned want = l < 32 ? 64u : (l < 34 ? 128u : 0u);
    unsigned spins = 0;
    while (!__all(__hip_atomic_load(p, __ATOMIC_RELAXED, __HIP_MEMORY_SCOPE_AGENT) >= want)) {
      __builtin_amdgcn_s_sleep(8);
      if (++spins > SPINCAP) { __hip_atomic_store(err, 1u, __ATOMIC_RELAXED, __HIP_MEMORY_SCOPE_AGENT); break; }
      if ((spins & 1023u) == 0u && __hip_atomic_load(err, __ATOMIC_RELAXED, __HIP_MEMORY_SCOPE_AGENT) != 0u) break;
    }
    __builtin_amdgcn_fence(__ATOMIC_ACQUIRE, "agent");
    asm volatile("s_waitcnt vmcnt(0)" ::: "memory");
  }
  __syncthreads();
}
#define do_attn(sg_, hg_, ci_) { attn_unit((sg_), (hg_) * 8 + (threadIdx.x >> 6), threadIdx.x & 63, ws); wg_signal(cw + CW_AT + 16 * (ci_), 1); }

template <int NS>
__device__ __forceinline__ void tail_unit(int u, int idx, const u16* x, const u16* Wo, u16* out, unsigned char* ws, LAS char* lds) {
  unsigned* cw = (unsigned*)(ws + WS_ERR);
  unsigned* err = cw + CW_ERR;
  constexpr int KS = HD / NS, RR = 64 / NS;
  const int ct = idx / NS, ks = idx % NS;
  const __amdgpu_buffer_rsrc_t rsw = __builtin_amdgcn_make_buffer_rsrc((void*)ws, 0, (int)WS_TOTAL, 0x00020000);
#define TSTAMP(i)
  TSTAMP(0)
  TSTAMP(1)
  wg_wait(cw + CW_AT + 16 * u, 8u, err);
  TSTAMP(2)
  const int slot = (u - TAIL_U0) * 8 + ct;
  const size_t sbase = WS_SLAB + (size_t)slot * 16 * 32768;
  gemm_tile<64, 2, 4, false>((const u16*)(ws + WS_OT) + ks * KS, HD, Wo + (size_t)ks * KS * DM, DM, KS, u * 64, ct * 128, lds,
                             [&](int r, int c, f32x4 v) {
                               __builtin_amdgcn_raw_buffer_store_b128(__builtin_bit_cast(v4u, v), rsw,
                                   (int)(sbase + (size_t)ks * 32768 + (((r & 63) * 128 + (c & 127)) * 4)), 0, 16);
                             });
  TSTAMP(3)
  wg_signal(cw + CW_SK + 16 * slot, 1);
  TSTAMP(4)
  if (threadIdx.x < 64) wave_wait<false>(cw + CW_SK + 16 * slot, (unsigned)NS, err);
  __syncthreads();
  TSTAMP(5)
  if (threadIdx.x < RR * 32) {
    const int rr = RR * ks + (threadIdx.x >> 5), cc = (threadIdx.x & 31) * 4;
    v4u pv[NS];
#pragma unroll
    for (int k2 = 0; k2 < NS; ++k2) pv[k2] = ld_sc1(rsw, (int)(sbase + (size_t)k2 * 32768 + (rr * 128 + cc) * 4));
    f32x4 v = __builtin_bit_cast(f32x4, pv[0]);
#pragma unroll
    for (int k2 = 1; k2 < NS; ++k2) v += __builtin_bit_cast(f32x4, pv[k2]);
    const int r = u * 64 + rr, c = ct * 128 + cc;
    const v2u xx = *(const v2u*)(x + (size_t)r * DM + c);
    const v2u bb = *(const v2u*)((const u16*)(ws + WS_BO) + (size_t)r * DM + c);
    const float o0 = rbf(bflo(xx.x) + rbf(v[0])) + bflo(bb.x), o1 = rbf(bfhi(xx.x) + rbf(v[1])) + bfhi(bb.x);
    const float o2 = rbf(bflo(xx.y) + rbf(v[2])) + bflo(bb.y), o3 = rbf(bfhi(xx.y) + rbf(v[3])) + bfhi(bb.y);
    *(v2u*)(out + (size_t)r * DM + c) = v2u{pack2(o0, o1), pack2(o2, o3)};
  }
}

__global__ __launch_bounds__(512, 2) void k_mega(const u16* x, const u16* mem0, const u16* Wq, const u16* Wk, const u16* Wv, const u16* Wo,
                                                 const u16* w1, const u16* b1, const u16* w2, const u16* b2, u16* out, unsigned char* ws) {
  extern __shared__ __attribute__((aligned(16))) char smem[];
  LAS char* lds = (LAS char*)smem;
  const int b = blockIdx.x;
#define ASTAMP(i)
  ASTAMP(0)
  const int xcd = b & 7, i = b >> 4;
  unsigned* cw = (unsigned*)(ws + WS_ERR);
  unsigned* err = cw + CW_ERR;
  if (((b >> 3) & 1) == 0) {
    const int c = ((xcd * 4 + (i >> 2)) << 2) | (i & 3);
    chain_role(c, mem0, Wq, Wk, Wv, Wo, ws, lds);
    ASTAMP(6)
    wait_phase_abc(cw);
    tail_unit<16>(31, xcd * 16 + i, x, Wo, out, ws, lds);
    ASTAMP(7)
    return;
  }
  const int gi = i * 8 + xcd;
  task_qkv<64, 2, 4>(gi >> 6, 32 + (gi & 63), Wq, Wk, Wv, ws, lds);
  wg_signal(cw + CW_KV + 16 * (gi >> 6), 1);
  for (int t = gi; t < 64 + 7 * 64; t += 128) {
    if (t < 64) {
      task_qkv<128, 2, 4>(1, 32 + t, Wq, Wk, Wv, ws, lds);
      wg_signal(cw + CW_KV + 16 * 2, 2);
    } else {
      const int rt = 1 + ((t - 64) >> 6);
      task_qkv<256, 4, 2>(rt, 32 + ((t - 64) & 63), Wq, Wk, Wv, ws, lds);
      wg_signal(cw + CW_KV + 16 * 4 * rt, 4);
    }
  }
  ASTAMP(1)
  for (int t = gi; t < 8 * 28; t += 128) task_mlp1<256, 4, 2>(t & 7, t >> 3, w1, b1, ws, lds);
  wg_signal(cw + CW_MLP1, 1);
  for (int t = gi; t < 8 * 32; t += 128) task_qkv<256, 4, 2>(t & 7, t >> 3, Wq, Wk, Wv, ws, lds);
  wg_signal(cw + CW_Q, 1);
  ASTAMP(2)
  wg_wait(cw + CW_MLP1, 128u, err);
  task_mlp2<128, 2, 4>(gi >> 3, gi & 7, w2, b2, ws, lds);
  wg_signal(cw + CW_MLP2, 1);
  ASTAMP(3)
  wait_phase_abc(cw);
  for (int t = gi; t < 160; t += 128) do_attn(t >> 2, t & 3, (t >> 4) * 2)
  if (gi >= 96) {
    for (int t = 160 + (gi - 96); t < 256; t += 32) do_attn(t >> 2, t & 3, t < 192 ? (t >> 4) * 2 : (t >> 3))
  } else {
    {
      const int pr = gi >> 3;
      wg_wait(cw + CW_AT + 16 * 2 * pr, 16u, err);
      task_wo<128, 2, 4>(pr, gi & 7, Wo, x, out, ws, lds);
    }
    if (gi < 64) {
      for (int u = TAIL_U0; u < 31; u += 2) tail_unit<8>(u, (gi & 7) * 8 + (gi >> 3), x, Wo, out, ws, lds);
    } else {
      for (int u = TAIL_U0 + 1; u < 31; u += 2) tail_unit<4>(u, (gi & 7) * 4 + ((gi - 64) >> 3), x, Wo, out, ws, lds);
    }
  }
  ASTAMP(7)
}

extern "C" void kernel_launch(void* const* d_in, const int* in_sizes, int n_in, void* d_out, int out_size, void* d_ws, size_t ws_size,
                              hipStream_t stream) {
  const u16* x = (const u16*)d_in[0];
  const u16* mem0 = (const u16*)d_in[1];
  const u16* Wq = (const u16*)d_in[2];
  const u16* Wk = (const u16*)d_in[3];
  const u16* Wv = (const u16*)d_in[4];
  const u16* Wo = (const u16*)d_in[5];
  const u16* w1 = (const u16*)d_in[8];
  const u16* b1 = (const u16*)d_in[9];
  const u16* w2 = (const u16*)d_in[10];
  const u16* b2 = (const u16*)d_in[11];
  unsigned char* ws = (unsigned char*)d_ws;
  u16* out = (u16*)d_out;
  constexpr int L256 = GemmCfg<256>::LDS_BYTES;
  constexpr int LMEGA = L256 > CH_LDS ? L256 : CH_LDS;
  (void)hipFuncSetAttribute((const void*)k_mega, hipFuncAttributeMaxDynamicSharedMemorySize, LMEGA);
  k_ln<<<S_LEN / 4, 256, 0, stream>>>(x, ws);
  k_mega<<<256, 512, LMEGA, stream>>>(x, mem0, Wq, Wk, Wv, Wo, w1, b1, w2, b2, out, ws);
}
```

```cpp
#include <hip/hip_runtime.h>
#include <hip/hip_bf16.h>
#include <stdint.h>

constexpr int S_LEN = 2048, DM = 1024, NH = 32, DH = 128, HD = NH * DH;
constexpr int MEMN = 8, SEG = 32, NSEG = S_LEN / SEG;
constexpr int FF = 3584;

typedef unsigned v4u __attribute__((ext_vector_type(4)));
typedef unsigned v2u __attribute__((ext_vector_type(2)));
typedef float f32x4 __attribute__((ext_vector_type(4)));
typedef float f32x2 __attribute__((ext_vector_type(2)));
typedef short bf16x8 __attribute__((ext_vector_type(8)));
typedef short s16x4 __attribute__((ext_vector_type(4)));
typedef __bf16 bfx2 __attribute__((ext_vector_type(2)));
typedef unsigned short u16;
#define LAS __attribute__((address_space(3)))
#define GAS __attribute__((address_space(1)))

constexpr size_t WS_XN = 0;
constexpr size_t WS_Y = WS_XN + (size_t)S_LEN * DM * 2;
constexpr size_t WS_Q = WS_Y + (size_t)S_LEN * DM * 2;
constexpr size_t WS_K = WS_Q + (size_t)S_LEN * HD * 2;
constexpr size_t WS_VT = WS_K + (size_t)S_LEN * HD * 2;
constexpr size_t WS_H = WS_VT + (size_t)S_LEN * HD * 2;
constexpr size_t WS_BO = WS_H + (size_t)S_LEN * FF * 2;
constexpr size_t WS_OT = WS_BO + (size_t)S_LEN * DM * 2;
constexpr size_t WS_SLAB = WS_OT + (size_t)S_LEN * HD * 2;
constexpr int TAIL_U0 = 24;
constexpr size_t WS_HOP = WS_SLAB + (size_t)64 * 16 * 64 * 128 * 4;
constexpr size_t HOP_MEM = 0;
constexpr size_t HOP_QK = HOP_MEM + (size_t)NSEG * 16384;
constexpr size_t HOP_OB = HOP_QK + (size_t)NSEG * NH * 4096;
constexpr size_t HOP_VM = HOP_OB + (size_t)NSEG * 65536;
constexpr size_t HOP_BYTES = HOP_VM + (size_t)NSEG * HD * MEMN * 2;
constexpr size_t WS_ERR = WS_HOP + HOP_BYTES;
constexpr size_t WS_TOTAL = WS_ERR + 16384;
constexpr int CW_ERR = 0, CW_MLP1 = 16, CW_Q = 32, CW_MLP2 = 48, CW_KV = 64  , CW_AT = 64 + 512  , CW_SK = 64 + 1024  ;

#define PF_POS 0
#define DLY1 14
#define DLY2 0
#define DLY3 14
#define DLYR 0
constexpr unsigned SENT = 0xFFFFFFFFu;
constexpr unsigned SPINCAP = 1u << 20;

__device__ __forceinline__ float bf2f(unsigned b16) { return __builtin_bit_cast(float, b16 << 16); }
__device__ __forceinline__ float bflo(unsigned x) { return __builtin_bit_cast(float, x << 16); }
__device__ __forceinline__ float bfhi(unsigned x) { return __builtin_bit_cast(float, x & 0xFFFF0000u); }
__device__ __forceinline__ unsigned pack2(float a, float b) {
  f32x2 v = {a, b};
  bfx2 r = __builtin_convertvector(v, bfx2);
  return __builtin_bit_cast(unsigned, r);
}
__device__ __forceinline__ float rbf(float a) { return (float)(__bf16)a; }
__device__ __forceinline__ unsigned nosent(unsigned x) { return x == SENT ? 0xFFFFFFFEu : x; }
__device__ __forceinline__ f32x4 mfma16(v4u a, v4u b, f32x4 c) {
  return __builtin_amdgcn_mfma_f32_16x16x32_bf16(__builtin_bit_cast(bf16x8, a), __builtin_bit_cast(bf16x8, b), c, 0, 0, 0);
}
__device__ __forceinline__ v2u trread(LAS const char* p) {
  typedef short v4i16_t __attribute__((ext_vector_type(4)));
  return __builtin_bit_cast(v2u, __builtin_amdgcn_ds_read_tr16_b64_v4i16((LAS v4i16_t*)p));
}
__device__ __forceinline__ float ror(float x, int) = delete;
template <int N> __device__ __forceinline__ float row_ror(float x) {
  return __builtin_bit_cast(float, __builtin_amdgcn_update_dpp(0, __builtin_bit_cast(int, x), 0x120 + N, 0xf, 0xf, false));
}

__device__ __forceinline__ float wave_sum(float v) {
#pragma unroll
  for (int o = 32; o > 0; o >>= 1) v += __shfl_xor(v, o);
  return v;
}
__device__ __forceinline__ void ln16(float (&v)[16]) {
  float s = 0.f;
#pragma unroll
  for (int i = 0; i < 16; ++i) s += v[i];
  s = wave_sum(s);
  const float mean = s * (1.0f / DM);
  float q = 0.f;
#pragma unroll
  for (int i = 0; i < 16; ++i) { float d = v[i] - mean; q += d * d; }
  q = wave_sum(q);
  const float mb = rbf(mean);
  const float vb = rbf(q * (1.0f / DM));
  const float den = rbf(sqrtf(rbf(vb + 1e-5f)));
#pragma unroll
  for (int i = 0; i < 16; ++i) v[i] = rbf(rbf(v[i] - mb) / den);
}
__global__ __launch_bounds__(256) void k_ln(const u16* __restrict__ x, unsigned char* __restrict__ ws) {
  const int tid = threadIdx.x, lane = tid & 63, w = tid >> 6;
  const int row = blockIdx.x * 4 + w;
  if (blockIdx.x < 16) ((unsigned*)(ws + WS_ERR))[blockIdx.x * 256 + tid] = 0u;
  const v4u* xr = (const v4u*)(x + (size_t)row * DM);
  v4u a = xr[lane], b = xr[64 + lane];
  float v[16];
  v[0] = bflo(a.x); v[1] = bfhi(a.x); v[2] = bflo(a.y); v[3] = bfhi(a.y);
  v[4] = bflo(a.z); v[5] = bfhi(a.z); v[6] = bflo(a.w); v[7] = bfhi(a.w);
  v[8] = bflo(b.x); v[9] = bfhi(b.x); v[10] = bflo(b.y); v[11] = bfhi(b.y);
  v[12] = bflo(b.z); v[13] = bfhi(b.z); v[14] = bflo(b.w); v[15] = bfhi(b.w);
  ln16(v);
  v4u* xn = (v4u*)(ws + WS_XN + (size_t)row * DM * 2);
  xn[lane] = v4u{pack2(v[0], v[1]), pack2(v[2], v[3]), pack2(v[4], v[5]), pack2(v[6], v[7])};
  xn[64 + lane] = v4u{pack2(v[8], v[9]), pack2(v[10], v[11]), pack2(v[12], v[13]), pack2(v[14], v[15])};
  ln16(v);
  v4u* y = (v4u*)(ws + WS_Y + (size_t)row * DM * 2);
  y[lane] = v4u{pack2(v[0], v[1]), pack2(v[2], v[3]), pack2(v[4], v[5]), pack2(v[6], v[7])};
  y[64 + lane] = v4u{pack2(v[8], v[9]), pack2(v[10], v[11]), pack2(v[12], v[13]), pack2(v[14], v[15])};
}

template <int BM> struct GemmCfg {
  static constexpr int BN = 128, BK = 64;
  static constexpr int A_BYTES = BM * BK * 2, B_BYTES = BK * BN * 2, ST_BYTES = A_BYTES + B_BYTES;
  static constexpr int NST = BM == 256 ? 3 : (BM == 128 ? 4 : 6);
  static constexpr int LDS_BYTES = NST * ST_BYTES;
};

template <int N> __device__ __forceinline__ void wait_vm() { asm volatile("s_waitcnt vmcnt(%0)" ::"n"(N) : "memory"); }

template <int BM, int WM, int WN, bool TOUT, class Epi>
__device__ __forceinline__ void gemm_tile(const u16* __restrict__ A, int lda, const u16* __restrict__ B, int ldb, int K,
                                          int brow, int bcol, LAS char* lds, Epi epi) {
  using C = GemmCfg<BM>;
  constexpr int TM = BM / WM / 16, TN = C::BN / WN / 16;
  constexpr int AI = C::A_BYTES / 8192, BI = C::B_BYTES / 8192;
  const int tid = threadIdx.x, lane = tid & 63, w = __builtin_amdgcn_readfirstlane(tid >> 6);
  const int wr = w / WN, wc = w % WN, fr = lane & 15, fq = lane >> 4;
  const u16* asrc[AI];
  const u16* bsrc[BI];
#pragma unroll
  for (int i = 0; i < AI; ++i) {
    const int ii = w * AI + i, row = ii * 8 + (lane >> 3), ch = (lane & 7) ^ (row & 7);
    asrc[i] = A + (size_t)(brow + row) * lda + ch * 8;
  }
#pragma unroll
  for (int i = 0; i < BI; ++i) {
    const int ii = w * BI + i, row = ii * 4 + (lane >> 4), ch = (lane & 15) ^ (((row & 3) << 2) | ((row >> 2) & 3));
    bsrc[i] = B + (size_t)row * ldb + bcol + ch * 8;
  }
  auto stage = [&](int t, int buf) {
    LAS char* base = lds + buf * C::ST_BYTES;
#pragma unroll
    for (int i = 0; i < AI; ++i)
      __builtin_amdgcn_global_load_lds((const GAS void*)(asrc[i] + t * 64), (LAS void*)(base + (w * AI + i) * 1024), 16, 0, 0);
#pragma unroll
    for (int i = 0; i < BI; ++i)
      __builtin_amdgcn_global_load_lds((const GAS void*)(bsrc[i] + (size_t)t * 64 * ldb), (LAS void*)(base + C::A_BYTES + (w * BI + i) * 1024), 16, 0, 0);
  };
  int aoff[TM][2], boff[TN][2][2];
#pragma unroll
  for (int m = 0; m < TM; ++m)
#pragma unroll
    for (int k = 0; k < 2; ++k) {
      const int row = wr * (TM * 16) + m * 16 + fr, ch = k * 4 + fq;
      aoff[m][k] = row * 128 + ((ch ^ (row & 7)) * 16);
    }
  {
    const int q = fr >> 2, p = lane & 3;
#pragma unroll
    for (int n = 0; n < TN; ++n)
#pragma unroll
      for (int k = 0; k < 2; ++k)
#pragma unroll
        for (int t = 0; t < 2; ++t) {
          const int row = 32 * k + 8 * fq + 4 * t + q, c = wc * TN + n, ch = 2 * c + (p >> 1);
          boff[n][k][t] = C::A_BYTES + 256 * row + 16 * (ch ^ (((row & 3) << 2) | ((row >> 2) & 3))) + 8 * (p & 1);
        }
  }
  f32x4 acc[TM][TN];
#pragma unroll
  for (int m = 0; m < TM; ++m)
#pragma unroll
    for (int n = 0; n < TN; ++n) acc[m][n] = f32x4{0.f, 0.f, 0.f, 0.f};
  const int nt = K / 64;
  constexpr int NST = C::NST, PER = AI + BI;
#pragma unroll
  for (int i = 0; i < NST - 1; ++i)
    if (i < nt) stage(i, i);
  int rb = 0, sb = NST - 1;
  for (int t = 0; t < nt; ++t) {
    {
      const int rem = nt - 1 - t;
      if (rem >= NST - 2) wait_vm<(NST - 2) * PER>();
      else if (NST > 5 && rem == 3) wait_vm<3 * PER>();
      else if (NST > 4 && rem == 2) wait_vm<2 * PER>();
      else if (NST > 3 && rem == 1) wait_vm<PER>();
      else wait_vm<0>();
    }
    asm volatile("s_waitcnt lgkmcnt(0)" ::: "memory");
    __builtin_amdgcn_s_barrier();
    asm volatile("" ::: "memory");
    if (t + NST - 1 < nt) stage(t + NST - 1, sb);
    LAS const char* base = lds + rb * C::ST_BYTES;
    v4u af[TM][2], bfr[TN][2];
#pragma unroll
    for (int k = 0; k < 2; ++k) {
#pragma unroll
      for (int n = 0; n < TN; ++n) {
        v2u lo = trread(base + boff[n][k][0]), hi = trread(base + boff[n][k][1]);
        bfr[n][k] = v4u{lo.x, lo.y, hi.x, hi.y};
      }
#pragma unroll
      for (int m = 0; m < TM; ++m) af[m][k] = *(LAS const v4u*)(base + aoff[m][k]);
    }
    __builtin_amdgcn_s_setprio(1);
#pragma unroll
    for (int k = 0; k < 2; ++k)
#pragma unroll
      for (int m = 0; m < TM; ++m)
#pragma unroll
        for (int n = 0; n < TN; ++n)
          acc[m][n] = TOUT ? mfma16(af[m][k], bfr[n][k], acc[m][n]) : mfma16(bfr[n][k], af[m][k], acc[m][n]);
    __builtin_amdgcn_s_setprio(0);
    rb = rb == NST - 1 ? 0 : rb + 1;
    sb = sb == NST - 1 ? 0 : sb + 1;
  }
#pragma unroll
  for (int m = 0; m < TM; ++m)
#pragma unroll
    for (int n = 0; n < TN; ++n) {
      const int r0 = brow + wr * (TM * 16) + m * 16, c0 = bcol + wc * (TN * 16) + n * 16;
      if (TOUT) epi(r0 + fq * 4, c0 + fr, acc[m][n]);
      else epi(r0 + fr, c0 + fq * 4, acc[m][n]);
    }
  asm volatile("s_waitcnt lgkmcnt(0)" ::: "memory");
  __builtin_amdgcn_s_barrier();
}

__device__ __forceinline__ void wt8(unsigned char* ws, size_t off, v2u v) {
  const __amdgpu_buffer_rsrc_t rs = __builtin_amdgcn_make_buffer_rsrc((void*)ws, 0, (int)WS_TOTAL, 0x00020000);
  __builtin_amdgcn_raw_buffer_store_b64(v, rs, (int)off, 0, 16);
}
__device__ __forceinline__ float gelu_tanh(float x) {
  const float u = 0.7978845608028654f * (x + 0.044715f * x * x * x);
  const float e = __expf(2.0f * u);
  const float t = 1.0f - 2.0f / (e + 1.0f);
  return 0.5f * x * (1.0f + t);
}

template <int BM, int WM, int WN>
__device__ __forceinline__ void task_qkv(int rt, int ct, const u16* Wq, const u16* Wk, const u16* Wv, unsigned char* ws, LAS char* lds) {
  const u16* xn = (const u16*)(ws + WS_XN);
  const int which = ct >> 5, bcol = (ct & 31) * 128, brow = rt * BM;
  if (which < 2) {
    const size_t ob = which == 0 ? WS_Q : WS_K;
    gemm_tile<BM, WM, WN, false>(xn, DM, which == 0 ? Wq : Wk, HD, DM, brow, bcol, lds, [&](int r, int c, f32x4 v) {
      wt8(ws, ob + ((size_t)r * HD + c) * 2, v2u{pack2(v[0], v[1]), pack2(v[2], v[3])});
    });
  } else {
    gemm_tile<BM, WM, WN, true>(xn, DM, Wv, HD, DM, brow, bcol, lds, [&](int r, int c, f32x4 v) {
      const int seg = r >> 5, t = r & 31, p = 8 * ((t & 15) >> 2) + 4 * (t >> 4);
      wt8(ws, WS_VT + (((size_t)seg * HD + c) * 32 + p) * 2, v2u{pack2(v[0], v[1]), pack2(v[2], v[3])});
    });
  }
}
template <int BM, int WM, int WN>
__device__ __forceinline__ void task_mlp1(int rt, int ct, const u16* w1, const u16* b1, unsigned char* ws, LAS char* lds) {
  const u16* y = (const u16*)(ws + WS_Y);
  gemm_tile<BM, WM, WN, false>(y, DM, w1, FF, DM, rt * BM, ct * 128, lds, [&](int r, int c, f32x4 v) {
    const v2u bb = *(const v2u*)(b1 + c);
    const float o0 = gelu_tanh(rbf(rbf(v[0]) + bflo(bb.x))), o1 = gelu_tanh(rbf(rbf(v[1]) + bfhi(bb.x)));
    const float o2 = gelu_tanh(rbf(rbf(v[2]) + bflo(bb.y))), o3 = gelu_tanh(rbf(rbf(v[3]) + bfhi(bb.y)));
    wt8(ws, WS_H + ((size_t)r * FF + c) * 2, v2u{pack2(o0, o1), pack2(o2, o3)});
  });
}
template <int BM, int WM, int WN>
__device__ __forceinline__ void task_mlp2(int rt, int ct, const u16* w2, const u16* b2, unsigned char* ws, LAS char* lds) {
  const u16* h = (const u16*)(ws + WS_H);
  gemm_tile<BM, WM, WN, false>(h, FF, w2, DM, FF, rt * BM, ct * 128, lds, [&](int r, int c, f32x4 v) {
    const v2u bb = *(const v2u*)(b2 + c);
    wt8(ws, WS_BO + ((size_t)r * DM + c) * 2, v2u{pack2(rbf(v[0]) + bflo(bb.x), rbf(v[1]) + bfhi(bb.x)), pack2(rbf(v[2]) + bflo(bb.y), rbf(v[3]) + bfhi(bb.y))});
  });
}
template <int BM, int WM, int WN>
__device__ __forceinline__ void task_wo(int rt, int ct, const u16* Wo, const u16* x, u16* out, unsigned char* ws, LAS char* lds) {
  const u16* ot = (const u16*)(ws + WS_OT);
  const u16* bo = (const u16*)(ws + WS_BO);
  gemm_tile<BM, WM, WN, false>(ot, HD, Wo, DM, HD, rt * BM, ct * 128, lds, [&](int r, int c, f32x4 v) {
    const v2u xx = *(const v2u*)(x + (size_t)r * DM + c);
    const v2u bb = *(const v2u*)(bo + (size_t)r * DM + c);
    const float o0 = rbf(bflo(xx.x) + rbf(v[0])) + bflo(bb.x), o1 = rbf(bfhi(xx.x) + rbf(v[1])) + bfhi(bb.x);
    const float o2 = rbf(bflo(xx.y) + rbf(v[2])) + bflo(bb.y), o3 = rbf(bfhi(xx.y) + rbf(v[3])) + bfhi(bb.y);
    *(v2u*)(out + (size_t)r * DM + c) = v2u{pack2(o0, o1), pack2(o2, o3)};
  });
}

__device__ __forceinline__ bool rdy(v4u v) { return max(max(v.x, v.y), max(v.z, v.w)) != SENT; }
__device__ __forceinline__ v4u ld_sc1(__amdgpu_buffer_rsrc_t rs, int off) { return __builtin_amdgcn_raw_buffer_load_b128(rs, off, 0, 16); }

template <int N>
__device__ __forceinline__ void poll(__amdgpu_buffer_rsrc_t rs, int off, v4u (&d)[N], unsigned* err) {
  unsigned spins = 0;
  for (;;) {
    unsigned mx = 0;
#pragma unroll
    for (int i = 0; i < N; ++i) d[i] = ld_sc1(rs, off + i * 1024);
    asm volatile("" ::: "memory");
#pragma unroll
    for (int i = 0; i < N; ++i) mx = max(max(mx, max(d[i].x, d[i].y)), max(d[i].z, d[i].w));
    if (__all(mx != SENT)) break;
    if (DLYR) __builtin_amdgcn_s_sleep(DLYR);
    if (++spins > SPINCAP) {
      if ((threadIdx.x & 63) == 0) __hip_atomic_store(err, 1u, __ATOMIC_RELAXED, __HIP_MEMORY_SCOPE_AGENT);
      break;
    }
    if ((spins & 1023u) == 0u && __hip_atomic_load(err, __ATOMIC_RELAXED, __HIP_MEMORY_SCOPE_AGENT) != 0u) break;
  }
}
__device__ __forceinline__ float xor16(float x) {
  auto r = __builtin_amdgcn_permlane16_swap(__builtin_bit_cast(unsigned, x), __builtin_bit_cast(unsigned, x), false, false);
  const int odd = (threadIdx.x >> 4) & 1;
  return __builtin_bit_cast(float, odd ? r[0] : r[1]);
}
__device__ __forceinline__ float xor32(float x) {
  auto r = __builtin_amdgcn_permlane32_swap(__builtin_bit_cast(unsigned, x), __builtin_bit_cast(unsigned, x), false, false);
  const int hi = (threadIdx.x >> 5) & 1;
  return __builtin_bit_cast(float, hi ? r[0] : r[1]);
}

template <bool ACQ = true>
__device__ __forceinline__ void wave_wait(unsigned* ctr, unsigned want, unsigned* err) {
  unsigned spins = 0;
  while (__hip_atomic_load(ctr, __ATOMIC_RELAXED, __HIP_MEMORY_SCOPE_AGENT) < want) {
    __builtin_amdgcn_s_sleep(4);
    if (++spins > SPINCAP) { __hip_atomic_store(err, 1u, __ATOMIC_RELAXED, __HIP_MEMORY_SCOPE_AGENT); break; }
    if ((spins & 1023u) == 0u && __hip_atomic_load(err, __ATOMIC_RELAXED, __HIP_MEMORY_SCOPE_AGENT) != 0u) break;
  }
  if (ACQ) {
    __builtin_amdgcn_fence(__ATOMIC_ACQUIRE, "agent");
    asm volatile("s_waitcnt vmcnt(0)" ::: "memory");
  } else {
    __builtin_amdgcn_fence(__ATOMIC_ACQUIRE, "wavefront");
  }
}
constexpr int CH_RED = 0;
constexpr int CH_VM = 24576;
constexpr int CH_RED2 = 25088;
constexpr int CH_WV = 27136;
constexpr int CH_KV = CH_WV + 65536;
constexpr int CH_CNT = CH_KV + 20480;
constexpr int CH_LDS = CH_CNT + 64;

__device__ __forceinline__ void chain_role(int c, const u16* __restrict__ mem0, const u16* __restrict__ Wq, const u16* __restrict__ Wk,
                                           const u16* __restrict__ Wv, const u16* __restrict__ Wo, unsigned char* ws, LAS char* lds) {
  const int tid = threadIdx.x, lane = tid & 63, w = __builtin_amdgcn_readfirstlane(tid >> 6);
  const int fr = lane & 15, g = lane >> 4;
  const int h = c >> 2, q4 = c & 3, mh = c & 1, nb = c >> 1;
  unsigned* err = (unsigned*)(ws + WS_ERR);
  const __amdgpu_buffer_rsrc_t rs = __builtin_amdgcn_make_buffer_rsrc((void*)(ws + WS_HOP), 0, (int)HOP_BYTES, 0x00020000);
  v4u wq[4][4], wo[16];
  LAS v4u* wvl = (LAS v4u*)(lds + CH_WV) + w * 512 + lane;
  {
    LAS char* stg = lds + CH_WV + w * 8192;
    const int q = fr >> 2, p = lane & 3;
    auto ld_wo = [&](int hf, v4u (&t)[8]) {
#pragma unroll
      for (int i = 0; i < 8; ++i) t[i] = *(const v4u*)(Wo + (size_t)(512 * w + 256 * hf + i * 32 + (lane >> 1)) * DM + 16 * nb + (lane & 1) * 8);
    };
    auto ld_w = [&](const u16* W, v4u (&t)[8]) {
#pragma unroll
      for (int i = 0; i < 8; ++i) t[i] = *(const v4u*)(W + (size_t)(128 * w + i * 16 + (lane >> 2)) * HD + h * 128 + 32 * q4 + (lane & 3) * 8);
    };
    auto tr_wo = [&](v4u (&t)[8], v4u* dst) {
#pragma unroll
      for (int i = 0; i < 8; ++i) *(LAS v4u*)(stg + (i * 32 + (lane >> 1)) * 32 + (lane & 1) * 16) = t[i];
      __builtin_amdgcn_s_waitcnt(0xC07F);
      asm volatile("" ::: "memory");
#pragma unroll
      for (int ks = 0; ks < 8; ++ks) {
        v2u lo = trread(stg + (32 * ks + 8 * g + q) * 32 + (4 * p) * 2);
        v2u hi = trread(stg + (32 * ks + 8 * g + 4 + q) * 32 + (4 * p) * 2);
        dst[ks] = v4u{lo.x, lo.y, hi.x, hi.y};
      }
      __builtin_amdgcn_s_waitcnt(0xC07F);
      asm volatile("" ::: "memory");
    };
    auto tr_w = [&](v4u (&t)[8], v4u* dst) {
#pragma unroll
      for (int i = 0; i < 8; ++i) *(LAS v4u*)(stg + (i * 16 + (lane >> 2)) * 64 + (lane & 3) * 16) = t[i];
      __builtin_amdgcn_s_waitcnt(0xC07F);
      asm volatile("" ::: "memory");
#pragma unroll
      for (int dt = 0; dt < 2; ++dt)
#pragma unroll
        for (int ks = 0; ks < 4; ++ks) {
          v2u lo = trread(stg + (32 * ks + 8 * g + q) * 64 + (16 * dt + 4 * p) * 2);
          v2u hi = trread(stg + (32 * ks + 8 * g + 4 + q) * 64 + (16 * dt + 4 * p) * 2);
          dst[dt * 4 + ks] = v4u{lo.x, lo.y, hi.x, hi.y};
        }
      __builtin_amdgcn_s_waitcnt(0xC07F);
      asm volatile("" ::: "memory");
    };
    v4u t0[8], t1[8], t2[8];
    ld_wo(0, t0);
    ld_wo(1, t1);
    ld_w(Wq, t2);
    tr_wo(t0, &wo[0]);
    ld_w(Wk, t0);
    tr_wo(t1, &wo[8]);
    ld_w(Wv, t1);
    tr_w(t2, &wq[0][0]);
    tr_w(t0, &wq[2][0]);
    tr_w(t1, t2);
#pragma unroll
    for (int i = 0; i < 8; ++i) wvl[i * 64] = t2[i];
  }
  const float scale = 0.08838834764831845f;
  const float L2E = 1.4426950408889634f;
  LAS f32x4* red = (LAS f32x4*)(lds + CH_RED);
  LAS f32x4* red2 = (LAS f32x4*)(lds + CH_RED2);
  LAS u16* vml = (LAS u16*)(lds + CH_VM);
  LAS v4u* kvl = (LAS v4u*)(lds + CH_KV) + lane;
  LAS unsigned* vcnt = (LAS unsigned*)(lds + CH_CNT);
  if (tid == 0) *vcnt = 0u;
  const __amdgpu_buffer_rsrc_t rsw = __builtin_amdgcn_make_buffer_rsrc((void*)ws, 0, (int)WS_TOTAL, 0x00020000);
  auto kv_prefetch = [&](int sn) {
    if ((sn & 1) == 0) wave_wait<false>(err + CW_KV + 16 * (sn >> 1), 64u, err);
    v4u t[10];
#pragma unroll
    for (int tt = 0; tt < 2; ++tt)
#pragma unroll
      for (int j = 0; j < 4; ++j) t[tt * 4 + j] = ld_sc1(rsw, (int)(WS_K + ((size_t)(32 * sn + 16 * tt + fr) * HD + h * 128 + 32 * j + 8 * g) * 2));
#pragma unroll
    for (int dt = 0; dt < 2; ++dt) t[8 + dt] = ld_sc1(rsw, (int)(WS_VT + ((((size_t)sn * NH + h) * 128 + 32 * q4 + 16 * dt + fr) * 32 + 8 * g) * 2));
#pragma unroll
    for (int i = 0; i < 10; ++i) kvl[((sn & 1) * 10 + i) * 64] = t[i];
  };
  if (w == 2) { wave_wait<false>(err + CW_KV + 16, 64u, err); kv_prefetch(0); }

#define STAMP(i)
  for (int s = 0; s < NSEG; ++s) {
    v4u mf[2];
    if (s == 0) {
#pragma unroll
      for (int p = 0; p < 2; ++p)
        mf[p] = *(const v4u*)(mem0 + (size_t)(fr & 7) * DM + 128 * w + 32 * (2 * p + (fr >> 3)) + 8 * g);
    } else {
      if (DLY1) __builtin_amdgcn_s_sleep(DLY1);
      poll<2>(rs, (int)(HOP_MEM + (size_t)s * 16384) + ((16 * w + 4 * (fr >> 3) + g) * 8 + (fr & 7)) * 16, mf, err);
    }
    STAMP(0)
    {
      f32x4 a0[4], a1[4];
#pragma unroll
      for (int t = 0; t < 4; ++t) {
        a0[t] = mfma16(wq[t][0], mf[0], f32x4{0.f, 0.f, 0.f, 0.f});
        a1[t] = mfma16(wq[t][1], mf[0], f32x4{0.f, 0.f, 0.f, 0.f});
      }
#pragma unroll
      for (int t = 0; t < 4; ++t) {
        a0[t] = mfma16(wq[t][2], mf[1], a0[t]);
        a1[t] = mfma16(wq[t][3], mf[1], a1[t]);
      }
#pragma unroll
      for (int t = 0; t < 4; ++t) {
        f32x4 x = (fr & 8) ? a1[t] : a0[t];
#pragma unroll
        for (int r = 0; r < 4; ++r) x[r] += row_ror<8>(x[r]);
        if (fr < 8) red[(w * 6 + t) * 32 + g * 8 + fr] = x;
      }
    }
    __syncthreads();
    STAMP(1)
    if (tid >= 384) {
      const int u = tid - 384;
      const int t6 = u >> 5, gg = (u >> 3) & 3, m = u & 7;
      f32x4 x = red[t6 * 32 + (u & 31)];
#pragma unroll
      for (int ww = 1; ww < 8; ++ww) x += red[(ww * 6 + t6) * 32 + (u & 31)];
      const unsigned lo = pack2(x[0], x[1]), hi = pack2(x[2], x[3]);
      const int mat = t6 >> 1, dt = t6 & 1;
      const int off = (int)(HOP_QK + ((size_t)s * NH + h) * 4096) + ((q4 * 2 + mat) * 8 + m) * 64 + (16 * dt + 4 * gg) * 2;
      __builtin_amdgcn_raw_buffer_store_b64(v2u{nosent(lo), nosent(hi)}, rs, off, 0, 16);
    }
    {
      f32x4 a0[2], a1[2];
#pragma unroll
      for (int t = 0; t < 2; ++t) {
        a0[t] = mfma16(wvl[(t * 4 + 0) * 64], mf[0], f32x4{0.f, 0.f, 0.f, 0.f});
        a1[t] = mfma16(wvl[(t * 4 + 1) * 64], mf[0], f32x4{0.f, 0.f, 0.f, 0.f});
      }
#pragma unroll
      for (int t = 0; t < 2; ++t) {
        a0[t] = mfma16(wvl[(t * 4 + 2) * 64], mf[1], a0[t]);
        a1[t] = mfma16(wvl[(t * 4 + 3) * 64], mf[1], a1[t]);
      }
#pragma unroll
      for (int t = 0; t < 2; ++t) {
        f32x4 x = (fr & 8) ? a1[t] : a0[t];
#pragma unroll
        for (int r = 0; r < 4; ++r) x[r] += row_ror<8>(x[r]);
        if (fr < 8) red[(w * 6 + 4 + t) * 32 + g * 8 + fr] = x;
      }
    }
    __builtin_amdgcn_s_waitcnt(0xC07F);
    asm volatile("" ::: "memory");
    if (lane == 0) __hip_atomic_fetch_add(vcnt, 1u, __ATOMIC_RELAXED, __HIP_MEMORY_SCOPE_WORKGROUP);
    STAMP(2)
    if (w == 1) {
      while (__hip_atomic_load(vcnt, __ATOMIC_RELAXED, __HIP_MEMORY_SCOPE_WORKGROUP) < 8u * (unsigned)(s + 1)) __builtin_amdgcn_s_sleep(1);
      asm volatile("" ::: "memory");
      const int dt = lane >> 5, gg = (lane >> 3) & 3, m = lane & 7;
      f32x4 x = red[(4 + dt) * 32 + (lane & 31)];
#pragma unroll
      for (int ww = 1; ww < 8; ++ww) x += red[(ww * 6 + 4 + dt) * 32 + (lane & 31)];
      const unsigned lo = pack2(x[0], x[1]), hi = pack2(x[2], x[3]);
      const int d = 16 * dt + 4 * gg;
      vml[(d + 0) * 8 + m] = (u16)(lo & 0xFFFF);
      vml[(d + 1) * 8 + m] = (u16)(lo >> 16);
      vml[(d + 2) * 8 + m] = (u16)(hi & 0xFFFF);
      vml[(d + 3) * 8 + m] = (u16)(hi >> 16);
      __builtin_amdgcn_s_waitcnt(0xC07F);
      asm volatile("" ::: "memory");
      if (lane < 32)
      {
        v4u t = *(LAS v4u*)(vml + lane * 8);
        t.x = nosent(t.x); t.y = nosent(t.y); t.z = nosent(t.z); t.w = nosent(t.w);
        __builtin_amdgcn_raw_buffer_store_b128(t, rs, (int)(HOP_VM + (((size_t)s * NH + h) * 128 + 32 * q4 + lane) * 16), 0, 16);
      }
    }
    if (s == NSEG - 1) break;
    if (w == 2 && s + 1 < NSEG - 1) kv_prefetch(s + 1);
    v4u pt, pm;
    if (w == 0) {
      v4u X[8];
      {
        const int base = (int)(HOP_QK + ((size_t)s * NH + h) * 4096) + (fr & 7) * 64 + g * 16;
        unsigned spins = 0;
        if (DLY2) __builtin_amdgcn_s_sleep(DLY2);
        for (;;) {
          unsigned mx = 0;
#pragma unroll
          for (int i = 0; i < 8; ++i) X[i] = ld_sc1(rs, base + i * 512);
          asm volatile("" ::: "memory");
#pragma unroll
          for (int i = 0; i < 8; ++i) mx = max(max(mx, max(X[i].x, X[i].y)), max(X[i].z, X[i].w));
          if (__all(mx != SENT)) break;
          if (++spins > SPINCAP) {
            if (lane == 0) __hip_atomic_store(err, 1u, __ATOMIC_RELAXED, __HIP_MEMORY_SCOPE_AGENT);
            break;
          }
          if ((spins & 1023u) == 0u && __hip_atomic_load(err, __ATOMIC_RELAXED, __HIP_MEMORY_SCOPE_AGENT) != 0u) break;
        }
      }
      STAMP(3)
      f32x4 s0 = {0.f, 0.f, 0.f, 0.f}, s1 = s0, s2 = s0;
#pragma unroll
      for (int j = 0; j < 4; ++j) {
        s1 = mfma16(kvl[((s & 1) * 10 + j) * 64], X[2 * j], s1);
        s2 = mfma16(kvl[((s & 1) * 10 + 4 + j) * 64], X[2 * j], s2);
        s0 = mfma16(X[2 * j + 1], X[2 * j], s0);
      }
      const bool up = fr >= 8;
      f32x4 st = up ? s2 : s1;
      const float m01 = up ? s0[1] : s0[0], m23 = up ? s0[3] : s0[2];
      const float sm = g >= 2 ? m23 : m01;
      unsigned p0 = pack2(st[0], st[1]), p1 = pack2(st[2], st[3]), p2 = pack2(sm, sm);
      p0 = pack2(bflo(p0) * scale, bfhi(p0) * scale);
      p1 = pack2(bflo(p1) * scale, bfhi(p1) * scale);
      p2 = pack2(bflo(p2) * scale, bflo(p2) * scale);
      float v0 = bflo(p0), v1 = bfhi(p0), v2 = bflo(p1), v3 = bfhi(p1), v4 = bflo(p2);
      float mx = fmaxf(fmaxf(fmaxf(v0, v1), fmaxf(v2, v3)), v4);
      mx = fmaxf(mx, row_ror<8>(mx));
      mx = fmaxf(mx, xor16(mx));
      mx = fmaxf(mx, xor32(mx));
      const float nm = -mx * L2E;
      v0 = __builtin_amdgcn_exp2f(__builtin_fmaf(v0, L2E, nm));
      v1 = __builtin_amdgcn_exp2f(__builtin_fmaf(v1, L2E, nm));
      v2 = __builtin_amdgcn_exp2f(__builtin_fmaf(v2, L2E, nm));
      v3 = __builtin_amdgcn_exp2f(__builtin_fmaf(v3, L2E, nm));
      v4 = __builtin_amdgcn_exp2f(__builtin_fmaf(v4, L2E, nm));
      float sum = ((v0 + v1) + (v2 + v3)) + v4;
      sum += row_ror<8>(sum);
      sum += xor16(sum);
      sum += xor32(sum);
      const float inv = __builtin_amdgcn_rcpf(sum);
      pt.x = pack2(v0 * inv, v1 * inv);
      pt.y = pack2(v2 * inv, v3 * inv);
      pt.z = (unsigned)__builtin_amdgcn_update_dpp(0, (int)pt.x, 0x128, 0xf, 0xf, false);
      pt.w = (unsigned)__builtin_amdgcn_update_dpp(0, (int)pt.y, 0x128, 0xf, 0xf, false);
      v4 *= inv;
      pm.x = pack2(v4, row_ror<8>(v4));
      pm.y = 0u; pm.z = 0u; pm.w = 0u;
    }
    __syncthreads();
    if (w == 0) {
      f32x4 o[2];
#pragma unroll
      for (int dt = 0; dt < 2; ++dt) o[dt] = mfma16(kvl[((s & 1) * 10 + 8 + dt) * 64], pt, f32x4{0.f, 0.f, 0.f, 0.f});
#pragma unroll
      for (int dt = 0; dt < 2; ++dt) {
        const unsigned t = *(LAS unsigned*)(vml + (16 * dt + fr) * 8 + 4 * (g & 1) + 2 * (g >> 1));
        o[dt] = mfma16(v4u{t, 0u, 0u, 0u}, pm, o[dt]);
      }
      if (fr < 8) {
#pragma unroll
        for (int dt = 0; dt < 2; ++dt) {
          const int k8 = h * 16 + 4 * q4 + 2 * dt + (g >> 1);
          const int off = (int)(HOP_OB + (size_t)s * 65536) + (((fr >> 2) * 512 + k8) * 4 + (fr & 3)) * 16 + (g & 1) * 8;
          __builtin_amdgcn_raw_buffer_store_b64(v2u{nosent(pack2(o[dt][0], o[dt][1])), nosent(pack2(o[dt][2], o[dt][3]))}, rs, off, 0, 16);
        }
      }
    }
    __syncthreads();
    STAMP(4)
    {
      v4u of[4];
      if (DLY3) __builtin_amdgcn_s_sleep(DLY3);
      poll<4>(rs, (int)(HOP_OB + (size_t)s * 65536) + ((mh * 512 + 64 * w + 4 * (fr >> 2) + g) * 4 + (fr & 3)) * 16, of, err);
      STAMP(5)
      f32x4 a[4];
#pragma unroll
      for (int r4 = 0; r4 < 4; ++r4) a[r4] = mfma16(wo[r4], of[0], f32x4{0.f, 0.f, 0.f, 0.f});
#pragma unroll
      for (int i = 1; i < 4; ++i)
#pragma unroll
        for (int r4 = 0; r4 < 4; ++r4) a[r4] = mfma16(wo[4 * i + r4], of[i], a[r4]);
      const int r4 = fr >> 2;
      f32x4 x = r4 == 0 ? a[0] : (r4 == 1 ? a[1] : (r4 == 2 ? a[2] : a[3]));
#pragma unroll
      for (int r = 0; r < 4; ++r) { x[r] += row_ror<8>(x[r]); x[r] += row_ror<4>(x[r]); }
      if (fr < 4) red2[w * 16 + g * 4 + fr] = x;
    }
    __syncthreads();
    if (tid < 16) {
      f32x4 x = red2[tid];
#pragma unroll
      for (int ww = 1; ww < 8; ++ww) x += red2[ww * 16 + tid];
      const int gg = tid >> 2, m4 = tid & 3;
      const int k8 = 2 * nb + (gg >> 1);
      const int off = (int)(HOP_MEM + (size_t)(s + 1) * 16384) + (k8 * 8 + 4 * mh + m4) * 16 + (gg & 1) * 8;
      __builtin_amdgcn_raw_buffer_store_b64(v2u{nosent(pack2(x[0], x[1])), nosent(pack2(x[2], x[3]))}, rs, off, 0, 16);
    }
    STAMP(6)
  }
  __syncthreads();
}

__device__ __forceinline__ void attn_unit(int s, int h, int lane, unsigned char* ws, LAS char* ostg) {
  const int fr = lane & 15, g = lane >> 4;
  const u16* Q = (const u16*)(ws + WS_Q);
  const u16* Kx = (const u16*)(ws + WS_K);
  const u16* Vt = (const u16*)(ws + WS_VT);
  const __amdgpu_buffer_rsrc_t rs = __builtin_amdgcn_make_buffer_rsrc((void*)(ws + WS_HOP), 0, (int)HOP_BYTES, 0x00020000);
  unsigned* err = (unsigned*)(ws + WS_ERR);
  const float scale = 0.08838834764831845f;
  f32x4 sc[3][2];
#pragma unroll
  for (int a = 0; a < 3; ++a)
#pragma unroll
    for (int b = 0; b < 2; ++b) sc[a][b] = f32x4{0.f, 0.f, 0.f, 0.f};
  v4u qf[4][2], vtf[8];
  {
    v4u kf[4][2];
#pragma unroll
    for (int j = 0; j < 4; ++j)
#pragma unroll
      for (int t = 0; t < 2; ++t) {
        qf[j][t] = *(const v4u*)(Q + (size_t)(32 * s + 16 * t + fr) * HD + h * 128 + 32 * j + 8 * g);
        kf[j][t] = *(const v4u*)(Kx + (size_t)(32 * s + 16 * t + fr) * HD + h * 128 + 32 * j + 8 * g);
      }
#pragma unroll
    for (int dt = 0; dt < 8; ++dt) vtf[dt] = *(const v4u*)(Vt + (((size_t)s * NH + h) * 128 + 16 * dt + fr) * 32 + 8 * g);
#pragma unroll
    for (int j = 0; j < 4; ++j)
#pragma unroll
      for (int a = 0; a < 2; ++a)
#pragma unroll
        for (int b = 0; b < 2; ++b) sc[1 + a][b] = mfma16(kf[j][a], qf[j][b], sc[1 + a][b]);
  }
  v4u km[4];
  v4u vmr[2];
  {
    const int o1 = (int)(HOP_QK + ((size_t)s * NH + h) * 4096) + 512 + (fr & 7) * 64 + g * 16;
    const int o2 = (int)(HOP_VM + ((size_t)s * NH + h) * 2048) + lane * 16;
    unsigned spins = 0;
    for (;;) {
      unsigned mx = 0;
#pragma unroll
      for (int i = 0; i < 4; ++i) km[i] = ld_sc1(rs, o1 + i * 1024);
#pragma unroll
      for (int i = 0; i < 2; ++i) vmr[i] = ld_sc1(rs, o2 + i * 1024);
      asm volatile("" ::: "memory");
#pragma unroll
      for (int i = 0; i < 4; ++i) mx = max(max(mx, max(km[i].x, km[i].y)), max(km[i].z, km[i].w));
#pragma unroll
      for (int i = 0; i < 2; ++i) mx = max(max(mx, max(vmr[i].x, vmr[i].y)), max(vmr[i].z, vmr[i].w));
      if (__all(mx != SENT)) break;
      __builtin_amdgcn_s_sleep(16);
      if (++spins > SPINCAP) {
        if (lane == 0) __hip_atomic_store(err, 1u, __ATOMIC_RELAXED, __HIP_MEMORY_SCOPE_AGENT);
        break;
      }
      if ((spins & 1023u) == 0u && __hip_atomic_load(err, __ATOMIC_RELAXED, __HIP_MEMORY_SCOPE_AGENT) != 0u) break;
    }
  }
#pragma unroll
  for (int j = 0; j < 4; ++j)
#pragma unroll
    for (int b = 0; b < 2; ++b) sc[0][b] = mfma16(km[j], qf[j][b], sc[0][b]);
  v4u pt[2], pm[2];
#pragma unroll
  for (int b = 0; b < 2; ++b) {
    const int qi = 16 * b + fr;
    float v[12];
    bool ok[12];
#pragma unroll
    for (int r = 0; r < 4; ++r) {
      v[r] = rbf(rbf(sc[0][b][r]) * scale); ok[r] = g < 2;
      v[4 + r] = rbf(rbf(sc[1][b][r]) * scale); ok[4 + r] = (4 * g + r) <= qi;
      v[8 + r] = rbf(rbf(sc[2][b][r]) * scale); ok[8 + r] = (16 + 4 * g + r) <= qi;
    }
    float mx = -3.0e38f;
#pragma unroll
    for (int i = 0; i < 12; ++i) mx = ok[i] ? fmaxf(mx, v[i]) : mx;
    mx = fmaxf(mx, __shfl_xor(mx, 16));
    mx = fmaxf(mx, __shfl_xor(mx, 32));
    float sum = 0.f;
#pragma unroll
    for (int i = 0; i < 12; ++i) { v[i] = ok[i] ? __expf(v[i] - mx) : 0.f; sum += v[i]; }
    sum += __shfl_xor(sum, 16);
    sum += __shfl_xor(sum, 32);
    const float inv = 1.0f / sum;
    pt[b].x = pack2(v[4] * inv, v[5] * inv); pt[b].y = pack2(v[6] * inv, v[7] * inv);
    pt[b].z = pack2(v[8] * inv, v[9] * inv); pt[b].w = pack2(v[10] * inv, v[11] * inv);
    pm[b].x = pack2(v[0] * inv, v[1] * inv); pm[b].y = pack2(v[2] * inv, v[3] * inv); pm[b].z = 0u; pm[b].w = 0u;
  }
#pragma unroll
  for (int dt = 0; dt < 8; ++dt) {
    v4u vmf;
    {
      const int src = (16 * dt + fr) & 63;
      const v4u vv = vmr[dt >> 2];
      const unsigned a0 = __shfl(vv.x, src), a1 = __shfl(vv.y, src), a2 = __shfl(vv.z, src), a3 = __shfl(vv.w, src);
      vmf.x = g == 0 ? a0 : (g == 1 ? a2 : 0u);
      vmf.y = g == 0 ? a1 : (g == 1 ? a3 : 0u);
      vmf.z = 0u; vmf.w = 0u;
    }
#pragma unroll
    for (int b = 0; b < 2; ++b) {
      f32x4 o = {0.f, 0.f, 0.f, 0.f};
      o = mfma16(vtf[dt], pt[b], o);
      o = mfma16(vmf, pm[b], o);
      *(LAS v2u*)(ostg + (16 * b + fr) * 272 + (16 * dt + 4 * g) * 2) = v2u{pack2(o[0], o[1]), pack2(o[2], o[3])};
    }
  }
  __builtin_amdgcn_s_waitcnt(0xC07F);
  asm volatile("" ::: "memory");
  {
    const __amdgpu_buffer_rsrc_t rsw = __builtin_amdgcn_make_buffer_rsrc((void*)ws, 0, (int)WS_TOTAL, 0x00020000);
#pragma unroll
    for (int i = 0; i < 8; ++i) {
      const int row = 4 * i + g;
      const v4u v = *(LAS v4u*)(ostg + row * 272 + fr * 16);
      __builtin_amdgcn_raw_buffer_store_b128(v, rsw, (int)(WS_OT + ((size_t)(32 * s + row) * HD + h * 128) * 2 + fr * 16), 0, 16);
    }
  }
  __builtin_amdgcn_s_waitcnt(0xC07F);
}
__device__ __forceinline__ void wg_signal(unsigned* ctr, int n) {
  asm volatile("s_waitcnt vmcnt(0)" ::: "memory");
  __syncthreads();
  if (threadIdx.x < n) __hip_atomic_fetch_add(ctr + 16 * threadIdx.x, 1u, __ATOMIC_RELAXED, __HIP_MEMORY_SCOPE_AGENT);
}
__device__ __forceinline__ void wg_wait(unsigned* ctr, unsigned want, unsigned* err) {
  if (threadIdx.x < 64) wave_wait(ctr, want, err);
  __syncthreads();
}

__device__ __forceinline__ void wait_phase_abc(unsigned* cw) {
  unsigned* err = cw + CW_ERR;
  if (threadIdx.x < 64) {
    const int l = threadIdx.x;
    unsigned* p = l < 32 ? cw + CW_KV + 16 * l : (l == 32 ? cw + CW_Q : cw + CW_MLP2);
    const unsigned want = l < 32 ? 64u : (l < 34 ? 128u : 0u);
    unsigned spins = 0;
    while (!__all(__hip_atomic_load(p, __ATOMIC_RELAXED, __HIP_MEMORY_SCOPE_AGENT) >= want)) {
      __builtin_amdgcn_s_sleep(8);
      if (++spins > SPINCAP) { __hip_atomic_store(err, 1u, __ATOMIC_RELAXED, __HIP_MEMORY_SCOPE_AGENT); break; }
      if ((spins & 1023u) == 0u && __hip_atomic_load(err, __ATOMIC_RELAXED, __HIP_MEMORY_SCOPE_AGENT) != 0u) break;
    }
    __builtin_amdgcn_fence(__ATOMIC_ACQUIRE, "agent");
    asm volatile("s_waitcnt vmcnt(0)" ::: "memory");
  }
  __syncthreads();
}
#define do_attn(sg_, hg_, ci_) { attn_unit((sg_), (hg_) * 8 + (threadIdx.x >> 6), threadIdx.x & 63, ws, lds + (threadIdx.x >> 6) * 8704); wg_signal(cw + CW_AT + 16 * (ci_), 1); }

template <int NS>
__device__ __forceinline__ void tail_unit(int u, int idx, const u16* x, const u16* Wo, u16* out, unsigned char* ws, LAS char* lds) {
  unsigned* cw = (unsigned*)(ws + WS_ERR);
  unsigned* err = cw + CW_ERR;
  constexpr int KS = HD / NS, RR = 64 / NS;
  const int ct = idx / NS, ks = idx % NS;
  const __amdgpu_buffer_rsrc_t rsw = __builtin_amdgcn_make_buffer_rsrc((void*)ws, 0, (int)WS_TOTAL, 0x00020000);
#define TSTAMP(i)
  TSTAMP(0)
  TSTAMP(1)
  wg_wait(cw + CW_AT + 16 * u, 8u, err);
  TSTAMP(2)
  const int slot = (u - TAIL_U0) * 8 + ct;
  const size_t sbase = WS_SLAB + (size_t)slot * 16 * 32768;
  gemm_tile<64, 2, 4, false>((const u16*)(ws + WS_OT) + ks * KS, HD, Wo + (size_t)ks * KS * DM, DM, KS, u * 64, ct * 128, lds,
                             [&](int r, int c, f32x4 v) {
                               __builtin_amdgcn_raw_buffer_store_b128(__builtin_bit_cast(v4u, v), rsw,
                                   (int)(sbase + (size_t)ks * 32768 + (((r & 63) * 128 + (c & 127)) * 4)), 0, 16);
                             });
  TSTAMP(3)
  wg_signal(cw + CW_SK + 16 * slot, 1);
  TSTAMP(4)
  if (threadIdx.x < 64) wave_wait<false>(cw + CW_SK + 16 * slot, (unsigned)NS, err);
  __syncthreads();
  TSTAMP(5)
  if (threadIdx.x < RR * 32) {
    const int rr = RR * ks + (threadIdx.x >> 5), cc = (threadIdx.x & 31) * 4;
    v4u pv[NS];
#pragma unroll
    for (int k2 = 0; k2 < NS; ++k2) pv[k2] = ld_sc1(rsw, (int)(sbase + (size_t)k2 * 32768 + (rr * 128 + cc) * 4));
    f32x4 v = __builtin_bit_cast(f32x4, pv[0]);
#pragma unroll
    for (int k2 = 1; k2 < NS; ++k2) v += __builtin_bit_cast(f32x4, pv[k2]);
    const int r = u * 64 + rr, c = ct * 128 + cc;
    const v2u xx = *(const v2u*)(x + (size_t)r * DM + c);
    const v2u bb = *(const v2u*)((const u16*)(ws + WS_BO) + (size_t)r * DM + c);
    const float o0 = rbf(bflo(xx.x) + rbf(v[0])) + bflo(bb.x), o1 = rbf(bfhi(xx.x) + rbf(v[1])) + bfhi(bb.x);
    const float o2 = rbf(bflo(xx.y) + rbf(v[2])) + bflo(bb.y), o3 = rbf(bfhi(xx.y) + rbf(v[3])) + bfhi(bb.y);
    *(v2u*)(out + (size_t)r * DM + c) = v2u{pack2(o0, o1), pack2(o2, o3)};
  }
}

__global__ __launch_bounds__(512, 2) void k_mega(const u16* x, const u16* mem0, const u16* Wq, const u16* Wk, const u16* Wv, const u16* Wo,
                                                 const u16* w1, const u16* b1, const u16* w2, const u16* b2, u16* out, unsigned char* ws) {
  extern __shared__ __attribute__((aligned(16))) char smem[];
  LAS char* lds = (LAS char*)smem;
  const int b = blockIdx.x;
#define ASTAMP(i)
  ASTAMP(0)
  const int xcd = b & 7, i = b >> 4;
  unsigned* cw = (unsigned*)(ws + WS_ERR);
  unsigned* err = cw + CW_ERR;
  if (((b >> 3) & 1) == 0) {
    const int c = ((xcd * 4 + (i >> 2)) << 2) | (i & 3);
    chain_role(c, mem0, Wq, Wk, Wv, Wo, ws, lds);
    ASTAMP(6)
    wait_phase_abc(cw);
    tail_unit<16>(31, xcd * 16 + i, x, Wo, out, ws, lds);
    ASTAMP(7)
    return;
  }
  const int gi = i * 8 + xcd;
  {
    const __amdgpu_buffer_rsrc_t rsh = __builtin_amdgcn_make_buffer_rsrc((void*)(ws + WS_HOP), 0, (int)HOP_BYTES, 0x00020000);
    constexpr int PER = (int)(HOP_BYTES / 128);
    static_assert(HOP_BYTES % (128 * 8192) == 0, "fill split");
    const v4u sv = {SENT, SENT, SENT, SENT};
    for (int o = threadIdx.x * 16; o < PER; o += 8192) __builtin_amdgcn_raw_buffer_store_b128(sv, rsh, gi * PER + o, 0, 16);
  }
  task_qkv<64, 2, 4>(gi >> 6, 32 + (gi & 63), Wq, Wk, Wv, ws, lds);
  wg_signal(cw + CW_KV + 16 * (gi >> 6), 1);
  for (int t = gi; t < 64 + 7 * 64; t += 128) {
    if (t < 64) {
      task_qkv<128, 2, 4>(1, 32 + t, Wq, Wk, Wv, ws, lds);
      wg_signal(cw + CW_KV + 16 * 2, 2);
    } else {
      const int rt = 1 + ((t - 64) >> 6);
      task_qkv<256, 4, 2>(rt, 32 + ((t - 64) & 63), Wq, Wk, Wv, ws, lds);
      wg_signal(cw + CW_KV + 16 * 4 * rt, 4);
    }
  }
  ASTAMP(1)
  for (int t = gi; t < 8 * 28; t += 128) task_mlp1<256, 4, 2>(t & 7, t >> 3, w1, b1, ws, lds);
  wg_signal(cw + CW_MLP1, 1);
  for (int t = gi; t < 8 * 32; t += 128) task_qkv<256, 4, 2>(t & 7, t >> 3, Wq, Wk, Wv, ws, lds);
  wg_signal(cw + CW_Q, 1);
  ASTAMP(2)
  wg_wait(cw + CW_MLP1, 128u, err);
  task_mlp2<128, 2, 4>(gi >> 3, gi & 7, w2, b2, ws, lds);
  wg_signal(cw + CW_MLP2, 1);
  ASTAMP(3)
  wait_phase_abc(cw);
  for (int t = gi; t < 160; t += 128) do_attn(t >> 2, t & 3, (t >> 4) * 2)
  if (gi >= 96) {
    for (int t = 160 + (gi - 96); t < 256; t += 32) do_attn(t >> 2, t & 3, t < 192 ? (t >> 4) * 2 : (t >> 3))
  } else {
    {
      const int pr = gi >> 3;
      wg_wait(cw + CW_AT + 16 * 2 * pr, 16u, err);
      task_wo<128, 2, 4>(pr, gi & 7, Wo, x, out, ws, lds);
    }
    if (gi < 64) {
      for (int u = TAIL_U0; u < 31; u += 2) tail_unit<8>(u, (gi & 7) * 8 + (gi >> 3), x, Wo, out, ws, lds);
    } else {
      for (int u = TAIL_U0 + 1; u < 31; u += 2) tail_unit<4>(u, (gi & 7) * 4 + ((gi - 64) >> 3), x, Wo, out, ws, lds);
    }
  }
  ASTAMP(7)
}

extern "C" void kernel_launch(void* const* d_in, const int* in_sizes, int n_in, void* d_out, int out_size, void* d_ws, size_t ws_size,
                              hipStream_t stream) {
  const u16* x = (const u16*)d_in[0];
  const u16* mem0 = (const u16*)d_in[1];
  const u16* Wq = (const u16*)d_in[2];
  const u16* Wk = (const u16*)d_in[3];
  const u16* Wv = (const u16*)d_in[4];
  const u16* Wo = (const u16*)d_in[5];
  const u16* w1 = (const u16*)d_in[8];
  const u16* b1 = (const u16*)d_in[9];
  const u16* w2 = (const u16*)d_in[10];
  const u16* b2 = (const u16*)d_in[11];
  unsigned char* ws = (unsigned char*)d_ws;
  u16* out = (u16*)d_out;
  constexpr int L256 = GemmCfg<256>::LDS_BYTES;
  constexpr int LMEGA = L256 > CH_LDS ? L256 : CH_LDS;
  (void)hipFuncSetAttribute((const void*)k_mega, hipFuncAttributeMaxDynamicSharedMemorySize, LMEGA);
  k_ln<<<S_LEN / 4, 256, 0, stream>>>(x, ws);
  k_mega<<<256, 512, LMEGA, stream>>>(x, mem0, Wq, Wk, Wv, Wo, w1, b1, w2, b2, out, ws);
}
```

```cpp
#include <hip/hip_runtime.h>
#include <hip/hip_bf16.h>
#include <stdint.h>

constexpr int S_LEN = 2048, DM = 1024, NH = 32, DH = 128, HD = NH * DH;
constexpr int MEMN = 8, SEG = 32, NSEG = S_LEN / SEG;
constexpr int FF = 3584;

typedef unsigned v4u __attribute__((ext_vector_type(4)));
typedef unsigned v2u __attribute__((ext_vector_type(2)));
typedef float f32x4 __attribute__((ext_vector_type(4)));
typedef float f32x2 __attribute__((ext_vector_type(2)));
typedef short bf16x8 __attribute__((ext_vector_type(8)));
typedef short s16x4 __attribute__((ext_vector_type(4)));
typedef __bf16 bfx2 __attribute__((ext_vector_type(2)));
typedef unsigned short u16;
#define LAS __attribute__((address_space(3)))
#define GAS __attribute__((address_space(1)))

constexpr size_t WS_XN = 0;
constexpr size_t WS_Y = WS_XN + (size_t)S_LEN * DM * 2;
constexpr size_t WS_Q = WS_Y + (size_t)S_LEN * DM * 2;
constexpr size_t WS_K = WS_Q + (size_t)S_LEN * HD * 2;
constexpr size_t WS_VT = WS_K + (size_t)S_LEN * HD * 2;
constexpr size_t WS_H = WS_VT + (size_t)S_LEN * HD * 2;
constexpr size_t WS_BO = WS_H + (size_t)S_LEN * FF * 2;
constexpr size_t WS_OT = WS_BO + (size_t)S_LEN * DM * 2;
constexpr size_t WS_SLAB = WS_OT + (size_t)S_LEN * HD * 2;
constexpr int TAIL_U0 = 24;
constexpr size_t WS_HOP = WS_SLAB + (size_t)64 * 16 * 64 * 128 * 4;
constexpr size_t HOP_MEM = 0;
constexpr size_t HOP_QK = HOP_MEM + (size_t)NSEG * 16384;
constexpr size_t HOP_OB = HOP_QK + (size_t)NSEG * NH * 4096;
constexpr size_t HOP_VM = HOP_OB + (size_t)NSEG * 65536;
constexpr size_t HOP_BYTES = HOP_VM + (size_t)NSEG * HD * MEMN * 2;
constexpr size_t WS_ERR = WS_HOP + HOP_BYTES;
constexpr size_t WS_TOTAL = WS_ERR + 16384;
constexpr int CW_ERR = 0, CW_MLP1 = 16, CW_Q = 32, CW_MLP2 = 48, CW_KV = 64  , CW_AT = 64 + 512  , CW_SK = 64 + 1024  ;

#define PF_POS 0
#define DLY1 15
#define DLY2 2
#define DLY3 14
#define DLYR 0
constexpr unsigned SENT = 0xFFFFFFFFu;
constexpr unsigned SPINCAP = 1u << 20;

__device__ __forceinline__ float bf2f(unsigned b16) { return __builtin_bit_cast(float, b16 << 16); }
__device__ __forceinline__ float bflo(unsigned x) { return __builtin_bit_cast(float, x << 16); }
__device__ __forceinline__ float bfhi(unsigned x) { return __builtin_bit_cast(float, x & 0xFFFF0000u); }
__device__ __forceinline__ unsigned pack2(float a, float b) {
  f32x2 v = {a, b};
  bfx2 r = __builtin_convertvector(v, bfx2);
  return __builtin_bit_cast(unsigned, r);
}
__device__ __forceinline__ float rbf(float a) { return (float)(__bf16)a; }
__device__ __forceinline__ unsigned nosent(unsigned x) { return x == SENT ? 0xFFFFFFFEu : x; }
__device__ __forceinline__ f32x4 mfma16(v4u a, v4u b, f32x4 c) {
  return __builtin_amdgcn_mfma_f32_16x16x32_bf16(__builtin_bit_cast(bf16x8, a), __builtin_bit_cast(bf16x8, b), c, 0, 0, 0);
}
__device__ __forceinline__ v2u trread(LAS const char* p) {
  typedef short v4i16_t __attribute__((ext_vector_type(4)));
  return __builtin_bit_cast(v2u, __builtin_amdgcn_ds_read_tr16_b64_v4i16((LAS v4i16_t*)p));
}
__device__ __forceinline__ float ror(float x, int) = delete;
template <int N> __device__ __forceinline__ float row_ror(float x) {
  return __builtin_bit_cast(float, __builtin_amdgcn_update_dpp(0, __builtin_bit_cast(int, x), 0x120 + N, 0xf, 0xf, false));
}

__device__ __forceinline__ float wave_sum(float v) {
#pragma unroll
  for (int o = 32; o > 0; o >>= 1) v += __shfl_xor(v, o);
  return v;
}
__device__ __forceinline__ void ln16(float (&v)[16]) {
  float s = 0.f;
#pragma unroll
  for (int i = 0; i < 16; ++i) s += v[i];
  s = wave_sum(s);
  const float mean = s * (1.0f / DM);
  float q = 0.f;
#pragma unroll
  for (int i = 0; i < 16; ++i) { float d = v[i] - mean; q += d * d; }
  q = wave_sum(q);
  const float mb = rbf(mean);
  const float vb = rbf(q * (1.0f / DM));
  const float den = rbf(sqrtf(rbf(vb + 1e-5f)));
#pragma unroll
  for (int i = 0; i < 16; ++i) v[i] = rbf(rbf(v[i] - mb) / den);
}
__global__ __launch_bounds__(256) void k_ln(const u16* __restrict__ x, unsigned char* __restrict__ ws) {
  const int tid = threadIdx.x, lane = tid & 63, w = tid >> 6;
  const int row = blockIdx.x * 4 + w;
  if (blockIdx.x < 16) ((unsigned*)(ws + WS_ERR))[blockIdx.x * 256 + tid] = 0u;
  {
    v4u sv = {SENT, SENT, SENT, SENT};
    v4u* p = (v4u*)(ws + WS_HOP);
    const size_t n = HOP_BYTES / 16;
    for (size_t i = (size_t)blockIdx.x * 256 + tid; i < n; i += (size_t)512 * 256) p[i] = sv;
  }
  const v4u* xr = (const v4u*)(x + (size_t)row * DM);
  v4u a = xr[lane], b = xr[64 + lane];
  float v[16];
  v[0] = bflo(a.x); v[1] = bfhi(a.x); v[2] = bflo(a.y); v[3] = bfhi(a.y);
  v[4] = bflo(a.z); v[5] = bfhi(a.z); v[6] = bflo(a.w); v[7] = bfhi(a.w);
  v[8] = bflo(b.x); v[9] = bfhi(b.x); v[10] = bflo(b.y); v[11] = bfhi(b.y);
  v[12] = bflo(b.z); v[13] = bfhi(b.z); v[14] = bflo(b.w); v[15] = bfhi(b.w);
  ln16(v);
  v4u* xn = (v4u*)(ws + WS_XN + (size_t)row * DM * 2);
  xn[lane] = v4u{pack2(v[0], v[1]), pack2(v[2], v[3]), pack2(v[4], v[5]), pack2(v[6], v[7])};
  xn[64 + lane] = v4u{pack2(v[8], v[9]), pack2(v[10], v[11]), pack2(v[12], v[13]), pack2(v[14], v[15])};
  ln16(v);
  v4u* y = (v4u*)(ws + WS_Y + (size_t)row * DM * 2);
  y[lane] = v4u{pack2(v[0], v[1]), pack2(v[2], v[3]), pack2(v[4], v[5]), pack2(v[6], v[7])};
  y[64 + lane] = v4u{pack2(v[8], v[9]), pack2(v[10], v[11]), pack2(v[12], v[13]), pack2(v[14], v[15])};
}

template <int BM> struct GemmCfg {
  static constexpr int BN = 128, BK = 64;
  static constexpr int A_BYTES = BM * BK * 2, B_BYTES = BK * BN * 2, ST_BYTES = A_BYTES + B_BYTES;
  static constexpr int NST = BM == 256 ? 3 : (BM == 128 ? 4 : 6);
  static constexpr int LDS_BYTES = NST * ST_BYTES;
};

template <int N> __device__ __forceinline__ void wait_vm() { asm volatile("s_waitcnt vmcnt(%0)" ::"n"(N) : "memory"); }

template <int BM, int WM, int WN, bool TOUT, class Epi>
__device__ __forceinline__ void gemm_tile(const u16* __restrict__ A, int lda, const u16* __restrict__ B, int ldb, int K,
                                          int brow, int bcol, LAS char* lds, Epi epi) {
  using C = GemmCfg<BM>;
  constexpr int TM = BM / WM / 16, TN = C::BN / WN / 16;
  constexpr int AI = C::A_BYTES / 8192, BI = C::B_BYTES / 8192;
  const int tid = threadIdx.x, lane = tid & 63, w = __builtin_amdgcn_readfirstlane(tid >> 6);
  const int wr = w / WN, wc = w % WN, fr = lane & 15, fq = lane >> 4;
  const u16* asrc[AI];
  const u16* bsrc[BI];
#pragma unroll
  for (int i = 0; i < AI; ++i) {
    const int ii = w * AI + i, row = ii * 8 + (lane >> 3), ch = (lane & 7) ^ (row & 7);
    asrc[i] = A + (size_t)(brow + row) * lda + ch * 8;
  }
#pragma unroll
  for (int i = 0; i < BI; ++i) {
    const int ii = w * BI + i, row = ii * 4 + (lane >> 4), ch = (lane & 15) ^ (((row & 3) << 2) | ((row >> 2) & 3));
    bsrc[i] = B + (size_t)row * ldb + bcol + ch * 8;
  }
  auto stage = [&](int t, int buf) {
    LAS char* base = lds + buf * C::ST_BYTES;
#pragma unroll
    for (int i = 0; i < AI; ++i)
      __builtin_amdgcn_global_load_lds((const GAS void*)(asrc[i] + t * 64), (LAS void*)(base + (w * AI + i) * 1024), 16, 0, 0);
#pragma unroll
    for (int i = 0; i < BI; ++i)
      __builtin_amdgcn_global_load_lds((const GAS void*)(bsrc[i] + (size_t)t * 64 * ldb), (LAS void*)(base + C::A_BYTES + (w * BI + i) * 1024), 16, 0, 0);
  };
  int aoff[TM][2], boff[TN][2][2];
#pragma unroll
  for (int m = 0; m < TM; ++m)
#pragma unroll
    for (int k = 0; k < 2; ++k) {
      const int row = wr * (TM * 16) + m * 16 + fr, ch = k * 4 + fq;
      aoff[m][k] = row * 128 + ((ch ^ (row & 7)) * 16);
    }
  {
    const int q = fr >> 2, p = lane & 3;
#pragma unroll
    for (int n = 0; n < TN; ++n)
#pragma unroll
      for (int k = 0; k < 2; ++k)
#pragma unroll
        for (int t = 0; t < 2; ++t) {
          const int row = 32 * k + 8 * fq + 4 * t + q, c = wc * TN + n, ch = 2 * c + (p >> 1);
          boff[n][k][t] = C::A_BYTES + 256 * row + 16 * (ch ^ (((row & 3) << 2) | ((row >> 2) & 3))) + 8 * (p & 1);
        }
  }
  f32x4 acc[TM][TN];
#pragma unroll
  for (int m = 0; m < TM; ++m)
#pragma unroll
    for (int n = 0; n < TN; ++n) acc[m][n] = f32x4{0.f, 0.f, 0.f, 0.f};
  const int nt = K / 64;
  constexpr int NST = C::NST, PER = AI + BI;
#pragma unroll
  for (int i = 0; i < NST - 1; ++i)
    if (i < nt) stage(i, i);
  int rb = 0, sb = NST - 1;
  for (int t = 0; t < nt; ++t) {
    {
      const int rem = nt - 1 - t;
      if (rem >= NST - 2) wait_vm<(NST - 2) * PER>();
      else if (NST > 5 && rem == 3) wait_vm<3 * PER>();
      else if (NST > 4 && rem == 2) wait_vm<2 * PER>();
      else if (NST > 3 && rem == 1) wait_vm<PER>();
      else wait_vm<0>();
    }
    asm volatile("s_waitcnt lgkmcnt(0)" ::: "memory");
    __builtin_amdgcn_s_barrier();
    asm volatile("" ::: "memory");
    if (t + NST - 1 < nt) stage(t + NST - 1, sb);
    LAS const char* base = lds + rb * C::ST_BYTES;
    v4u af[TM][2], bfr[TN][2];
#pragma unroll
    for (int k = 0; k < 2; ++k) {
#pragma unroll
      for (int n = 0; n < TN; ++n) {
        v2u lo = trread(base + boff[n][k][0]), hi = trread(base + boff[n][k][1]);
        bfr[n][k] = v4u{lo.x, lo.y, hi.x, hi.y};
      }
#pragma unroll
      for (int m = 0; m < TM; ++m) af[m][k] = *(LAS const v4u*)(base + aoff[m][k]);
    }
    __builtin_amdgcn_s_setprio(1);
#pragma unroll
    for (int k = 0; k < 2; ++k)
#pragma unroll
      for (int m = 0; m < TM; ++m)
#pragma unroll
        for (int n = 0; n < TN; ++n)
          acc[m][n] = TOUT ? mfma16(af[m][k], bfr[n][k], acc[m][n]) : mfma16(bfr[n][k], af[m][k], acc[m][n]);
    __builtin_amdgcn_s_setprio(0);
    rb = rb == NST - 1 ? 0 : rb + 1;
    sb = sb == NST - 1 ? 0 : sb + 1;
  }
#pragma unroll
  for (int m = 0; m < TM; ++m)
#pragma unroll
    for (int n = 0; n < TN; ++n) {
      const int r0 = brow + wr * (TM * 16) + m * 16, c0 = bcol + wc * (TN * 16) + n * 16;
      if (TOUT) epi(r0 + fq * 4, c0 + fr, acc[m][n]);
      else epi(r0 + fr, c0 + fq * 4, acc[m][n]);
    }
  asm volatile("s_waitcnt lgkmcnt(0)" ::: "memory");
  __builtin_amdgcn_s_barrier();
}

__device__ __forceinline__ void wt8(unsigned char* ws, size_t off, v2u v) {
  const __amdgpu_buffer_rsrc_t rs = __builtin_amdgcn_make_buffer_rsrc((void*)ws, 0, (int)WS_TOTAL, 0x00020000);
  __builtin_amdgcn_raw_buffer_store_b64(v, rs, (int)off, 0, 16);
}
__device__ __forceinline__ float gelu_tanh(float x) {
  const float u = 0.7978845608028654f * (x + 0.044715f * x * x * x);
  const float e = __expf(2.0f * u);
  const float t = 1.0f - 2.0f / (e + 1.0f);
  return 0.5f * x * (1.0f + t);
}

template <int BM, int WM, int WN>
__device__ __forceinline__ void task_qkv(int rt, int ct, const u16* Wq, const u16* Wk, const u16* Wv, unsigned char* ws, LAS char* lds) {
  const u16* xn = (const u16*)(ws + WS_XN);
  const int which = ct >> 5, bcol = (ct & 31) * 128, brow = rt * BM;
  if (which < 2) {
    const size_t ob = which == 0 ? WS_Q : WS_K;
    gemm_tile<BM, WM, WN, false>(xn, DM, which == 0 ? Wq : Wk, HD, DM, brow, bcol, lds, [&](int r, int c, f32x4 v) {
      wt8(ws, ob + ((size_t)r * HD + c) * 2, v2u{pack2(v[0], v[1]), pack2(v[2], v[3])});
    });
  } else {
    gemm_tile<BM, WM, WN, true>(xn, DM, Wv, HD, DM, brow, bcol, lds, [&](int r, int c, f32x4 v) {
      const int seg = r >> 5, t = r & 31, p = 8 * ((t & 15) >> 2) + 4 * (t >> 4);
      wt8(ws, WS_VT + (((size_t)seg * HD + c) * 32 + p) * 2, v2u{pack2(v[0], v[1]), pack2(v[2], v[3])});
    });
  }
}
template <int BM, int WM, int WN>
__device__ __forceinline__ void task_mlp1(int rt, int ct, const u16* w1, const u16* b1, unsigned char* ws, LAS char* lds) {
  const u16* y = (const u16*)(ws + WS_Y);
  gemm_tile<BM, WM, WN, false>(y, DM, w1, FF, DM, rt * BM, ct * 128, lds, [&](int r, int c, f32x4 v) {
    const v2u bb = *(const v2u*)(b1 + c);
    const float o0 = gelu_tanh(rbf(rbf(v[0]) + bflo(bb.x))), o1 = gelu_tanh(rbf(rbf(v[1]) + bfhi(bb.x)));
    const float o2 = gelu_tanh(rbf(rbf(v[2]) + bflo(bb.y))), o3 = gelu_tanh(rbf(rbf(v[3]) + bfhi(bb.y)));
    wt8(ws, WS_H + ((size_t)r * FF + c) * 2, v2u{pack2(o0, o1), pack2(o2, o3)});
  });
}
template <int BM, int WM, int WN>
__device__ __forceinline__ void task_mlp2(int rt, int ct, const u16* w2, const u16* b2, unsigned char* ws, LAS char* lds) {
  const u16* h = (const u16*)(ws + WS_H);
  gemm_tile<BM, WM, WN, false>(h, FF, w2, DM, FF, rt * BM, ct * 128, lds, [&](int r, int c, f32x4 v) {
    const v2u bb = *(const v2u*)(b2 + c);
    wt8(ws, WS_BO + ((size_t)r * DM + c) * 2, v2u{pack2(rbf(v[0]) + bflo(bb.x), rbf(v[1]) + bfhi(bb.x)), pack2(rbf(v[2]) + bflo(bb.y), rbf(v[3]) + bfhi(bb.y))});
  });
}
template <int BM, int WM, int WN>
__device__ __forceinline__ void task_wo(int rt, int ct, const u16* Wo, const u16* x, u16* out, unsigned char* ws, LAS char* lds) {
  const u16* ot = (const u16*)(ws + WS_OT);
  const u16* bo = (const u16*)(ws + WS_BO);
  gemm_tile<BM, WM, WN, false>(ot, HD, Wo, DM, HD, rt * BM, ct * 128, lds, [&](int r, int c, f32x4 v) {
    const v2u xx = *(const v2u*)(x + (size_t)r * DM + c);
    const v2u bb = *(const v2u*)(bo + (size_t)r * DM + c);
    const float o0 = rbf(bflo(xx.x) + rbf(v[0])) + bflo(bb.x), o1 = rbf(bfhi(xx.x) + rbf(v[1])) + bfhi(bb.x);
    const float o2 = rbf(bflo(xx.y) + rbf(v[2])) + bflo(bb.y), o3 = rbf(bfhi(xx.y) + rbf(v[3])) + bfhi(bb.y);
    *(v2u*)(out + (size_t)r * DM + c) = v2u{pack2(o0, o1), pack2(o2, o3)};
  });
}

__device__ __forceinline__ bool rdy(v4u v) { return max(max(v.x, v.y), max(v.z, v.w)) != SENT; }
__device__ __forceinline__ v4u ld_sc1(__amdgpu_buffer_rsrc_t rs, int off) { return __builtin_amdgcn_raw_buffer_load_b128(rs, off, 0, 16); }

template <int N>
__device__ __forceinline__ void poll(__amdgpu_buffer_rsrc_t rs, int off, v4u (&d)[N], unsigned* err) {
  unsigned spins = 0;
  for (;;) {
    unsigned mx = 0;
#pragma unroll
    for (int i = 0; i < N; ++i) d[i] = ld_sc1(rs, off + i * 1024);
    asm volatile("" ::: "memory");
#pragma unroll
    for (int i = 0; i < N; ++i) mx = max(max(mx, max(d[i].x, d[i].y)), max(d[i].z, d[i].w));
    if (__all(mx != SENT)) break;
    if (DLYR) __builtin_amdgcn_s_sleep(DLYR);
    if (++spins > SPINCAP) {
      if ((threadIdx.x & 63) == 0) __hip_atomic_store(err, 1u, __ATOMIC_RELAXED, __HIP_MEMORY_SCOPE_AGENT);
      break;
    }
    if ((spins & 1023u) == 0u && __hip_atomic_load(err, __ATOMIC_RELAXED, __HIP_MEMORY_SCOPE_AGENT) != 0u) break;
  }
}
__device__ __forceinline__ float xor16(float x) {
  auto r = __builtin_amdgcn_permlane16_swap(__builtin_bit_cast(unsigned, x), __builtin_bit_cast(unsigned, x), false, false);
  const int odd = (threadIdx.x >> 4) & 1;
  return __builtin_bit_cast(float, odd ? r[0] : r[1]);
}
__device__ __forceinline__ float xor32(float x) {
  auto r = __builtin_amdgcn_permlane32_swap(__builtin_bit_cast(unsigned, x), __builtin_bit_cast(unsigned, x), false, false);
  const int hi = (threadIdx.x >> 5) & 1;
  return __builtin_bit_cast(float, hi ? r[0] : r[1]);
}

template <bool ACQ = true>
__device__ __forceinline__ void wave_wait(unsigned* ctr, unsigned want, unsigned* err) {
  unsigned spins = 0;
  while (__hip_atomic_load(ctr, __ATOMIC_RELAXED, __HIP_MEMORY_SCOPE_AGENT) < want) {
    __builtin_amdgcn_s_sleep(4);
    if (++spins > SPINCAP) { __hip_atomic_store(err, 1u, __ATOMIC_RELAXED, __HIP_MEMORY_SCOPE_AGENT); break; }
    if ((spins & 1023u) == 0u && __hip_atomic_load(err, __ATOMIC_RELAXED, __HIP_MEMORY_SCOPE_AGENT) != 0u) break;
  }
  if (ACQ) {
    __builtin_amdgcn_fence(__ATOMIC_ACQUIRE, "agent");
    asm volatile("s_waitcnt vmcnt(0)" ::: "memory");
  } else {
    __builtin_amdgcn_fence(__ATOMIC_ACQUIRE, "wavefront");
  }
}
constexpr int CH_RED = 0;
constexpr int CH_VM = 24576;
constexpr int CH_RED2 = 25088;
constexpr int CH_WV = 27136;
constexpr int CH_KV = CH_WV + 65536;
constexpr int CH_CNT = CH_KV + 20480;
constexpr int CH_LDS = CH_CNT + 64;

__device__ __forceinline__ void chain_role(int c, const u16* __restrict__ mem0, const u16* __restrict__ Wq, const u16* __restrict__ Wk,
                                           const u16* __restrict__ Wv, const u16* __restrict__ Wo, unsigned char* ws, LAS char* lds) {
  const int tid = threadIdx.x, lane = tid & 63, w = __builtin_amdgcn_readfirstlane(tid >> 6);
  const int fr = lane & 15, g = lane >> 4;
  const int h = c >> 2, q4 = c & 3, mh = c & 1, nb = c >> 1;
  unsigned* err = (unsigned*)(ws + WS_ERR);
  const __amdgpu_buffer_rsrc_t rs = __builtin_amdgcn_make_buffer_rsrc((void*)(ws + WS_HOP), 0, (int)HOP_BYTES, 0x00020000);
  v4u wq[4][4], wo[16];
  LAS v4u* wvl = (LAS v4u*)(lds + CH_WV) + w * 512 + lane;
  {
    LAS char* stg = lds + CH_WV + w * 8192;
    const int q = fr >> 2, p = lane & 3;
    auto ld_wo = [&](int hf, v4u (&t)[8]) {
#pragma unroll
      for (int i = 0; i < 8; ++i) t[i] = *(const v4u*)(Wo + (size_t)(512 * w + 256 * hf + i * 32 + (lane >> 1)) * DM + 16 * nb + (lane & 1) * 8);
    };
    auto ld_w = [&](const u16* W, v4u (&t)[8]) {
#pragma unroll
      for (int i = 0; i < 8; ++i) t[i] = *(const v4u*)(W + (size_t)(128 * w + i * 16 + (lane >> 2)) * HD + h * 128 + 32 * q4 + (lane & 3) * 8);
    };
    auto tr_wo = [&](v4u (&t)[8], v4u* dst) {
#pragma unroll
      for (int i = 0; i < 8; ++i) *(LAS v4u*)(stg + (i * 32 + (lane >> 1)) * 32 + (lane & 1) * 16) = t[i];
      __builtin_amdgcn_s_waitcnt(0xC07F);
      asm volatile("" ::: "memory");
#pragma unroll
      for (int ks = 0; ks < 8; ++ks) {
        v2u lo = trread(stg + (32 * ks + 8 * g + q) * 32 + (4 * p) * 2);
        v2u hi = trread(stg + (32 * ks + 8 * g + 4 + q) * 32 + (4 * p) * 2);
        dst[ks] = v4u{lo.x, lo.y, hi.x, hi.y};
      }
      __builtin_amdgcn_s_waitcnt(0xC07F);
      asm volatile("" ::: "memory");
    };
    auto tr_w = [&](v4u (&t)[8], v4u* dst) {
#pragma unroll
      for (int i = 0; i < 8; ++i) *(LAS v4u*)(stg + (i * 16 + (lane >> 2)) * 64 + (lane & 3) * 16) = t[i];
      __builtin_amdgcn_s_waitcnt(0xC07F);
      asm volatile("" ::: "memory");
#pragma unroll
      for (int dt = 0; dt < 2; ++dt)
#pragma unroll
        for (int ks = 0; ks < 4; ++ks) {
          v2u lo = trread(stg + (32 * ks + 8 * g + q) * 64 + (16 * dt + 4 * p) * 2);
          v2u hi = trread(stg + (32 * ks + 8 * g + 4 + q) * 64 + (16 * dt + 4 * p) * 2);
          dst[dt * 4 + ks] = v4u{lo.x, lo.y, hi.x, hi.y};
        }
      __builtin_amdgcn_s_waitcnt(0xC07F);
      asm volatile("" ::: "memory");
    };
    v4u t0[8], t1[8], t2[8];
    ld_wo(0, t0);
    ld_wo(1, t1);
    ld_w(Wq, t2);
    tr_wo(t0, &wo[0]);
    ld_w(Wk, t0);
    tr_wo(t1, &wo[8]);
    ld_w(Wv, t1);
    tr_w(t2, &wq[0][0]);
    tr_w(t0, &wq[2][0]);
    tr_w(t1, t2);
#pragma unroll
    for (int i = 0; i < 8; ++i) wvl[i * 64] = t2[i];
  }
  const float scale = 0.08838834764831845f;
  const float L2E = 1.4426950408889634f;
  LAS f32x4* red = (LAS f32x4*)(lds + CH_RED);
  LAS f32x4* red2 = (LAS f32x4*)(lds + CH_RED2);
  LAS u16* vml = (LAS u16*)(lds + CH_VM);
  LAS v4u* kvl = (LAS v4u*)(lds + CH_KV) + lane;
  LAS unsigned* vcnt = (LAS unsigned*)(lds + CH_CNT);
  if (tid == 0) *vcnt = 0u;
  const __amdgpu_buffer_rsrc_t rsw = __builtin_amdgcn_make_buffer_rsrc((void*)ws, 0, (int)WS_TOTAL, 0x00020000);
  auto kv_prefetch = [&](int sn) {
    if ((sn & 1) == 0) wave_wait<false>(err + CW_KV + 16 * (sn >> 1), 64u, err);
    v4u t[10];
#pragma unroll
    for (int tt = 0; tt < 2; ++tt)
#pragma unroll
      for (int j = 0; j < 4; ++j) t[tt * 4 + j] = ld_sc1(rsw, (int)(WS_K + ((size_t)(32 * sn + 16 * tt + fr) * HD + h * 128 + 32 * j + 8 * g) * 2));
#pragma unroll
    for (int dt = 0; dt < 2; ++dt) t[8 + dt] = ld_sc1(rsw, (int)(WS_VT + ((((size_t)sn * NH + h) * 128 + 32 * q4 + 16 * dt + fr) * 32 + 8 * g) * 2));
#pragma unroll
    for (int i = 0; i < 10; ++i) kvl[((sn & 1) * 10 + i) * 64] = t[i];
  };
  if (w == 2) { wave_wait<false>(err + CW_KV + 16, 64u, err); kv_prefetch(0); }

#define STAMP(i)
  for (int s = 0; s < NSEG; ++s) {
    v4u mf[2];
    if (s == 0) {
#pragma unroll
      for (int p = 0; p < 2; ++p)
        mf[p] = *(const v4u*)(mem0 + (size_t)(fr & 7) * DM + 128 * w + 32 * (2 * p + (fr >> 3)) + 8 * g);
    } else {
      if (DLY1) __builtin_amdgcn_s_sleep(DLY1);
      poll<2>(rs, (int)(HOP_MEM + (size_t)s * 16384) + ((16 * w + 4 * (fr >> 3) + g) * 8 + (fr & 7)) * 16, mf, err);
    }
    STAMP(0)
    {
      f32x4 a0[4], a1[4];
#pragma unroll
      for (int t = 0; t < 4; ++t) {
        a0[t] = mfma16(wq[t][0], mf[0], f32x4{0.f, 0.f, 0.f, 0.f});
        a1[t] = mfma16(wq[t][1], mf[0], f32x4{0.f, 0.f, 0.f, 0.f});
      }
#pragma unroll
      for (int t = 0; t < 4; ++t) {
        a0[t] = mfma16(wq[t][2], mf[1], a0[t]);
        a1[t] = mfma16(wq[t][3], mf[1], a1[t]);
      }
#pragma unroll
      for (int t = 0; t < 4; ++t) {
        f32x4 x = (fr & 8) ? a1[t] : a0[t];
#pragma unroll
        for (int r = 0; r < 4; ++r) x[r] += row_ror<8>(x[r]);
        if (fr < 8) red[(w * 6 + t) * 32 + g * 8 + fr] = x;
      }
    }
    __syncthreads();
    STAMP(1)
    if (tid >= 384) {
      const int u = tid - 384;
      const int t6 = u >> 5, gg = (u >> 3) & 3, m = u & 7;
      f32x4 x = red[t6 * 32 + (u & 31)];
#pragma unroll
      for (int ww = 1; ww < 8; ++ww) x += red[(ww * 6 + t6) * 32 + (u & 31)];
      const unsigned lo = pack2(x[0], x[1]), hi = pack2(x[2], x[3]);
      const int mat = t6 >> 1, dt = t6 & 1;
      const int off = (int)(HOP_QK + ((size_t)s * NH + h) * 4096) + ((q4 * 2 + mat) * 8 + m) * 64 + (16 * dt + 4 * gg) * 2;
      __builtin_amdgcn_raw_buffer_store_b64(v2u{nosent(lo), nosent(hi)}, rs, off, 0, 16);
    }
    {
      f32x4 a0[2], a1[2];
#pragma unroll
      for (int t = 0; t < 2; ++t) {
        a0[t] = mfma16(wvl[(t * 4 + 0) * 64], mf[0], f32x4{0.f, 0.f, 0.f, 0.f});
        a1[t] = mfma16(wvl[(t * 4 + 1) * 64], mf[0], f32x4{0.f, 0.f, 0.f, 0.f});
      }
#pragma unroll
      for (int t = 0; t < 2; ++t) {
        a0[t] = mfma16(wvl[(t * 4 + 2) * 64], mf[1], a0[t]);
        a1[t] = mfma16(wvl[(t * 4 + 3) * 64], mf[1], a1[t]);
      }
#pragma unroll
      for (int t = 0; t < 2; ++t) {
        f32x4 x = (fr & 8) ? a1[t] : a0[t];
#pragma unroll
        for (int r = 0; r < 4; ++r) x[r] += row_ror<8>(x[r]);
        if (fr < 8) red[(w * 6 + 4 + t) * 32 + g * 8 + fr] = x;
      }
    }
    __builtin_amdgcn_s_waitcnt(0xC07F);
    asm volatile("" ::: "memory");
    if (lane == 0) __hip_atomic_fetch_add(vcnt, 1u, __ATOMIC_RELAXED, __HIP_MEMORY_SCOPE_WORKGROUP);
    STAMP(2)
    if (w == 1) {
      while (__hip_atomic_load(vcnt, __ATOMIC_RELAXED, __HIP_MEMORY_SCOPE_WORKGROUP) < 8u * (unsigned)(s + 1)) __builtin_amdgcn_s_sleep(1);
      asm volatile("" ::: "memory");
      const int dt = lane >> 5, gg = (lane >> 3) & 3, m = lane & 7;
      f32x4 x = red[(4 + dt) * 32 + (lane & 31)];
#pragma unroll
      for (int ww = 1; ww < 8; ++ww) x += red[(ww * 6 + 4 + dt) * 32 + (lane & 31)];
      const unsigned lo = pack2(x[0], x[1]), hi = pack2(x[2], x[3]);
      const int d = 16 * dt + 4 * gg;
      vml[(d + 0) * 8 + m] = (u16)(lo & 0xFFFF);
      vml[(d + 1) * 8 + m] = (u16)(lo >> 16);
      vml[(d + 2) * 8 + m] = (u16)(hi & 0xFFFF);
      vml[(d + 3) * 8 + m] = (u16)(hi >> 16);
      __builtin_amdgcn_s_waitcnt(0xC07F);
      asm volatile("" ::: "memory");
      if (lane < 32)
      {
        v4u t = *(LAS v4u*)(vml + lane * 8);
        t.x = nosent(t.x); t.y = nosent(t.y); t.z = nosent(t.z); t.w = nosent(t.w);
        __builtin_amdgcn_raw_buffer_store_b128(t, rs, (int)(HOP_VM + (((size_t)s * NH + h) * 128 + 32 * q4 + lane) * 16), 0, 16);
      }
    }
    if (s == NSEG - 1) break;
    if (w == 2 && s + 1 < NSEG - 1) kv_prefetch(s + 1);
    v4u pt, pm;
    if (w == 0) {
      v4u X[8];
      {
        const int base = (int)(HOP_QK + ((size_t)s * NH + h) * 4096) + (fr & 7) * 64 + g * 16;
        unsigned spins = 0;
        if (DLY2) __builtin_amdgcn_s_sleep(DLY2);
        for (;;) {
          unsigned mx = 0;
#pragma unroll
          for (int i = 0; i < 8; ++i) X[i] = ld_sc1(rs, base + i * 512);
          asm volatile("" ::: "memory");
#pragma unroll
          for (int i = 0; i < 8; ++i) mx = max(max(mx, max(X[i].x, X[i].y)), max(X[i].z, X[i].w));
          if (__all(mx != SENT)) break;
          if (++spins > SPINCAP) {
            if (lane == 0) __hip_atomic_store(err, 1u, __ATOMIC_RELAXED, __HIP_MEMORY_SCOPE_AGENT);
            break;
          }
          if ((spins & 1023u) == 0u && __hip_atomic_load(err, __ATOMIC_RELAXED, __HIP_MEMORY_SCOPE_AGENT) != 0u) break;
        }
      }
      STAMP(3)
      f32x4 s0 = {0.f, 0.f, 0.f, 0.f}, s1 = s0, s2 = s0;
#pragma unroll
      for (int j = 0; j < 4; ++j) {
        s1 = mfma16(kvl[((s & 1) * 10 + j) * 64], X[2 * j], s1);
        s2 = mfma16(kvl[((s & 1) * 10 + 4 + j) * 64], X[2 * j], s2);
        s0 = mfma16(X[2 * j + 1], X[2 * j], s0);
      }
      const bool up = fr >= 8;
      f32x4 st = up ? s2 : s1;
      const float m01 = up ? s0[1] : s0[0], m23 = up ? s0[3] : s0[2];
      const float sm = g >= 2 ? m23 : m01;
      unsigned p0 = pack2(st[0], st[1]), p1 = pack2(st[2], st[3]), p2 = pack2(sm, sm);
      p0 = pack2(bflo(p0) * scale, bfhi(p0) * scale);
      p1 = pack2(bflo(p1) * scale, bfhi(p1) * scale);
      p2 = pack2(bflo(p2) * scale, bflo(p2) * scale);
      float v0 = bflo(p0), v1 = bfhi(p0), v2 = bflo(p1), v3 = bfhi(p1), v4 = bflo(p2);
      float mx = fmaxf(fmaxf(fmaxf(v0, v1), fmaxf(v2, v3)), v4);
      mx = fmaxf(mx, row_ror<8>(mx));
      mx = fmaxf(mx, xor16(mx));
      mx = fmaxf(mx, xor32(mx));
      const float nm = -mx * L2E;
      v0 = __builtin_amdgcn_exp2f(__builtin_fmaf(v0, L2E, nm));
      v1 = __builtin_amdgcn_exp2f(__builtin_fmaf(v1, L2E, nm));
      v2 = __builtin_amdgcn_exp2f(__builtin_fmaf(v2, L2E, nm));
      v3 = __builtin_amdgcn_exp2f(__builtin_fmaf(v3, L2E, nm));
      v4 = __builtin_amdgcn_exp2f(__builtin_fmaf(v4, L2E, nm));
      float sum = ((v0 + v1) + (v2 + v3)) + v4;
      sum += row_ror<8>(sum);
      sum += xor16(sum);
      sum += xor32(sum);
      const float inv = __builtin_amdgcn_rcpf(sum);
      pt.x = pack2(v0 * inv, v1 * inv);
      pt.y = pack2(v2 * inv, v3 * inv);
      pt.z = (unsigned)__builtin_amdgcn_update_dpp(0, (int)pt.x, 0x128, 0xf, 0xf, false);
      pt.w = (unsigned)__builtin_amdgcn_update_dpp(0, (int)pt.y, 0x128, 0xf, 0xf, false);
      v4 *= inv;
      pm.x = pack2(v4, row_ror<8>(v4));
      pm.y = 0u; pm.z = 0u; pm.w = 0u;
    }
    __syncthreads();
    if (w == 0) {
      f32x4 o[2];
#pragma unroll
      for (int dt = 0; dt < 2; ++dt) o[dt] = mfma16(kvl[((s & 1) * 10 + 8 + dt) * 64], pt, f32x4{0.f, 0.f, 0.f, 0.f});
#pragma unroll
      for (int dt = 0; dt < 2; ++dt) {
        const unsigned t = *(LAS unsigned*)(vml + (16 * dt + fr) * 8 + 4 * (g & 1) + 2 * (g >> 1));
        o[dt] = mfma16(v4u{t, 0u, 0u, 0u}, pm, o[dt]);
      }
      if (fr < 8) {
#pragma unroll
        for (int dt = 0; dt < 2; ++dt) {
          const int k8 = h * 16 + 4 * q4 + 2 * dt + (g >> 1);
          const int off = (int)(HOP_OB + (size_t)s * 65536) + (((fr >> 2) * 512 + k8) * 4 + (fr & 3)) * 16 + (g & 1) * 8;
          __builtin_amdgcn_raw_buffer_store_b64(v2u{nosent(pack2(o[dt][0], o[dt][1])), nosent(pack2(o[dt][2], o[dt][3]))}, rs, off, 0, 16);
        }
      }
    }
    __syncthreads();
    STAMP(4)
    {
      v4u of[4];
      if (DLY3) __builtin_amdgcn_s_sleep(DLY3);
      poll<4>(rs, (int)(HOP_OB + (size_t)s * 65536) + ((mh * 512 + 64 * w + 4 * (fr >> 2) + g) * 4 + (fr & 3)) * 16, of, err);
      STAMP(5)
      f32x4 a[4];
#pragma unroll
      for (int r4 = 0; r4 < 4; ++r4) a[r4] = mfma16(wo[r4], of[0], f32x4{0.f, 0.f, 0.f, 0.f});
#pragma unroll
      for (int i = 1; i < 4; ++i)
#pragma unroll
        for (int r4 = 0; r4 < 4; ++r4) a[r4] = mfma16(wo[4 * i + r4], of[i], a[r4]);
      const int r4 = fr >> 2;
      f32x4 x = r4 == 0 ? a[0] : (r4 == 1 ? a[1] : (r4 == 2 ? a[2] : a[3]));
#pragma unroll
      for (int r = 0; r < 4; ++r) { x[r] += row_ror<8>(x[r]); x[r] += row_ror<4>(x[r]); }
      if (fr < 4) red2[w * 16 + g * 4 + fr] = x;
    }
    __syncthreads();
    if (tid < 16) {
      f32x4 x = red2[tid];
#pragma unroll
      for (int ww = 1; ww < 8; ++ww) x += red2[ww * 16 + tid];
      const int gg = tid >> 2, m4 = tid & 3;
      const int k8 = 2 * nb + (gg >> 1);
      const int off = (int)(HOP_MEM + (size_t)(s + 1) * 16384) + (k8 * 8 + 4 * mh + m4) * 16 + (gg & 1) * 8;
      __builtin_amdgcn_raw_buffer_store_b64(v2u{nosent(pack2(x[0], x[1])), nosent(pack2(x[2], x[3]))}, rs, off, 0, 16);
    }
    STAMP(6)
  }
  __syncthreads();
}

__device__ __forceinline__ void attn_unit(int s, int h, int lane, unsigned char* ws, LAS char* ostg) {
  const int fr = lane & 15, g = lane >> 4;
  const u16* Q = (const u16*)(ws + WS_Q);
  const u16* Kx = (const u16*)(ws + WS_K);
  const u16* Vt = (const u16*)(ws + WS_VT);
  const __amdgpu_buffer_rsrc_t rs = __builtin_amdgcn_make_buffer_rsrc((void*)(ws + WS_HOP), 0, (int)HOP_BYTES, 0x00020000);
  unsigned* err = (unsigned*)(ws + WS_ERR);
  const float scale = 0.08838834764831845f;
  f32x4 sc[3][2];
#pragma unroll
  for (int a = 0; a < 3; ++a)
#pragma unroll
    for (int b = 0; b < 2; ++b) sc[a][b] = f32x4{0.f, 0.f, 0.f, 0.f};
  v4u qf[4][2], vtf[8];
  {
    v4u kf[4][2];
#pragma unroll
    for (int j = 0; j < 4; ++j)
#pragma unroll
      for (int t = 0; t < 2; ++t) {
        qf[j][t] = *(const v4u*)(Q + (size_t)(32 * s + 16 * t + fr) * HD + h * 128 + 32 * j + 8 * g);
        kf[j][t] = *(const v4u*)(Kx + (size_t)(32 * s + 16 * t + fr) * HD + h * 128 + 32 * j + 8 * g);
      }
#pragma unroll
    for (int dt = 0; dt < 8; ++dt) vtf[dt] = *(const v4u*)(Vt + (((size_t)s * NH + h) * 128 + 16 * dt + fr) * 32 + 8 * g);
#pragma unroll
    for (int j = 0; j < 4; ++j)
#pragma unroll
      for (int a = 0; a < 2; ++a)
#pragma unroll
        for (int b = 0; b < 2; ++b) sc[1 + a][b] = mfma16(kf[j][a], qf[j][b], sc[1 + a][b]);
  }
  v4u km[4];
  v4u vmr[2];
  {
    const int o1 = (int)(HOP_QK + ((size_t)s * NH + h) * 4096) + 512 + (fr & 7) * 64 + g * 16;
    const int o2 = (int)(HOP_VM + ((size_t)s * NH + h) * 2048) + lane * 16;
    unsigned spins = 0;
    for (;;) {
      unsigned mx = 0;
#pragma unroll
      for (int i = 0; i < 4; ++i) km[i] = ld_sc1(rs, o1 + i * 1024);
#pragma unroll
      for (int i = 0; i < 2; ++i) vmr[i] = ld_sc1(rs, o2 + i * 1024);
      asm volatile("" ::: "memory");
#pragma unroll
      for (int i = 0; i < 4; ++i) mx = max(max(mx, max(km[i].x, km[i].y)), max(km[i].z, km[i].w));
#pragma unroll
      for (int i = 0; i < 2; ++i) mx = max(max(mx, max(vmr[i].x, vmr[i].y)), max(vmr[i].z, vmr[i].w));
      if (__all(mx != SENT)) break;
      __builtin_amdgcn_s_sleep(16);
      if (++spins > SPINCAP) {
        if (lane == 0) __hip_atomic_store(err, 1u, __ATOMIC_RELAXED, __HIP_MEMORY_SCOPE_AGENT);
        break;
      }
      if ((spins & 1023u) == 0u && __hip_atomic_load(err, __ATOMIC_RELAXED, __HIP_MEMORY_SCOPE_AGENT) != 0u) break;
    }
  }
#pragma unroll
  for (int j = 0; j < 4; ++j)
#pragma unroll
    for (int b = 0; b < 2; ++b) sc[0][b] = mfma16(km[j], qf[j][b], sc[0][b]);
  v4u pt[2], pm[2];
#pragma unroll
  for (int b = 0; b < 2; ++b) {
    const int qi = 16 * b + fr;
    float v[12];
    bool ok[12];
#pragma unroll
    for (int r = 0; r < 4; ++r) {
      v[r] = rbf(rbf(sc[0][b][r]) * scale); ok[r] = g < 2;
      v[4 + r] = rbf(rbf(sc[1][b][r]) * scale); ok[4 + r] = (4 * g + r) <= qi;
      v[8 + r] = rbf(rbf(sc[2][b][r]) * scale); ok[8 + r] = (16 + 4 * g + r) <= qi;
    }
    float mx = -3.0e38f;
#pragma unroll
    for (int i = 0; i < 12; ++i) mx = ok[i] ? fmaxf(mx, v[i]) : mx;
    mx = fmaxf(mx, __shfl_xor(mx, 16));
    mx = fmaxf(mx, __shfl_xor(mx, 32));
    float sum = 0.f;
#pragma unroll
    for (int i = 0; i < 12; ++i) { v[i] = ok[i] ? __expf(v[i] - mx) : 0.f; sum += v[i]; }
    sum += __shfl_xor(sum, 16);
    sum += __shfl_xor(sum, 32);
    const float inv = 1.0f / sum;
    pt[b].x = pack2(v[4] * inv, v[5] * inv); pt[b].y = pack2(v[6] * inv, v[7] * inv);
    pt[b].z = pack2(v[8] * inv, v[9] * inv); pt[b].w = pack2(v[10] * inv, v[11] * inv);
    pm[b].x = pack2(v[0] * inv, v[1] * inv); pm[b].y = pack2(v[2] * inv, v[3] * inv); pm[b].z = 0u; pm[b].w = 0u;
  }
#pragma unroll
  for (int dt = 0; dt < 8; ++dt) {
    v4u vmf;
    {
      const int src = (16 * dt + fr) & 63;
      const v4u vv = vmr[dt >> 2];
      const unsigned a0 = __shfl(vv.x, src), a1 = __shfl(vv.y, src), a2 = __shfl(vv.z, src), a3 = __shfl(vv.w, src);
      vmf.x = g == 0 ? a0 : (g == 1 ? a2 : 0u);
      vmf.y = g == 0 ? a1 : (g == 1 ? a3 : 0u);
      vmf.z = 0u; vmf.w = 0u;
    }
#pragma unroll
    for (int b = 0; b < 2; ++b) {
      f32x4 o = {0.f, 0.f, 0.f, 0.f};
      o = mfma16(vtf[dt], pt[b], o);
      o = mfma16(vmf, pm[b], o);
      *(LAS v2u*)(ostg + (16 * b + fr) * 272 + (16 * dt + 4 * g) * 2) = v2u{pack2(o[0], o[1]), pack2(o[2], o[3])};
    }
  }
  __builtin_amdgcn_s_waitcnt(0xC07F);
  asm volatile("" ::: "memory");
  {
    const __amdgpu_buffer_rsrc_t rsw = __builtin_amdgcn_make_buffer_rsrc((void*)ws, 0, (int)WS_TOTAL, 0x00020000);
#pragma unroll
    for (int i = 0; i < 8; ++i) {
      const int row = 4 * i + g;
      const v4u v = *(LAS v4u*)(ostg + row * 272 + fr * 16);
      __builtin_amdgcn_raw_buffer_store_b128(v, rsw, (int)(WS_OT + ((size_t)(32 * s + row) * HD + h * 128) * 2 + fr * 16), 0, 16);
    }
  }
  __builtin_amdgcn_s_waitcnt(0xC07F);
}
__device__ __forceinline__ void wg_signal(unsigned* ctr, int n) {
  asm volatile("s_waitcnt vmcnt(0)" ::: "memory");
  __syncthreads();
  if (threadIdx.x < n) __hip_atomic_fetch_add(ctr + 16 * threadIdx.x, 1u, __ATOMIC_RELAXED, __HIP_MEMORY_SCOPE_AGENT);
}
__device__ __forceinline__ void wg_wait(unsigned* ctr, unsigned want, unsigned* err) {
  if (threadIdx.x < 64) wave_wait(ctr, want, err);
  __syncthreads();
}

__device__ __forceinline__ void wait_phase_abc(unsigned* cw) {
  unsigned* err = cw + CW_ERR;
  if (threadIdx.x < 64) {
    const int l = threadIdx.x;
    unsigned* p = l < 32 ? cw + CW_KV + 16 * l : (l == 32 ? cw + CW_Q : cw + CW_MLP2);
    const unsigned want = l < 32 ? 64u : (l < 34 ? 128u : 0u);
    unsigned spins = 0;
    while (!__all(__hip_atomic_load(p, __ATOMIC_RELAXED, __HIP_MEMORY_SCOPE_AGENT) >= want)) {
      __builtin_amdgcn_s_sleep(8);
      if (++spins > SPINCAP) { __hip_atomic_store(err, 1u, __ATOMIC_RELAXED, __HIP_MEMORY_SCOPE_AGENT); break; }
      if ((spins & 1023u) == 0u && __hip_atomic_load(err, __ATOMIC_RELAXED, __HIP_MEMORY_SCOPE_AGENT) != 0u) break;
    }
    __builtin_amdgcn_fence(__ATOMIC_ACQUIRE, "agent");
    asm volatile("s_waitcnt vmcnt(0)" ::: "memory");
  }
  __syncthreads();
}
#define do_attn(sg_, hg_, ci_) { attn_unit((sg_), (hg_) * 8 + (threadIdx.x >> 6), threadIdx.x & 63, ws, lds + (threadIdx.x >> 6) * 8704); wg_signal(cw + CW_AT + 16 * (ci_), 1); }

template <int NS>
__device__ __forceinline__ void tail_unit(int u, int idx, const u16* x, const u16* Wo, u16* out, unsigned char* ws, LAS char* lds) {
  unsigned* cw = (unsigned*)(ws + WS_ERR);
  unsigned* err = cw + CW_ERR;
  constexpr int KS = HD / NS, RR = 64 / NS;
  const int ct = idx / NS, ks = idx % NS;
  const __amdgpu_buffer_rsrc_t rsw = __builtin_amdgcn_make_buffer_rsrc((void*)ws, 0, (int)WS_TOTAL, 0x00020000);
#define TSTAMP(i)
  TSTAMP(0)
  TSTAMP(1)
  wg_wait(cw + CW_AT + 16 * u, 8u, err);
  TSTAMP(2)
  const int slot = (u - TAIL_U0) * 8 + ct;
  const size_t sbase = WS_SLAB + (size_t)slot * 16 * 32768;
  gemm_tile<64, 2, 4, false>((const u16*)(ws + WS_OT) + ks * KS, HD, Wo + (size_t)ks * KS * DM, DM, KS, u * 64, ct * 128, lds,
                             [&](int r, int c, f32x4 v) {
                               __builtin_amdgcn_raw_buffer_store_b128(__builtin_bit_cast(v4u, v), rsw,
                                   (int)(sbase + (size_t)ks * 32768 + (((r & 63) * 128 + (c & 127)) * 4)), 0, 16);
                             });
  TSTAMP(3)
  wg_signal(cw + CW_SK + 16 * slot, 1);
  TSTAMP(4)
  if (threadIdx.x < 64) wave_wait<false>(cw + CW_SK + 16 * slot, (unsigned)NS, err);
  __syncthreads();
  TSTAMP(5)
  if (threadIdx.x < RR * 32) {
    const int rr = RR * ks + (threadIdx.x >> 5), cc = (threadIdx.x & 31) * 4;
    v4u pv[NS];
#pragma unroll
    for (int k2 = 0; k2 < NS; ++k2) pv[k2] = ld_sc1(rsw, (int)(sbase + (size_t)k2 * 32768 + (rr * 128 + cc) * 4));
    f32x4 v = __builtin_bit_cast(f32x4, pv[0]);
#pragma unroll
    for (int k2 = 1; k2 < NS; ++k2) v += __builtin_bit_cast(f32x4, pv[k2]);
    const int r = u * 64 + rr, c = ct * 128 + cc;
    const v2u xx = *(const v2u*)(x + (size_t)r * DM + c);
    const v2u bb = *(const v2u*)((const u16*)(ws + WS_BO) + (size_t)r * DM + c);
    const float o0 = rbf(bflo(xx.x) + rbf(v[0])) + bflo(bb.x), o1 = rbf(bfhi(xx.x) + rbf(v[1])) + bfhi(bb.x);
    const float o2 = rbf(bflo(xx.y) + rbf(v[2])) + bflo(bb.y), o3 = rbf(bfhi(xx.y) + rbf(v[3])) + bfhi(bb.y);
    *(v2u*)(out + (size_t)r * DM + c) = v2u{pack2(o0, o1), pack2(o2, o3)};
  }
}

__global__ __launch_bounds__(512, 2) void k_mega(const u16* x, const u16* mem0, const u16* Wq, const u16* Wk, const u16* Wv, const u16* Wo,
                                                 const u16* w1, const u16* b1, const u16* w2, const u16* b2, u16* out, unsigned char* ws) {
  extern __shared__ __attribute__((aligned(16))) char smem[];
  LAS char* lds = (LAS char*)smem;
  const int b = blockIdx.x;
#define ASTAMP(i)
  ASTAMP(0)
  const int xcd = b & 7, i = b >> 4;
  unsigned* cw = (unsigned*)(ws + WS_ERR);
  unsigned* err = cw + CW_ERR;
  if (((b >> 3) & 1) == 0) {
    const int c = ((xcd * 4 + (i >> 2)) << 2) | (i & 3);
    chain_role(c, mem0, Wq, Wk, Wv, Wo, ws, lds);
    ASTAMP(6)
    wait_phase_abc(cw);
    tail_unit<16>(31, xcd * 16 + i, x, Wo, out, ws, lds);
    ASTAMP(7)
    return;
  }
  const int gi = i * 8 + xcd;
  task_qkv<64, 2, 4>(gi >> 6, 32 + (gi & 63), Wq, Wk, Wv, ws, lds);
  wg_signal(cw + CW_KV + 16 * (gi >> 6), 1);
  for (int t = gi; t < 64 + 7 * 64; t += 128) {
    if (t < 64) {
      task_qkv<128, 2, 4>(1, 32 + t, Wq, Wk, Wv, ws, lds);
      wg_signal(cw + CW_KV + 16 * 2, 2);
    } else {
      const int rt = 1 + ((t - 64) >> 6);
      task_qkv<256, 4, 2>(rt, 32 + ((t - 64) & 63), Wq, Wk, Wv, ws, lds);
      wg_signal(cw + CW_KV + 16 * 4 * rt, 4);
    }
  }
  ASTAMP(1)
  for (int t = gi; t < 8 * 28; t += 128) task_mlp1<256, 4, 2>(t & 7, t >> 3, w1, b1, ws, lds);
  wg_signal(cw + CW_MLP1, 1);
  for (int t = gi; t < 8 * 32; t += 128) task_qkv<256, 4, 2>(t & 7, t >> 3, Wq, Wk, Wv, ws, lds);
  wg_signal(cw + CW_Q, 1);
  ASTAMP(2)
  wg_wait(cw + CW_MLP1, 128u, err);
  task_mlp2<128, 2, 4>(gi >> 3, gi & 7, w2, b2, ws, lds);
  wg_signal(cw + CW_MLP2, 1);
  ASTAMP(3)
  wait_phase_abc(cw);
  for (int t = gi; t < 160; t += 128) do_attn(t >> 2, t & 3, (t >> 4) * 2)
  if (gi >= 96) {
    for (int t = 160 + (gi - 96); t < 256; t += 32) do_attn(t >> 2, t & 3, t < 192 ? (t >> 4) * 2 : (t >> 3))
  } else {
    {
      const int pr = gi >> 3;
      wg_wait(cw + CW_AT + 16 * 2 * pr, 16u, err);
      task_wo<128, 2, 4>(pr, gi & 7, Wo, x, out, ws, lds);
    }
    if (gi < 64) {
      for (int u = TAIL_U0; u < 31; u += 2) tail_unit<8>(u, (gi & 7) * 8 + (gi >> 3), x, Wo, out, ws, lds);
    } else {
      for (int u = TAIL_U0 + 1; u < 31; u += 2) tail_unit<4>(u, (gi & 7) * 4 + ((gi - 64) >> 3), x, Wo, out, ws, lds);
    }
  }
  ASTAMP(7)
}

extern "C" void kernel_launch(void* const* d_in, const int* in_sizes, int n_in, void* d_out, int out_size, void* d_ws, size_t ws_size,
                              hipStream_t stream) {
  const u16* x = (const u16*)d_in[0];
  const u16* mem0 = (const u16*)d_in[1];
  const u16* Wq = (const u16*)d_in[2];
  const u16* Wk = (const u16*)d_in[3];
  const u16* Wv = (const u16*)d_in[4];
  const u16* Wo = (const u16*)d_in[5];
  const u16* w1 = (const u16*)d_in[8];
  const u16* b1 = (const u16*)d_in[9];
  const u16* w2 = (const u16*)d_in[10];
  const u16* b2 = (const u16*)d_in[11];
  unsigned char* ws = (unsigned char*)d_ws;
  u16* out = (u16*)d_out;
  constexpr int L256 = GemmCfg<256>::LDS_BYTES;
  constexpr int LMEGA = L256 > CH_LDS ? L256 : CH_LDS;
  (void)hipFuncSetAttribute((const void*)k_mega, hipFuncAttributeMaxDynamicSharedMemorySize, LMEGA);
  k_ln<<<S_LEN / 4, 256, 0, stream>>>(x, ws);
  k_mega<<<256, 512, LMEGA, stream>>>(x, mem0, Wq, Wk, Wv, Wo, w1, b1, w2, b2, out, ws);
}
```
